```python
import torch

def _softmax(x, axis=-1):
    return torch.softmax(x, dim=axis)

def _gelu(x):
    return torch.nn.functional.gelu(x, approximate='tanh')

def _silu(x):
    return torch.nn.functional.silu(x)

def _sigmoid(x):
    return torch.sigmoid(x)

def _layernorm(x, axis=-1, eps=1e-05):
    m = x.mean(dim=axis, keepdim=True)
    v = x.var(dim=axis, keepdim=True, unbiased=False)
    return (x - m) / torch.sqrt(v + eps)

def _scatter_add(tgt, idx, src, axis=0):
    return tgt.clone().index_add_(axis, idx, src)

def _segment_sum(src, seg, n):
    out = torch.zeros((n,) + src.shape[1:], dtype=src.dtype)
    return out.index_add_(0, seg, src)

def _pad(x, pw):
    flat = tuple((v for pair in reversed(pw) for v in pair))
    return torch.nn.functional.pad(x, flat)
kernel_name = 'gated_mix__memory_aug_recurrent_mem_tr__glu_gated_geglu__prompt'

def setup_inputs():

    _w = lambda a: (lambda t: t.to(torch.bfloat16) if t.is_floating_point() else t)(torch.as_tensor(a))

    def _A_setup():
        B, S, Dm = (1, 2048, 640)
        H, Dh = (32, 128)
        M, seg = (8, 32)
        seg = min(seg, S)
        S = S // seg * seg
        return {'x': _w((lambda *s: torch.randn(*s, dtype=torch.bfloat16) * 0.05)(B, S, Dm)), 'mem0': _w((lambda *s: torch.randn(*s, dtype=torch.bfloat16) * 0.4)(B, M, Dm)), 'Wq': _w((lambda *s: torch.randn(*s, dtype=torch.bfloat16) * 6.4)(Dm, H, Dh) * Dm ** (-0.5)), 'Wk': _w((lambda *s: torch.randn(*s, dtype=torch.bfloat16) * 6.4)(Dm, H, Dh) * Dm ** (-0.5)), 'Wv': _w((lambda *s: torch.randn(*s, dtype=torch.bfloat16) * 0.4)(Dm, H, Dh) * Dm ** (-0.5)), 'Wo': _w((lambda *s: torch.randn(*s, dtype=torch.bfloat16) * 0.4)(H, Dh, Dm) * (H * Dh) ** (-0.5)), 'seg_len': seg}
    _A_in = _A_setup()

    def _B_setup():
        B, S, Dm, Df = (1, 2048, 640, 1728)
        return {'x': _w((lambda *s: torch.randn(*s, dtype=torch.bfloat16) * 0.05)(B, S, Dm)), 'w_gate': _w((lambda *s: torch.randn(*s, dtype=torch.bfloat16) * 1.6)(Dm, Df) * Dm ** (-0.5)), 'w_up': _w((lambda *s: torch.randn(*s, dtype=torch.bfloat16) * 1.6)(Dm, Df) * Dm ** (-0.5)), 'w_down': _w((lambda *s: torch.randn(*s, dtype=torch.bfloat16) * 1.6)(Df, Dm) * Df ** (-0.5))}
    _B_in = _B_setup()
    return {'A_x': _A_in['x'], 'A_mem0': _A_in['mem0'], 'A_Wq': _A_in['Wq'], 'A_Wk': _A_in['Wk'], 'A_Wv': _A_in['Wv'], 'A_Wo': _A_in['Wo'], 'A_seg_len': _A_in['seg_len'], 'B_x': _B_in['x'], 'B_w_gate': _B_in['w_gate'], 'B_w_up': _B_in['w_up'], 'B_w_down': _B_in['w_down'], 'gate_w': _w((lambda *s: torch.randn(*s, dtype=torch.bfloat16) * 0.05)(640) * 16.0)}

def reference(A_x, A_mem0, A_Wq, A_Wk, A_Wv, A_Wo, A_seg_len, B_x, B_w_gate, B_w_up, B_w_down, gate_w):

    def _A_ref(x, mem0, Wq, Wk, Wv, Wo, seg_len):
        B, S, Dm = x.shape
        H, Dh = (Wq.shape[1], Wq.shape[2])
        M = mem0.shape[1]
        scale = Dh ** (-0.5)
        n_seg = S // seg_len
        L = M + seg_len
        idx = torch.arange(L)
        mask = (idx[:, None] < M) | (idx[:, None] >= idx[None, :])
        mem = mem0
        outs = []
        for s in range(n_seg):
            xs = x[:, s * seg_len:(s + 1) * seg_len, :].clone()
            h = torch.concatenate([mem, xs], axis=1)
            q = torch.einsum('btd,dhe->bhte', h, Wq)
            k = torch.einsum('btd,dhe->bhte', h, Wk)
            v = torch.einsum('btd,dhe->bhte', h, Wv)
            scores = torch.einsum('bhqe,bhke->bhqk', q, k) * scale
            scores = torch.where(mask[None, None], scores, -1000000000.0)
            p = _softmax(scores, axis=-1)
            o = torch.einsum('bhqk,bhke->bhqe', p, v)
            o = torch.einsum('bhte,hed->btd', o, Wo)
            mem = o[:, :M, :].clone()
            outs.append(o[:, M:, :])
        return torch.concatenate(outs, axis=1)

    def _B_ref(x, w_gate, w_up, w_down):
        g = torch.einsum('bsd,df->bsf', x, w_gate)
        u = torch.einsum('bsd,df->bsf', x, w_up)
        h = _gelu(g) * u
        return torch.einsum('bsf,fd->bsd', h, w_down)
    x = A_x
    g = _sigmoid((x * gate_w).sum(-1, keepdims=True))
    a = _A_ref(x, A_mem0, A_Wq, A_Wk, A_Wv, A_Wo, A_seg_len)
    b = _B_ref(x, B_w_gate, B_w_up, B_w_down)
    return g * a + (1 - g) * b
```

```cpp
#include <hip/hip_runtime.h>
#include <hip/hip_bf16.h>
#include <stdint.h>

typedef __bf16 bf16x8 __attribute__((ext_vector_type(8)));
typedef __bf16 bf16x16 __attribute__((ext_vector_type(16)));
typedef float f32x4 __attribute__((ext_vector_type(4)));
typedef unsigned u32x4 __attribute__((ext_vector_type(4)));
typedef unsigned u32x2 __attribute__((ext_vector_type(2)));
typedef unsigned short u16;

constexpr int SEQ = 2048, DM = 640, NH = 32, DH = 128, SEG = 32, NSEG = 64, DF = 1728;
constexpr int NSL = 80;

constexpr size_t SZ_WP = (size_t)32 * 4 * 40 * 64 * 32;
constexpr size_t OFF_CTL = 0;
constexpr size_t OFF_MEM = 4096;
constexpr size_t OFF_P = OFF_MEM + (size_t)64 * 10240;
constexpr size_t OFF_UM = OFF_P + (size_t)64 * 20480;
constexpr size_t OFF_SENT_END = OFF_UM + (size_t)64 * 327680;
constexpr size_t OFF_KM = OFF_SENT_END;
constexpr size_t OFF_WQK = OFF_KM + (size_t)64 * 65536;
constexpr size_t OFF_WVO = OFF_WQK + SZ_WP;
constexpr size_t OFF_KX = OFF_WVO + SZ_WP;
constexpr size_t OFF_QX = OFF_KX + (size_t)SEQ * 4096 * 2;
constexpr size_t OFF_UX = OFF_QX + (size_t)SEQ * 4096 * 2;
constexpr size_t OFF_PX = OFF_UX + (size_t)64 * 80 * 32 * 8 * 32 * 2;
constexpr size_t OFF_WGT = OFF_PX + (size_t)64 * 32 * 32 * 40 * 2;
constexpr size_t OFF_WUT = OFF_WGT + (size_t)DF * DM * 2;
constexpr size_t OFF_WDT = OFF_WUT + (size_t)DF * DM * 2;
constexpr size_t OFF_HB = OFF_WDT + (size_t)DF * DM * 2;
constexpr size_t OFF_BO = OFF_HB + (size_t)SEQ * DF * 2;
constexpr size_t OFF_END = OFF_BO + (size_t)SEQ * DM * 2;

#define AUX_SC1 16
#define RSRC_W3 0x00020000

__device__ __forceinline__ float bf2f(u16 v) { return __uint_as_float(((unsigned)v) << 16); }
__device__ __forceinline__ u16 f2bf(float f) { __bf16 b = (__bf16)f; return __builtin_bit_cast(u16, b); }
__device__ __forceinline__ float rbf(float f) { return (float)((__bf16)f); }
__device__ __forceinline__ unsigned pk2(float a, float b) { return (unsigned)f2bf(a) | ((unsigned)f2bf(b) << 16); }
__device__ __forceinline__ unsigned nosent(unsigned x) { return x == 0xFFFFFFFFu ? 0xFFFEFFFEu : x; }
__device__ __forceinline__ bool vok(u32x4 v) {
  return (v.x != 0xFFFFFFFFu) & (v.y != 0xFFFFFFFFu) & (v.z != 0xFFFFFFFFu) & (v.w != 0xFFFFFFFFu);
}
__device__ __forceinline__ int kperm(int gb, int eb) {
  return 32 * (eb >> 3) + 16 * (gb >> 1) + 8 * ((eb >> 1) & 1) + 4 * (gb & 1) + 2 * ((eb >> 2) & 1) + (eb & 1);
}
__device__ __forceinline__ f32x4 smf(bf16x8 a, bf16x16 b, f32x4 c, int idx) {
  return __builtin_amdgcn_smfmac_f32_16x16x64_bf16(a, b, c, idx, 0, 0);
}
__device__ __forceinline__ f32x4 mf16(bf16x8 a, bf16x8 b, f32x4 c) {
  return __builtin_amdgcn_mfma_f32_16x16x32_bf16(a, b, c, 0, 0, 0);
}
__device__ __forceinline__ float comb(float a, float b) {
  auto r = __builtin_amdgcn_permlane32_swap(__float_as_uint(a), __float_as_uint(b), false, false);
  return __uint_as_float(r[0]) + __uint_as_float(r[1]);
}

__global__ void k_repack(const u16* __restrict__ Wq, const u16* __restrict__ Wk, const u16* __restrict__ Wv,
                         const u16* __restrict__ Wo, u16* __restrict__ wqk, u16* __restrict__ wvo) {
  int idx = blockIdx.x * blockDim.x + threadIdx.x;
  const int PER = 32 * 4 * 40 * 64;
  if (idx >= 2 * PER) return;
  int which = idx / PER; idx -= which * PER;
  int lane = idx & 63, f = (idx >> 6) % 40, w = (idx / (64 * 40)) & 3, h = idx / (64 * 40 * 4);
  int n = lane & 15, gb = lane >> 4;
  u16 v[16];
  if (which == 0) {
    int tn = f / 10, i = f % 10;
    const u16* src = w < 2 ? Wq : Wk;
    int col = h * 128 + 64 * (w & 1) + 16 * tn + n;
#pragma unroll
    for (int eb = 0; eb < 16; ++eb) v[eb] = src[(size_t)(64 * i + kperm(gb, eb)) * 4096 + col];
  } else if (f < 20) {
    int tn = f / 10, i = f % 10;
    int col = h * 128 + 32 * w + 16 * tn + n;
#pragma unroll
    for (int eb = 0; eb < 16; ++eb) v[eb] = Wv[(size_t)(64 * i + kperm(gb, eb)) * 4096 + col];
  } else {
    int f2 = f - 20, t = f2 >> 1, i = f2 & 1;
    int col = 160 * w + 16 * t + n;
#pragma unroll
    for (int eb = 0; eb < 16; ++eb) v[eb] = Wo[(size_t)(h * 128 + 64 * i + kperm(gb, eb)) * 640 + col];
  }
  u32x4 o0, o1;
  o0.x = v[0] | (v[1] << 16); o0.y = v[2] | (v[3] << 16); o0.z = v[4] | (v[5] << 16); o0.w = v[6] | (v[7] << 16);
  o1.x = v[8] | (v[9] << 16); o1.y = v[10] | (v[11] << 16); o1.z = v[12] | (v[13] << 16); o1.w = v[14] | (v[15] << 16);
  u32x4* dst = (u32x4*)((which == 0 ? wqk : wvo) + (size_t)idx * 16);
  dst[0] = o0; dst[1] = o1;
}

__global__ void k_transpose(const u16* __restrict__ src, u16* __restrict__ dst, int R, int C) {
  __shared__ u16 t[32][33];
  int bx = blockIdx.x * 32, by = blockIdx.y * 32;
  for (int j = threadIdx.y; j < 32; j += 8) {
    int r = by + j, c = bx + threadIdx.x;
    if (r < R && c < C) t[j][threadIdx.x] = src[(size_t)r * C + c];
  }
  __syncthreads();
  for (int j = threadIdx.y; j < 32; j += 8) {
    int c = bx + j, r = by + threadIdx.x;
    if (r < R && c < C) dst[(size_t)c * R + r] = t[threadIdx.x][j];
  }
}

__device__ __forceinline__ bf16x16 ldfrag(const u16* base, int f, int lane) {
  const u32x4* p = (const u32x4*)(base + ((size_t)f * 64 + lane) * 16);
  u32x4 a = p[0], b = p[1];
  typedef unsigned u32x8 __attribute__((ext_vector_type(8)));
  u32x8 c = {a.x, a.y, a.z, a.w, b.x, b.y, b.z, b.w};
  return __builtin_bit_cast(bf16x16, c);
}

constexpr int XS = 656;
constexpr int OS = 264;
constexpr int VS = 136;

__device__ __forceinline__ void stage_x(const u16* __restrict__ x, int t, u16* xs, int tid) {
  for (int p = tid; p < 2560; p += 256) {
    int row = p / 80, c = p % 80;
    u32x4 v = *(const u32x4*)(x + (size_t)(t * 32 + row) * 640 + c * 8);
    *(u32x4*)(xs + row * XS + c * 8) = v;
  }
}

__global__ void __launch_bounds__(256, 1) k_pre(const u16* __restrict__ x, unsigned char* __restrict__ ws, int nch) {
  extern __shared__ __attribute__((aligned(16))) unsigned char smem[];
  u16* xs = (u16*)smem;
  u16* os = (u16*)(smem + 41984);
  const int tid = threadIdx.x, w = tid >> 6, lane = tid & 63;
  const int r16 = lane & 15, g = lane >> 4, par = r16 >> 3, r = r16 & 7;
  const int idx = par ? 0xEEEE : 0x4444;
  const int b = blockIdx.x;
  const int h = b & 31, role = (b >> 5) & 1, ch = b >> 6;
  const int per = NSEG / nch;
  if (role == 0) {
    const u16* wp = (const u16*)(ws + OFF_WQK) + (size_t)(h * 4 + w) * 40 * 64 * 16;
    bf16x16 W[4][10];
#pragma unroll
    for (int tn = 0; tn < 4; ++tn)
#pragma unroll
      for (int i = 0; i < 10; ++i) W[tn][i] = ldfrag(wp, tn * 10 + i, lane);
    for (int t = ch * per; t < (ch + 1) * per; ++t) {
      __syncthreads();
      stage_x(x, t, xs, tid);
      __syncthreads();
#pragma unroll 1
      for (int m = 0; m < 4; ++m) {
        f32x4 acc[4] = {};
#pragma unroll
        for (int i = 0; i < 10; ++i) {
          bf16x8 a = *(const bf16x8*)(xs + (8 * m + r) * XS + 64 * i + 8 * (2 * g + par));
#pragma unroll
          for (int tn = 0; tn < 4; ++tn) acc[tn] = smf(a, W[tn][i], acc[tn], idx);
        }
#pragma unroll
        for (int pp = 0; pp < 2; ++pp)
#pragma unroll
          for (int j = 0; j < 4; ++j) {
            float v = comb(acc[2 * pp][j], acc[2 * pp + 1][j]);
            int tile = 2 * pp + (lane >> 5), tok = 8 * m + 4 * (g & 1) + j;
            os[tok * OS + 64 * w + 16 * tile + r16] = f2bf(v);
          }
      }
      __syncthreads();
      u16* qx = (u16*)(ws + OFF_QX) + (size_t)(t * 32 + h) * 4096;
      u16* kx = (u16*)(ws + OFF_KX) + (size_t)(t * 32 + h) * 4096;
      for (int p = tid; p < 1024; p += 256) {
        int row = p >> 5, c = p & 31;
        u32x4 v = *(const u32x4*)(os + row * OS + c * 8);
        if (c < 16) *(u32x4*)(qx + row * 128 + c * 8) = v;
        else *(u32x4*)(kx + row * 128 + (c - 16) * 8) = v;
      }
    }
  } else {
    const u16* wp = (const u16*)(ws + OFF_WVO) + (size_t)(h * 4 + w) * 40 * 64 * 16;
    bf16x16 Wv[2][10], Wo[10][2];
#pragma unroll
    for (int tn = 0; tn < 2; ++tn)
#pragma unroll
      for (int i = 0; i < 10; ++i) Wv[tn][i] = ldfrag(wp, tn * 10 + i, lane);
#pragma unroll
    for (int t2 = 0; t2 < 10; ++t2)
#pragma unroll
      for (int i = 0; i < 2; ++i) Wo[t2][i] = ldfrag(wp, 20 + t2 * 2 + i, lane);
    u16* vs = os;
    for (int t = ch * per; t < (ch + 1) * per; ++t) {
      __syncthreads();
      stage_x(x, t, xs, tid);
      __syncthreads();
#pragma unroll 1
      for (int m = 0; m < 4; ++m) {
        f32x4 acc[2] = {};
#pragma unroll
        for (int i = 0; i < 10; ++i) {
          bf16x8 a = *(const bf16x8*)(xs + (8 * m + r) * XS + 64 * i + 8 * (2 * g + par));
#pragma unroll
          for (int tn = 0; tn < 2; ++tn) acc[tn] = smf(a, Wv[tn][i], acc[tn], idx);
        }
#pragma unroll
        for (int j = 0; j < 4; ++j) {
          float v = comb(acc[0][j], acc[1][j]);
          int tile = (lane >> 5), tok = 8 * m + 4 * (g & 1) + j;
          vs[tok * VS + 32 * w + 16 * tile + r16] = f2bf(v);
        }
      }
      __syncthreads();
      u16* ux = (u16*)(ws + OFF_UX) + (size_t)t * 80 * 32 * 256;
#pragma unroll 1
      for (int m = 0; m < 4; ++m) {
        bf16x8 a0 = *(const bf16x8*)(vs + (8 * m + r) * VS + 8 * (2 * g + par));
        bf16x8 a1 = *(const bf16x8*)(vs + (8 * m + r) * VS + 64 + 8 * (2 * g + par));
#pragma unroll
        for (int pp = 0; pp < 5; ++pp) {
          f32x4 c0 = {}, c1 = {};
          c0 = smf(a0, Wo[2 * pp][0], c0, idx); c0 = smf(a1, Wo[2 * pp][1], c0, idx);
          c1 = smf(a0, Wo[2 * pp + 1][0], c1, idx); c1 = smf(a1, Wo[2 * pp + 1][1], c1, idx);
          float v0 = comb(c0[0], c1[0]), v1 = comb(c0[1], c1[1]), v2 = comb(c0[2], c1[2]), v3 = comb(c0[3], c1[3]);
          int col = 160 * w + 16 * (2 * pp + (lane >> 5)) + r16;
          int tok = 8 * m + 4 * (g & 1);
          u32x2 o = {pk2(v0, v1), pk2(v2, v3)};
          *(u32x2*)(ux + ((size_t)((col >> 3) * 32 + h) * 8 + (col & 7)) * 32 + tok) = o;
        }
      }
    }
  }
}

constexpr unsigned SPIN_CAP = 1u << 18;

__global__ void __launch_bounds__(256, 1) k_rec(const u16* __restrict__ mem0, unsigned char* __restrict__ ws) {
  __shared__ __attribute__((aligned(16))) u16 sq[2][8 * 136];
  __shared__ __attribute__((aligned(16))) float sred[4][64];
  const int tid = threadIdx.x, w = tid >> 6, lane = tid & 63;
  const int r16 = lane & 15, g = lane >> 4, par = r16 >> 3, r = r16 & 7;
  const int idx = par ? 0xEEEE : 0x4444;
  const int b = blockIdx.x;
  const __amdgpu_buffer_rsrc_t rs = __builtin_amdgcn_make_buffer_rsrc((void*)ws, 0, (int)OFF_SENT_END, RSRC_W3);
  unsigned* ctl = (unsigned*)(ws + OFF_CTL);
  bool dead = false;

  if (b < 32) {
    const int h = b;
    const u16* wp = (const u16*)(ws + OFF_WQK) + (size_t)(h * 4 + w) * 40 * 64 * 16;
    bf16x16 W[4][10];
#pragma unroll
    for (int tn = 0; tn < 4; ++tn)
#pragma unroll
      for (int i = 0; i < 10; ++i) W[tn][i] = ldfrag(wp, tn * 10 + i, lane);
    const int moff = (int)OFF_MEM + (2 * g + par) * 128 + r * 16;
    for (int s = 0; s < NSEG; ++s) {
      bf16x8 kxf[2][4];
      if (w == 0) {
        const u16* kx = (const u16*)(ws + OFF_KX) + (size_t)(s * 32 + h) * 4096;
#pragma unroll
        for (int t2 = 0; t2 < 2; ++t2)
#pragma unroll
          for (int kk = 0; kk < 4; ++kk) kxf[t2][kk] = *(const bf16x8*)(kx + (16 * t2 + r16) * 128 + 32 * kk + 8 * g);
      }
      f32x4 acc[4];
      unsigned spins = 0;
      for (;;) {
        asm volatile("" ::: "memory");
        u32x4 a[10];
#pragma unroll
        for (int i = 0; i < 10; ++i) a[i] = __builtin_amdgcn_raw_buffer_load_b128(rs, moff + s * 10240 + i * 1024, 0, AUX_SC1);
        bool ok = true;
        if (__all(vok(a[0])) || dead) {
#pragma unroll
          for (int tn = 0; tn < 4; ++tn) acc[tn] = f32x4{0.f, 0.f, 0.f, 0.f};
#pragma unroll
          for (int i = 0; i < 10; ++i) {
            ok &= vok(a[i]);
            bf16x8 af = __builtin_bit_cast(bf16x8, a[i]);
#pragma unroll
            for (int tn = 0; tn < 4; ++tn) acc[tn] = smf(af, W[tn][i], acc[tn], idx);
          }
          if (__all(ok) || dead) break;
        }
        if (++spins > SPIN_CAP) { dead = true; if (lane == 0) atomicAdd(ctl, 1u); }
      }
      u16* dst = sq[w >> 1];
      u16* kmg = (u16*)(ws + OFF_KM) + (size_t)(s * 32 + h) * 1024;
#pragma unroll
      for (int pp = 0; pp < 2; ++pp)
#pragma unroll
        for (int j = 0; j < 4; ++j) {
          float v = comb(acc[2 * pp][j], acc[2 * pp + 1][j]);
          int tile = 2 * pp + (lane >> 5), row = 4 * (g & 1) + j;
          int col = 64 * (w & 1) + 16 * tile + r16;
          u16 bv = f2bf(v);
          dst[row * 136 + col] = bv;
          if (w >= 2) kmg[row * 128 + col] = bv;
        }
      __syncthreads();
      if (w == 0 && s < NSEG - 1) {
        f32x4 sc[3] = {};
#pragma unroll
        for (int kk = 0; kk < 4; ++kk) {
          bf16x8 qf = *(const bf16x8*)(sq[0] + r * 136 + 32 * kk + 8 * g);
          bf16x8 kf = *(const bf16x8*)(sq[1] + r * 136 + 32 * kk + 8 * g);
          sc[0] = mf16(kf, qf, sc[0]);
          sc[1] = mf16(kxf[0][kk], qf, sc[1]);
          sc[2] = mf16(kxf[1][kk], qf, sc[2]);
        }
        const float scale = 0.08838834764831845f;
        float sv[12];
        float mx = -3.0e38f;
#pragma unroll
        for (int t2 = 0; t2 < 3; ++t2)
#pragma unroll
          for (int j = 0; j < 4; ++j) {
            float v = rbf(rbf(sc[t2][j]) * scale);
            if (t2 == 0 && g >= 2) v = -3.0e38f;
            sv[t2 * 4 + j] = v;
            mx = fmaxf(mx, v);
          }
        mx = fmaxf(mx, __shfl_xor(mx, 16));
        mx = fmaxf(mx, __shfl_xor(mx, 32));
        float sum = 0.f;
#pragma unroll
        for (int q = 0; q < 12; ++q) {
          float e = __expf(sv[q] - mx);
          if (q < 4 && g >= 2) e = 0.f;
          sv[q] = e; sum += e;
        }
        sum += __shfl_xor(sum, 16);
        sum += __shfl_xor(sum, 32);
        float inv = 1.0f / sum;
        if (r16 < 8) {
          int pbase = (int)OFF_P + s * 20480 + h * 640 + r16 * 80;
#pragma unroll
          for (int t2 = 0; t2 < 3; ++t2) {
            u32x2 o = {nosent(pk2(sv[t2 * 4] * inv, sv[t2 * 4 + 1] * inv)), nosent(pk2(sv[t2 * 4 + 2] * inv, sv[t2 * 4 + 3] * inv))};
            int key = (t2 == 0 ? 0 : 8 + 16 * (t2 - 1)) + 4 * g;
            if (t2 > 0 || g < 2) __builtin_amdgcn_raw_buffer_store_b64(o, rs, pbase + key * 2, 0, AUX_SC1);
          }
        }
      }
      __syncthreads();
    }
  } else if (b < 64) {
    const int h = b - 32;
    const u16* wp = (const u16*)(ws + OFF_WVO) + (size_t)(h * 4 + w) * 40 * 64 * 16;
    bf16x16 Wv[2][10], Wo[10][2];
#pragma unroll
    for (int tn = 0; tn < 2; ++tn)
#pragma unroll
      for (int i = 0; i < 10; ++i) Wv[tn][i] = ldfrag(wp, tn * 10 + i, lane);
#pragma unroll
    for (int t2 = 0; t2 < 10; ++t2)
#pragma unroll
      for (int i = 0; i < 2; ++i) Wo[t2][i] = ldfrag(wp, 20 + t2 * 2 + i, lane);
    const int moff = (int)OFF_MEM + (2 * g + par) * 128 + r * 16;
    for (int s = 0; s < NSEG; ++s) {
      f32x4 acc[2];
      unsigned spins = 0;
      for (;;) {
        asm volatile("" ::: "memory");
        u32x4 a[10];
#pragma unroll
        for (int i = 0; i < 10; ++i) a[i] = __builtin_amdgcn_raw_buffer_load_b128(rs, moff + s * 10240 + i * 1024, 0, AUX_SC1);
        bool ok = true;
        if (__all(vok(a[0])) || dead) {
          acc[0] = f32x4{0.f, 0.f, 0.f, 0.f}; acc[1] = acc[0];
#pragma unroll
          for (int i = 0; i < 10; ++i) {
            ok &= vok(a[i]);
            bf16x8 af = __builtin_bit_cast(bf16x8, a[i]);
            acc[0] = smf(af, Wv[0][i], acc[0], idx);
            acc[1] = smf(af, Wv[1][i], acc[1], idx);
          }
          if (__all(ok) || dead) break;
        }
        if (++spins > SPIN_CAP) { dead = true; if (lane == 0) atomicAdd(ctl, 1u); }
      }
      u16* vs = sq[s & 1];
#pragma unroll
      for (int j = 0; j < 4; ++j) {
        float v = comb(acc[0][j], acc[1][j]);
        int row = 4 * (g & 1) + j, col = 32 * w + 16 * (lane >> 5) + r16;
        vs[row * 136 + col] = f2bf(v);
      }
      __syncthreads();
      bf16x8 a0 = *(const bf16x8*)(vs + r * 136 + 8 * (2 * g + par));
      bf16x8 a1 = *(const bf16x8*)(vs + r * 136 + 64 + 8 * (2 * g + par));
      const int ubase = (int)OFF_UM + s * 327680 + h * 128;
#pragma unroll
      for (int pp = 0; pp < 5; ++pp) {
        f32x4 c0 = {}, c1 = {};
        c0 = smf(a0, Wo[2 * pp][0], c0, idx); c1 = smf(a0, Wo[2 * pp + 1][0], c1, idx);
        c0 = smf(a1, Wo[2 * pp][1], c0, idx); c1 = smf(a1, Wo[2 * pp + 1][1], c1, idx);
        float v0 = comb(c0[0], c1[0]), v1 = comb(c0[1], c1[1]), v2 = comb(c0[2], c1[2]), v3 = comb(c0[3], c1[3]);
        int col = 160 * w + 16 * (2 * pp + (lane >> 5)) + r16;
        u32x2 o = {nosent(pk2(v0, v1)), nosent(pk2(v2, v3))};
        __builtin_amdgcn_raw_buffer_store_b64(o, rs, ubase + (col >> 3) * 4096 + (col & 7) * 16 + 8 * (g & 1), 0, AUX_SC1);
      }
    }
  } else if (b < 64 + NSL) {
    const int c = b - 64;
    if (tid < 32) {
      int row = tid >> 2, cp = tid & 3;
      unsigned v = *(const unsigned*)(mem0 + row * 640 + c * 8 + cp * 2);
      __builtin_amdgcn_raw_buffer_store_b32(nosent(v), rs, (int)OFF_MEM + c * 128 + row * 16 + cp * 4, 0, AUX_SC1);
    }
    const int n8 = r16 & 7;
    for (int s = 0; s < NSEG - 1; ++s) {
      const u16* ux = (const u16*)(ws + OFF_UX) + ((size_t)(s * 80 + c) * 32 + 8 * w) * 256;
      bf16x8 bx[8];
#pragma unroll
      for (int i = 0; i < 8; ++i) bx[i] = *(const bf16x8*)(ux + i * 256 + n8 * 32 + 8 * g);
      const int pb = (int)OFF_P + s * 20480 + (8 * w) * 640 + n8 * 80;
      const int ub = (int)OFF_UM + s * 327680 + c * 4096 + (8 * w) * 128 + n8 * 16;
      f32x4 acc;
      unsigned spins = 0;
      for (;;) {
        asm volatile("" ::: "memory");
        u32x4 pa[10], um[2];
#pragma unroll
        for (int i = 0; i < 8; ++i) pa[i] = __builtin_amdgcn_raw_buffer_load_b128(rs, pb + i * 640 + 16 + 16 * g, 0, AUX_SC1);
#pragma unroll
        for (int i = 0; i < 2; ++i) pa[8 + i] = __builtin_amdgcn_raw_buffer_load_b128(rs, pb + (4 * i + g) * 640, 0, AUX_SC1);
#pragma unroll
        for (int i = 0; i < 2; ++i) um[i] = __builtin_amdgcn_raw_buffer_load_b128(rs, ub + (4 * i + g) * 128, 0, AUX_SC1);
        bool ok = true;
#pragma unroll
        for (int i = 0; i < 10; ++i) ok &= vok(pa[i]);
        ok &= vok(um[0]) & vok(um[1]);
        if (__all(ok) || dead) {
          acc = f32x4{0.f, 0.f, 0.f, 0.f};
#pragma unroll
          for (int i = 0; i < 8; ++i) acc = mf16(__builtin_bit_cast(bf16x8, pa[i]), bx[i], acc);
#pragma unroll
          for (int i = 0; i < 2; ++i) acc = mf16(__builtin_bit_cast(bf16x8, pa[8 + i]), __builtin_bit_cast(bf16x8, um[i]), acc);
          break;
        }
        if (++spins > SPIN_CAP) { dead = true; if (lane == 0) atomicAdd(ctl, 1u); }
      }
      if (r16 < 8 && g < 2) {
#pragma unroll
        for (int j = 0; j < 4; ++j) sred[w][(4 * g + j) * 8 + r16] = acc[j];
      }
      __syncthreads();
      if (w == 0) {
        float v = sred[0][lane] + sred[1][lane] + sred[2][lane] + sred[3][lane];
        float vn = __shfl_xor(v, 1);
        if ((lane & 1) == 0) {
          unsigned o = nosent(pk2(v, vn));
          __builtin_amdgcn_raw_buffer_store_b32(o, rs, (int)OFF_MEM + (s + 1) * 10240 + c * 128 + lane * 2, 0, AUX_SC1);
        }
      }
      __syncthreads();
    }
  }
}

__global__ void __launch_bounds__(64) k_px(unsigned char* __restrict__ ws) {
  const int lane = threadIdx.x, r16 = lane & 15, g = lane >> 4;
  const int t = blockIdx.x >> 5, h = blockIdx.x & 31;
  const u16* qx = (const u16*)(ws + OFF_QX) + (size_t)(t * 32 + h) * 4096;
  const u16* kx = (const u16*)(ws + OFF_KX) + (size_t)(t * 32 + h) * 4096;
  const u16* km = (const u16*)(ws + OFF_KM) + (size_t)(t * 32 + h) * 1024;
  u16* px = (u16*)(ws + OFF_PX) + (size_t)(t * 32 + h) * 1280;
  const float scale = 0.08838834764831845f;
  for (int tt = 0; tt < 2; ++tt) {
    f32x4 sc[3] = {};
#pragma unroll
    for (int kk = 0; kk < 4; ++kk) {
      bf16x8 qf = *(const bf16x8*)(qx + (16 * tt + r16) * 128 + 32 * kk + 8 * g);
      bf16x8 k0 = *(const bf16x8*)(km + (r16 & 7) * 128 + 32 * kk + 8 * g);
      bf16x8 k1 = *(const bf16x8*)(kx + r16 * 128 + 32 * kk + 8 * g);
      bf16x8 k2 = *(const bf16x8*)(kx + (16 + r16) * 128 + 32 * kk + 8 * g);
      sc[0] = mf16(k0, qf, sc[0]);
      sc[1] = mf16(k1, qf, sc[1]);
      sc[2] = mf16(k2, qf, sc[2]);
    }
    const int tok = 16 * tt + r16;
    float sv[12];
    float mx = -3.0e38f;
#pragma unroll
    for (int t2 = 0; t2 < 3; ++t2)
#pragma unroll
      for (int j = 0; j < 4; ++j) {
        float v = rbf(rbf(sc[t2][j]) * scale);
        bool exists = (t2 > 0 || g < 2);
        if (t2 > 0) { int key = 16 * (t2 - 1) + 4 * g + j; if (key > tok) v = -1.0e9f; }
        v = rbf(v);
        if (!exists) v = -3.0e38f;
        sv[t2 * 4 + j] = v;
        mx = fmaxf(mx, v);
      }
    mx = fmaxf(mx, __shfl_xor(mx, 16));
    mx = fmaxf(mx, __shfl_xor(mx, 32));
    float sum = 0.f;
#pragma unroll
    for (int q = 0; q < 12; ++q) {
      float e = __expf(sv[q] - mx);
      if (q < 4 && g >= 2) e = 0.f;
      sv[q] = e; sum += e;
    }
    sum += __shfl_xor(sum, 16);
    sum += __shfl_xor(sum, 32);
    float inv = 1.0f / sum;
#pragma unroll
    for (int t2 = 0; t2 < 3; ++t2) {
      u32x2 o = {pk2(sv[t2 * 4] * inv, sv[t2 * 4 + 1] * inv), pk2(sv[t2 * 4 + 2] * inv, sv[t2 * 4 + 3] * inv)};
      int key = (t2 == 0 ? 0 : 8 + 16 * (t2 - 1)) + 4 * g;
      if (t2 > 0 || g < 2) *(u32x2*)(px + tok * 40 + key) = o;
    }
  }
}

__device__ __forceinline__ float gelu_tanh(float x) {
  float u = 0.7978845608028654f * (x + 0.044715f * x * x * x);
  return 0.5f * x * (1.0f + tanhf(u));
}

__global__ void __launch_bounds__(256) k_ffn1(const u16* __restrict__ x, unsigned char* __restrict__ ws) {
  const int tid = threadIdx.x, w = tid >> 6, lane = tid & 63, r16 = lane & 15, g = lane >> 4;
  const int m0 = blockIdx.x * 64 + (w >> 1) * 32, n0 = blockIdx.y * 64 + (w & 1) * 32;
  const u16* wg = (const u16*)(ws + OFF_WGT);
  const u16* wu = (const u16*)(ws + OFF_WUT);
  f32x4 ag[2][2] = {}, au[2][2] = {};
  for (int k = 0; k < 640; k += 32) {
    bf16x8 a[2], bg[2], bu[2];
#pragma unroll
    for (int i = 0; i < 2; ++i) {
      a[i] = *(const bf16x8*)(x + (size_t)(m0 + 16 * i + r16) * 640 + k + 8 * g);
      bg[i] = *(const bf16x8*)(wg + (size_t)(n0 + 16 * i + r16) * 640 + k + 8 * g);
      bu[i] = *(const bf16x8*)(wu + (size_t)(n0 + 16 * i + r16) * 640 + k + 8 * g);
    }
#pragma unroll
    for (int i = 0; i < 2; ++i)
#pragma unroll
      for (int j = 0; j < 2; ++j) { ag[i][j] = mf16(a[i], bg[j], ag[i][j]); au[i][j] = mf16(a[i], bu[j], au[i][j]); }
  }
  u16* hb = (u16*)(ws + OFF_HB);
#pragma unroll
  for (int i = 0; i < 2; ++i)
#pragma unroll
    for (int j = 0; j < 2; ++j)
#pragma unroll
      for (int q = 0; q < 4; ++q) {
        float gv = rbf(ag[i][j][q]), uv = rbf(au[i][j][q]);
        float hv = rbf(gelu_tanh(gv)) * uv;
        hb[(size_t)(m0 + 16 * i + 4 * g + q) * DF + n0 + 16 * j + r16] = f2bf(hv);
      }
}

__global__ void __launch_bounds__(256) k_ffn2(unsigned char* __restrict__ ws) {
  const int tid = threadIdx.x, w = tid >> 6, lane = tid & 63, r16 = lane & 15, g = lane >> 4;
  const int m0 = blockIdx.x * 64 + (w >> 1) * 32, n0 = blockIdx.y * 64 + (w & 1) * 32;
  const u16* hb = (const u16*)(ws + OFF_HB);
  const u16* wd = (const u16*)(ws + OFF_WDT);
  f32x4 acc[2][2] = {};
  for (int k = 0; k < DF; k += 32) {
    bf16x8 a[2], bb[2];
#pragma unroll
    for (int i = 0; i < 2; ++i) {
      a[i] = *(const bf16x8*)(hb + (size_t)(m0 + 16 * i + r16) * DF + k + 8 * g);
      bb[i] = *(const bf16x8*)(wd + (size_t)(n0 + 16 * i + r16) * DF + k + 8 * g);
    }
#pragma unroll
    for (int i = 0; i < 2; ++i)
#pragma unroll
      for (int j = 0; j < 2; ++j) acc[i][j] = mf16(a[i], bb[j], acc[i][j]);
  }
  u16* bo = (u16*)(ws + OFF_BO);
#pragma unroll
  for (int i = 0; i < 2; ++i)
#pragma unroll
    for (int j = 0; j < 2; ++j)
#pragma unroll
      for (int q = 0; q < 4; ++q)
        bo[(size_t)(m0 + 16 * i + 4 * g + q) * DM + n0 + 16 * j + r16] = f2bf(acc[i][j][q]);
}

__global__ void __launch_bounds__(256) k_out(const u16* __restrict__ x, const u16* __restrict__ gw,
                                             unsigned char* __restrict__ ws, u16* __restrict__ out) {
  __shared__ float sg[32];
  const int tid = threadIdx.x, w = tid >> 6, lane = tid & 63, r16 = lane & 15, g = lane >> 4;
  const int t = blockIdx.x, cg = blockIdx.y;
  {
    int tok = tid >> 3, part = tid & 7;
    float s = 0.f;
    for (int d = part; d < 640; d += 8) s += rbf(bf2f(x[(size_t)(t * 32 + tok) * 640 + d]) * bf2f(gw[d]));
    s += __shfl_xor(s, 1); s += __shfl_xor(s, 2); s += __shfl_xor(s, 4);
    if (part == 0) { float z = rbf(s); sg[tok] = rbf(1.0f / (1.0f + __expf(-z))); }
  }
  __syncthreads();
  const int n0 = cg * 128 + w * 32;
  const u16* px = (const u16*)(ws + OFF_PX) + (size_t)t * 32 * 1280;
  const u16* ux = (const u16*)(ws + OFF_UX) + (size_t)t * 80 * 32 * 256;
  const u16* um = (const u16*)(ws + OFF_UM) + (size_t)t * 163840;
  f32x4 acc[2][2] = {};
  for (int h = 0; h < 32; ++h) {
    bf16x8 a[2], bb[2];
#pragma unroll
    for (int i = 0; i < 2; ++i) {
      a[i] = *(const bf16x8*)(px + (size_t)h * 1280 + (16 * i + r16) * 40 + 8 + 8 * g);
      int col = n0 + 16 * i + r16;
      bb[i] = *(const bf16x8*)(ux + ((size_t)((col >> 3) * 32 + h) * 8 + (col & 7)) * 32 + 8 * g);
    }
#pragma unroll
    for (int i = 0; i < 2; ++i)
#pragma unroll
      for (int j = 0; j < 2; ++j) acc[i][j] = mf16(a[i], bb[j], acc[i][j]);
  }
  for (int hq = 0; hq < 8; ++hq) {
    bf16x8 a[2], bb[2];
    int h = 4 * hq + g;
#pragma unroll
    for (int i = 0; i < 2; ++i) {
      a[i] = *(const bf16x8*)(px + (size_t)h * 1280 + (16 * i + r16) * 40);
      int col = n0 + 16 * i + r16;
      bb[i] = *(const bf16x8*)(um + ((size_t)((col >> 3) * 32 + h) * 8 + (col & 7)) * 8);
    }
#pragma unroll
    for (int i = 0; i < 2; ++i)
#pragma unroll
      for (int j = 0; j < 2; ++j) acc[i][j] = mf16(a[i], bb[j], acc[i][j]);
  }
  const u16* bo = (const u16*)(ws + OFF_BO);
#pragma unroll
  for (int i = 0; i < 2; ++i)
#pragma unroll
    for (int j = 0; j < 2; ++j)
#pragma unroll
      for (int q = 0; q < 4; ++q) {
        int tok = 16 * i + 4 * g + q, col = n0 + 16 * j + r16;
        size_t o = (size_t)(t * 32 + tok) * 640 + col;
        float gv = sg[tok];
        float a = rbf(acc[i][j][q]), bv = bf2f(bo[o]);
        float res = rbf(gv * a) + rbf(rbf(1.0f - gv) * bv);
        out[o] = f2bf(res);
      }
}

extern "C" void kernel_launch(void* const* d_in, const int* in_sizes, int n_in, void* d_out, int out_size, void* d_ws,
                              size_t ws_size, hipStream_t stream) {
  const u16* x = (const u16*)d_in[0];
  const u16* mem0 = (const u16*)d_in[1];
  const u16* Wq = (const u16*)d_in[2];
  const u16* Wk = (const u16*)d_in[3];
  const u16* Wv = (const u16*)d_in[4];
  const u16* Wo = (const u16*)d_in[5];
  const u16* wgate = (const u16*)d_in[8];
  const u16* wup = (const u16*)d_in[9];
  const u16* wdown = (const u16*)d_in[10];
  const u16* gw = (const u16*)d_in[11];
  unsigned char* ws = (unsigned char*)d_ws;
  u16* out = (u16*)d_out;

  hipMemsetAsync(ws + OFF_CTL, 0, 4096, stream);
  hipMemsetAsync(ws + OFF_MEM, 0xFF, OFF_SENT_END - OFF_MEM, stream);
  k_repack<<<(2 * 32 * 4 * 40 * 64 + 255) / 256, 256, 0, stream>>>(Wq, Wk, Wv, Wo, (u16*)(ws + OFF_WQK), (u16*)(ws + OFF_WVO));
  k_transpose<<<dim3(DF / 32, DM / 32), dim3(32, 8), 0, stream>>>(wgate, (u16*)(ws + OFF_WGT), DM, DF);
  k_transpose<<<dim3(DF / 32, DM / 32), dim3(32, 8), 0, stream>>>(wup, (u16*)(ws + OFF_WUT), DM, DF);
  k_transpose<<<dim3(DM / 32, DF / 32), dim3(32, 8), 0, stream>>>(wdown, (u16*)(ws + OFF_WDT), DF, DM);
  k_pre<<<256, 256, 41984 + 16896, stream>>>(x, ws, 4);
  k_rec<<<64 + NSL, 256, 0, stream>>>(mem0, ws);
  k_px<<<64 * 32, 64, 0, stream>>>(ws);
  k_ffn1<<<dim3(SEQ / 64, DF / 64), 256, 0, stream>>>(x, ws);
  k_ffn2<<<dim3(SEQ / 64, DM / 64), 256, 0, stream>>>(ws);
  k_out<<<dim3(64, 5), 256, 0, stream>>>(x, gw, ws, out);
}
```

```cpp
#include <hip/hip_runtime.h>
#include <hip/hip_bf16.h>
#include <stdint.h>

typedef __bf16 bf16x8 __attribute__((ext_vector_type(8)));
typedef __bf16 bf16x16 __attribute__((ext_vector_type(16)));
typedef __bf16 bf16x2 __attribute__((ext_vector_type(2)));
typedef float f32x2 __attribute__((ext_vector_type(2)));
typedef float f32x4 __attribute__((ext_vector_type(4)));
typedef unsigned u32x4 __attribute__((ext_vector_type(4)));
typedef unsigned u32x2 __attribute__((ext_vector_type(2)));
typedef unsigned u32x8 __attribute__((ext_vector_type(8)));
typedef short s16x4 __attribute__((ext_vector_type(4)));
typedef unsigned short u16;
typedef unsigned char u8;

constexpr int SEQ = 2048, DM = 640, NSEG = 64, DF = 1728;
constexpr int NSL = 80;

constexpr size_t OFF_CTL = 0;
constexpr size_t CTL_BYTES = 16384;
constexpr int STEPB = 10240 + 20480 + 65536 + 327680;
constexpr size_t OFF_MEM = CTL_BYTES;
constexpr size_t OFF_P = OFF_MEM + 10240;
constexpr size_t OFF_KM = OFF_P + 20480;
constexpr size_t OFF_UM = OFF_KM + 65536;
constexpr size_t OFF_SENT_END = CTL_BYTES + (size_t)66 * STEPB;
constexpr size_t INIT_BYTES = CTL_BYTES + (size_t)3 * STEPB;
constexpr size_t OFF_KX = OFF_SENT_END;
constexpr size_t OFF_UX = OFF_KX + (size_t)SEQ * 4096 * 2;
constexpr size_t OFF_PX = OFF_UX + (size_t)64 * 80 * 32 * 8 * 32 * 2;
constexpr size_t OFF_BO = OFF_PX + (size_t)64 * 32 * 2560;
constexpr size_t OFF_END = OFF_BO + (size_t)SEQ * DM * 2;
constexpr int CW_TMO = 0, CW_QUEUE = 32, CW_KXF = 64, CW_UXF = 1024, CW_PXF = 2048, CW_FF = 3072;

#define AUX_SC1 16
#define RSRC_W3 0x00020000
#define OOB 0x7FFF0000

__device__ __forceinline__ float bf2f(u16 v) { return __uint_as_float(((unsigned)v) << 16); }
__device__ __forceinline__ u16 f2bf(float f) { __bf16 b = (__bf16)f; return __builtin_bit_cast(u16, b); }
__device__ __forceinline__ float rbf(float f) { return (float)((__bf16)f); }
__device__ __forceinline__ unsigned pk2(float a, float b) {
  f32x2 v = {a, b};
  return __builtin_bit_cast(unsigned, __builtin_convertvector(v, bf16x2));
}
__device__ __forceinline__ unsigned nosent(unsigned x) { return x < 0xFFFFFFFEu ? x : 0xFFFFFFFEu; }
__device__ __forceinline__ f32x4 smf(bf16x8 a, bf16x16 b, f32x4 c, int idx) {
  return __builtin_amdgcn_smfmac_f32_16x16x64_bf16(a, b, c, idx, 0, 0);
}
__device__ __forceinline__ f32x4 mf16(bf16x8 a, bf16x8 b, f32x4 c) {
  return __builtin_amdgcn_mfma_f32_16x16x32_bf16(a, b, c, 0, 0, 0);
}
__device__ __forceinline__ float comb(float a, float b) {
  auto r = __builtin_amdgcn_permlane32_swap(__float_as_uint(a), __float_as_uint(b), false, false);
  return __uint_as_float(r[0]) + __uint_as_float(r[1]);
}
__device__ __forceinline__ u32x4 ld128(__amdgpu_buffer_rsrc_t rs, int off) {
  return __builtin_amdgcn_raw_buffer_load_b128(rs, off, 0, AUX_SC1);
}
__device__ __forceinline__ unsigned ld32(__amdgpu_buffer_rsrc_t rs, int off) {
  return __builtin_amdgcn_raw_buffer_load_b32(rs, off, 0, AUX_SC1);
}
__device__ __forceinline__ void st128(u32x4 v, __amdgpu_buffer_rsrc_t rs, int off) {
  __builtin_amdgcn_raw_buffer_store_b128(v, rs, off, 0, AUX_SC1);
}
__device__ __forceinline__ void st64(u32x2 v, __amdgpu_buffer_rsrc_t rs, int off) {
  __builtin_amdgcn_raw_buffer_store_b64(v, rs, off, 0, AUX_SC1);
}
__device__ __forceinline__ void st32(unsigned v, __amdgpu_buffer_rsrc_t rs, int off) {
  __builtin_amdgcn_raw_buffer_store_b32(v, rs, off, 0, AUX_SC1);
}

constexpr unsigned SPIN_CAP = 1u << 18;
#define STAMP(slot) do { } while (0)
#define STAMP_FLUSH() do { } while (0)
#define LDS_BARRIER() do { asm volatile("s_waitcnt lgkmcnt(0)" ::: "memory"); __builtin_amdgcn_s_barrier(); asm volatile("" ::: "memory"); } while (0)
#define WAIT_VM0() asm volatile("s_waitcnt vmcnt(0)" ::: "memory")
#define WAIT_LGKM0() asm volatile("s_waitcnt lgkmcnt(0)" ::: "memory")
#define PIN_A(x) asm volatile("" : "+a"(x))
#define PIN_V(x) asm volatile("" : "+v"(x))
#define LAS __attribute__((address_space(3)))

__device__ __forceinline__ float xmax8(float v) {
  int o = __builtin_amdgcn_update_dpp(0, __float_as_int(v), 0x128, 0xf, 0xf, false);
  return fmaxf(v, __int_as_float(o));
}
__device__ __forceinline__ float xadd8(float v) {
  int o = __builtin_amdgcn_update_dpp(0, __float_as_int(v), 0x128, 0xf, 0xf, false);
  return v + __int_as_float(o);
}
__device__ __forceinline__ float xmax16(float v) {
  auto r = __builtin_amdgcn_permlane16_swap(__float_as_uint(v), __float_as_uint(v), false, false);
  return fmaxf(__uint_as_float(r[0]), __uint_as_float(r[1]));
}
__device__ __forceinline__ float xadd16(float v) {
  auto r = __builtin_amdgcn_permlane16_swap(__float_as_uint(v), __float_as_uint(v), false, false);
  return __uint_as_float(r[0]) + __uint_as_float(r[1]);
}
__device__ __forceinline__ float xmax32(float v) {
  auto r = __builtin_amdgcn_permlane32_swap(__float_as_uint(v), __float_as_uint(v), false, false);
  return fmaxf(__uint_as_float(r[0]), __uint_as_float(r[1]));
}
__device__ __forceinline__ float xadd32(float v) {
  auto r = __builtin_amdgcn_permlane32_swap(__float_as_uint(v), __float_as_uint(v), false, false);
  return __uint_as_float(r[0]) + __uint_as_float(r[1]);
}
__device__ __forceinline__ unsigned umax3(unsigned a, unsigned b, unsigned c) { return max(max(a, b), c); }
__device__ __forceinline__ float blo(unsigned p) { return __uint_as_float(p << 16); }
__device__ __forceinline__ float bhi(unsigned p) { return __uint_as_float(p & 0xFFFF0000u); }

template <int R, int CB>
__device__ __forceinline__ void dma_tile(const u8* src, int ldb, u8* lds, int lane) {
  constexpr int CPR = CB / 16;
  constexpr int N = R * CPR / 64;
#pragma unroll
  for (int q = 0; q < N; ++q) {
    int idx = q * 64 + lane;
    int row = idx / CPR, ch = idx % CPR;
    __builtin_amdgcn_global_load_lds((const unsigned*)(src + (size_t)row * ldb + ch * 16), (unsigned*)(lds + q * 1024), 16, 0, 0);
  }
}
__device__ __forceinline__ u32x2 tr4(const u8* tile, int rsb, int k0, int n0, int lane) {
  int i = lane & 15;
  const u8* p = tile + (k0 + (i >> 2)) * rsb + (n0 + 4 * (i & 3)) * 2;
  s16x4 v = __builtin_amdgcn_ds_read_tr16_b64_v4i16((LAS s16x4*)p);
  return __builtin_bit_cast(u32x2, v);
}
__device__ __forceinline__ bf16x16 frag_sparse(const u8* blk, int rsb, int n0, int lane) {
  int gb = lane >> 4;
  int kb = 16 * (gb >> 1) + 4 * (gb & 1);
  u32x2 t00 = tr4(blk, rsb, kb, n0, lane), t01 = tr4(blk, rsb, kb + 8, n0, lane);
  u32x2 t10 = tr4(blk, rsb, kb + 32, n0, lane), t11 = tr4(blk, rsb, kb + 40, n0, lane);
  u32x8 d = {t00.x, t01.x, t00.y, t01.y, t10.x, t11.x, t10.y, t11.y};
  return __builtin_bit_cast(bf16x16, d);
}
__device__ __forceinline__ bf16x8 frag_dense(const u8* blk, int rsb, int k0, int n0, int lane) {
  int g = lane >> 4;
  u32x2 a = tr4(blk, rsb, k0 + 8 * g, n0, lane), b = tr4(blk, rsb, k0 + 8 * g + 4, n0, lane);
  u32x4 d = {a.x, a.y, b.x, b.y};
  return __builtin_bit_cast(bf16x8, d);
}

template <int MODE> __device__ __forceinline__ int swz(int row) {
  return MODE == 0 ? 2 * (((row >> 1) & 1) | (((row >> 3) & 1) << 1)) : 2 * ((row & 3) | (((row >> 3) & 1) << 2));
}
__device__ __forceinline__ void glds16_asm(const void* gsrc, unsigned lds_dst) {
  unsigned keep;
  asm volatile("s_mov_b32 %0, m0\n\ts_mov_b32 m0, %2\n\ts_nop 0\n\tglobal_load_lds_dwordx4 %1, off\n\ts_mov_b32 m0, %0"
               : "=&s"(keep) : "v"(gsrc), "s"(lds_dst) : "memory");
}
__device__ __forceinline__ void glds16_s(const void* sbase, unsigned voff, unsigned lds_dst) {
  unsigned keep;
  asm volatile("s_mov_b32 %0, m0\n\ts_mov_b32 m0, %3\n\ts_nop 0\n\tglobal_load_lds_dwordx4 %1, %2\n\ts_mov_b32 m0, %0"
               : "=&s"(keep) : "v"(voff), "s"(sbase), "s"(lds_dst) : "memory");
}
__device__ __forceinline__ u32x2 trr(unsigned addr) {
  s16x4 v = __builtin_amdgcn_ds_read_tr16_b64_v4i16((LAS s16x4*)(size_t)addr);
  return __builtin_bit_cast(u32x2, v);
}
__device__ __forceinline__ bf16x8 trfrag(unsigned a0, unsigned a1) {
  u32x2 a = trr(a0), b = trr(a1);
  u32x4 d = {a.x, a.y, b.x, b.y};
  return __builtin_bit_cast(bf16x8, d);
}
template <int MODE, int CB>
__device__ __forceinline__ void dma_swz(const u8* src, int ldb, unsigned lds, int q, int lane) {
  constexpr int CPR = CB / 16;
  int idx = q * 64 + lane;
  int row = idx / CPR, ch = (idx % CPR) ^ swz<MODE>(row);
  glds16_asm(src + (size_t)row * ldb + ch * 16, lds + q * 1024);
}
template <int MODE>
__device__ __forceinline__ u32x2 tr4s(const u8* tile, int rsb, int k0, int n0, int lane) {
  int i = lane & 15;
  int row = k0 + (i >> 2), n = n0 + 4 * (i & 3);
  const u8* p = tile + row * rsb + (((n >> 3) ^ swz<MODE>(row)) << 4) + (n & 7) * 2;
  s16x4 v = __builtin_amdgcn_ds_read_tr16_b64_v4i16((LAS s16x4*)p);
  return __builtin_bit_cast(u32x2, v);
}
template <int MODE>
__device__ __forceinline__ bf16x8 frag_dense_s(const u8* blk, int rsb, int k0, int n0, int lane) {
  int g = lane >> 4;
  u32x2 a = tr4s<MODE>(blk, rsb, k0 + 8 * g, n0, lane), b = tr4s<MODE>(blk, rsb, k0 + 8 * g + 4, n0, lane);
  u32x4 d = {a.x, a.y, b.x, b.y};
  return __builtin_bit_cast(bf16x8, d);
}
__device__ __forceinline__ float gelu_tanh(float x) {
  float u = 0.7978845608028654f * (x + 0.044715f * x * x * x);
  return x * __builtin_amdgcn_rcpf(1.0f + __builtin_amdgcn_exp2f(-2.8853900817779268f * u));
}

__device__ __forceinline__ void wait_flag(__amdgpu_buffer_rsrc_t rs, int cw, unsigned v, bool& dead, unsigned* ctl, int lane) {
  unsigned spins = 0;
  while (!dead) {
    asm volatile("" ::: "memory");
    int f = (int)ld32(rs, cw * 4);
    if (__all(f >= (int)v)) break;
    __builtin_amdgcn_s_sleep(2);
    if (++spins > SPIN_CAP) { dead = true; if (lane == 0) atomicAdd(ctl, 1u); }
  }
  asm volatile("" ::: "memory");
}

#define SCHED() __builtin_amdgcn_sched_barrier(0)
#define PROJ_GEMM(ACC, WF) do { \
    bf16x8 A0_[4], A1_[4]; \
    _Pragma("unroll") for (int ks = 0; ks < 3; ++ks) { \
      A0_[ks] = *(const bf16x8*)(xs + r16 * XS + 32 * ks + 8 * g); A1_[ks] = *(const bf16x8*)(xs + (16 + r16) * XS + 32 * ks + 8 * g); } \
    _Pragma("unroll") for (int ks = 0; ks < 20; ++ks) { \
      if (ks + 3 < 20) { A0_[(ks + 3) & 3] = *(const bf16x8*)(xs + r16 * XS + 32 * (ks + 3) + 8 * g); \
                         A1_[(ks + 3) & 3] = *(const bf16x8*)(xs + (16 + r16) * XS + 32 * (ks + 3) + 8 * g); } \
      SCHED(); \
      _Pragma("unroll") for (int tn = 0; tn < 2; ++tn) { \
        ACC[0][tn] = mf16(A0_[ks & 3], WF[tn][ks], ks == 0 ? Z4 : ACC[0][tn]); \
        ACC[1][tn] = mf16(A1_[ks & 3], WF[tn][ks], ks == 0 ? Z4 : ACC[1][tn]); } \
      SCHED(); \
    } } while (0)

#define LDX(t_) do { _Pragma("unroll") for (int q = 0; q < 10; ++q) { int pc = q * 256 + tid; int row = pc / 80, cc = pc % 80; \
      xp[q] = *(const u32x4*)(p.x + (size_t)((t_) * 32 + row) * 640 + cc * 8); } } while (0)
#define STX() do { _Pragma("unroll") for (int q = 0; q < 10; ++q) { int pc = q * 256 + tid; int row = pc / 80, cc = pc % 80; \
      *(u32x4*)(xs + row * XS + cc * 8) = xp[q]; } } while (0)
constexpr int XS = 648;
constexpr int KS = 136;

struct Params {
  const u16* x; const u16* mem0; const u16* Wq; const u16* Wk; const u16* Wv; const u16* Wo;
  const u16* Wg; const u16* Wu; const u16* Wd; const u16* gw; u8* ws; u16* out;
};
constexpr int NCH = 10;
constexpr int NTASK = NSEG * NCH;

__global__ void __launch_bounds__(256, 1) k_main(Params p) {
  extern __shared__ __attribute__((aligned(16))) u8 smem[];
  u8* ws = p.ws;
  const int tid = threadIdx.x, w = __builtin_amdgcn_readfirstlane(tid >> 6), lane = tid & 63;
  const int r16 = lane & 15, g = lane >> 4, par = r16 >> 3, r = r16 & 7;
  const int idx = par ? 0xEEEE : 0x4444;
  const int b = blockIdx.x;
  const __amdgpu_buffer_rsrc_t rs = __builtin_amdgcn_make_buffer_rsrc((void*)ws, 0, (int)OFF_END, RSRC_W3);
  unsigned* ctl = (unsigned*)(ws + OFF_CTL);
  bool dead = false;
  const f32x4 Z4 = {0.f, 0.f, 0.f, 0.f};
  const u32x4 PZ = {0xFFFFFFFFu, 0xFFFFFFFFu, 0xFFFFFFFFu, 0xFFFFFFFFu};
  u8* wl = smem + w * 40960;

  if (b < 32) {
    const int h = b;
    u16* sq = (u16*)smem;
    unsigned* spl = (unsigned*)(smem + 4608);
    u32x4* smem_a = (u32x4*)(smem + 8192);
    bf16x16 W[4][10];
    {
      const u8* src = (const u8*)((w < 2 ? p.Wq : p.Wk) + h * 128 + 64 * (w & 1));
#pragma unroll
      for (int rd = 0; rd < 2; ++rd) {
#pragma unroll
        for (int i = 0; i < 5; ++i) dma_tile<64, 128>(src + (size_t)(rd * 5 + i) * 64 * 8192, 8192, wl + i * 8192, lane);
        WAIT_VM0();
#pragma unroll
        for (int i = 0; i < 5; ++i)
#pragma unroll
          for (int tn = 0; tn < 4; ++tn) W[tn][rd * 5 + i] = frag_sparse(wl + i * 8192, 128, 16 * tn, lane);
        WAIT_LGKM0();
      }
    }
#pragma unroll
    for (int tn = 0; tn < 4; ++tn)
#pragma unroll
      for (int i = 0; i < (tn < 2 ? 8 : 7); ++i) PIN_A(W[tn][i]);
    LDS_BARRIER();
    const int moff = (int)OFF_MEM + (2 * g + par) * 128 + r * 16;
    for (int s = 0; s < NSEG; ++s) {
      u32x4 kxf[2][4];
      if (w == 0 && s < NSEG - 1) {
        wait_flag(rs, CW_KXF + 16 * h, (unsigned)s, dead, ctl, lane);
        const int kxo = (int)OFF_KX + (s * 32 + h) * 8192 + r16 * 256 + 16 * g;
#pragma unroll
        for (int t2 = 0; t2 < 2; ++t2)
#pragma unroll
          for (int kk = 0; kk < 4; ++kk) kxf[t2][kk] = ld128(rs, kxo + t2 * 4096 + 64 * kk);
      }
      f32x4 acc[4];
      unsigned spins = 0;
      STAMP(0);
      for (;;) {
        asm volatile("" ::: "memory");
        u32x4 a0 = ld128(rs, moff + s * STEPB + w * 1024);
        u32x4 a1 = ld128(rs, moff + s * STEPB + (w + 4) * 1024);
        u32x4 a2 = ld128(rs, w < 2 ? moff + s * STEPB + (w + 8) * 1024 : OOB);
        asm volatile("" ::: "memory");
        unsigned mxv = umax3(umax3(a0.x, a0.z, a1.x), a1.z, umax3(a2.x, a2.z, 0u));
        if (__all(mxv != 0xFFFFFFFFu) || dead) {
          smem_a[w * 64 + lane] = a0; smem_a[(w + 4) * 64 + lane] = a1;
          if (w < 2) smem_a[(w + 8) * 64 + lane] = a2;
          break;
        }
        if (++spins > SPIN_CAP) { dead = true; if (lane == 0) atomicAdd(ctl, 1u); }
      }
      LDS_BARRIER();
#pragma unroll
      for (int tn = 0; tn < 4; ++tn) { acc[tn] = Z4; PIN_V(acc[tn]); }
      {
        u32x4 af[10];
#pragma unroll
        for (int i = 0; i < 10; ++i) af[i] = smem_a[i * 64 + lane];
        SCHED();
#pragma unroll
        for (int i = 0; i < 10; ++i)
#pragma unroll
          for (int tn = 0; tn < 4; ++tn) acc[tn] = smf(__builtin_bit_cast(bf16x8, af[i]), W[tn][i], acc[tn], idx);
      }
      STAMP(1);
      u16* dst = sq + (w >> 1) * (8 * KS) + (4 * (g & 1)) * KS + 64 * (w & 1) + 16 * (lane >> 5) + r16;
#pragma unroll
      for (int pp = 0; pp < 2; ++pp) {
        float v0 = comb(acc[2 * pp][0], acc[2 * pp + 1][0]), v1 = comb(acc[2 * pp][1], acc[2 * pp + 1][1]);
        float v2 = comb(acc[2 * pp][2], acc[2 * pp + 1][2]), v3 = comb(acc[2 * pp][3], acc[2 * pp + 1][3]);
        unsigned p01 = pk2(v0, v1), p23 = pk2(v2, v3);
        dst[32 * pp] = (u16)p01; dst[32 * pp + KS] = (u16)(p01 >> 16);
        dst[32 * pp + 2 * KS] = (u16)p23; dst[32 * pp + 3 * KS] = (u16)(p23 >> 16);
      }
      LDS_BARRIER();
      STAMP(2);
      if (w == 0 && s < NSEG - 1) {
        f32x4 sc[3] = {Z4, Z4, Z4};
#pragma unroll
        for (int kk = 0; kk < 4; ++kk) {
          bf16x8 qf = *(const bf16x8*)(sq + r * KS + 32 * kk + 8 * g);
          bf16x8 kf = *(const bf16x8*)(sq + 8 * KS + r * KS + 32 * kk + 8 * g);
          sc[1] = mf16(__builtin_bit_cast(bf16x8, kxf[0][kk]), qf, sc[1]);
          sc[2] = mf16(__builtin_bit_cast(bf16x8, kxf[1][kk]), qf, sc[2]);
          sc[0] = mf16(kf, qf, sc[0]);
        }
        const float NEG = -3.0e38f;
        float v[6];
        v[0] = par ? sc[1][2] : (g < 2 ? sc[0][0] : NEG);
        v[1] = par ? sc[1][3] : (g < 2 ? sc[0][1] : NEG);
        v[2] = par ? sc[2][0] : (g < 2 ? sc[0][2] : NEG);
        v[3] = par ? sc[2][1] : (g < 2 ? sc[0][3] : NEG);
        v[4] = par ? sc[2][2] : sc[1][0];
        v[5] = par ? sc[2][3] : sc[1][1];
        float mx = fmaxf(fmaxf(fmaxf(v[0], v[1]), fmaxf(v[2], v[3])), fmaxf(v[4], v[5]));
        mx = xmax32(xmax16(xmax8(mx)));
        const float scale = 0.08838834764831845f;
        const float L2E = 1.4426950408889634f;
        float mr = rbf(rbf(mx) * scale) * L2E;
        float e[6];
#pragma unroll
        for (int q = 0; q < 3; ++q) {
          unsigned p1 = pk2(v[2 * q], v[2 * q + 1]);
          unsigned p2 = pk2(blo(p1) * scale, bhi(p1) * scale);
          e[2 * q] = __builtin_amdgcn_exp2f(fmaf(blo(p2), L2E, -mr));
          e[2 * q + 1] = __builtin_amdgcn_exp2f(fmaf(bhi(p2), L2E, -mr));
        }
        float sum = ((e[0] + e[1]) + (e[2] + e[3])) + (e[4] + e[5]);
        sum = xadd32(xadd16(xadd8(sum)));
        float inv = __builtin_amdgcn_rcpf(sum);
        unsigned o0 = pk2(e[0] * inv, e[1] * inv), o1 = pk2(e[2] * inv, e[3] * inv), o2 = pk2(e[4] * inv, e[5] * inv);
        unsigned* pl = spl + r * 4;
        int dA = 32 + (g >> 1) * 32 + 2 * (g & 1), dB = dA + 64;
        int d0 = par ? dA + 1 : 2 * g, d1 = par ? dB : 2 * g + 1, d2 = par ? dB + 1 : dA;
        if (par || g < 2) { pl[d0] = o0; pl[d1] = o1; }
        pl[d2] = o2;
        WAIT_LGKM0();
        if (lane < 40) {
          u32x4 o = *(const u32x4*)(spl + lane * 4);
          o.x = nosent(o.x); o.y = nosent(o.y); o.z = nosent(o.z); o.w = nosent(o.w);
          st128(o, rs, (int)OFF_P + s * STEPB + h * 640 + lane * 16);
          st128(PZ, rs, (int)OFF_P + (s + 2) * STEPB + h * 640 + lane * 16);
        }
      }
      if (w == 1) {
#pragma unroll
        for (int q = 0; q < 2; ++q) {
          int pc = q * 64 + lane, row = pc >> 4, ch = pc & 15;
          u32x4 o = *(const u32x4*)(sq + 8 * KS + row * KS + ch * 8);
          o.x = nosent(o.x); o.y = nosent(o.y); o.z = nosent(o.z); o.w = nosent(o.w);
          st128(o, rs, (int)OFF_KM + s * STEPB + h * 2048 + pc * 16);
          st128(PZ, rs, (int)OFF_KM + (s + 2) * STEPB + h * 2048 + pc * 16);
        }
      }
      STAMP(3);
      LDS_BARRIER();
      STAMP_FLUSH();
    }
  } else if (b < 64) {
    const int h = b - 32;
    u16* sq = (u16*)smem;
    u32x4* smem_a = (u32x4*)(smem + 8192);
    bf16x16 Wv[2][10], Wo[10][2];
    {
      const u8* src = (const u8*)(p.Wv + h * 128 + 32 * w);
#pragma unroll
      for (int i = 0; i < 10; ++i) dma_tile<64, 64>(src + (size_t)i * 64 * 8192, 8192, wl + i * 4096, lane);
      WAIT_VM0();
#pragma unroll
      for (int i = 0; i < 10; ++i)
#pragma unroll
        for (int tn = 0; tn < 2; ++tn) Wv[tn][i] = frag_sparse(wl + i * 4096, 64, 16 * tn, lane);
      WAIT_LGKM0();
      const u8* so = (const u8*)(p.Wo + (size_t)h * 128 * 640 + 160 * w);
#pragma unroll
      for (int i = 0; i < 2; ++i) dma_tile<64, 320>(so + (size_t)i * 64 * 1280, 1280, wl + i * 20480, lane);
      WAIT_VM0();
#pragma unroll
      for (int i = 0; i < 2; ++i)
#pragma unroll
        for (int t2 = 0; t2 < 10; ++t2) Wo[t2][i] = frag_sparse(wl + i * 20480, 320, 16 * t2, lane);
      WAIT_LGKM0();
    }
#pragma unroll
    for (int tn = 0; tn < 2; ++tn)
#pragma unroll
      for (int i = 0; i < 6; ++i) PIN_A(Wv[tn][i]);
#pragma unroll
    for (int t2 = 0; t2 < 10; ++t2)
#pragma unroll
      for (int i = 0; i < 2; ++i) PIN_A(Wo[t2][i]);
    LDS_BARRIER();
    const int moff = (int)OFF_MEM + (2 * g + par) * 128 + r * 16;
    const int ucol = 160 * w + 16 * (lane >> 5) + r16;
    const int uoff = (int)OFF_UM + h * 128 + (ucol >> 3) * 4096 + (ucol & 7) * 16 + 8 * (g & 1);
    for (int s = 0; s < NSEG; ++s) {
      f32x4 acc[2];
      unsigned spins = 0;
      STAMP(0);
      for (;;) {
        asm volatile("" ::: "memory");
        u32x4 a0 = ld128(rs, moff + s * STEPB + w * 1024);
        u32x4 a1 = ld128(rs, moff + s * STEPB + (w + 4) * 1024);
        u32x4 a2 = ld128(rs, w < 2 ? moff + s * STEPB + (w + 8) * 1024 : OOB);
        asm volatile("" ::: "memory");
        unsigned mxv = umax3(umax3(a0.x, a0.z, a1.x), a1.z, umax3(a2.x, a2.z, 0u));
        if (__all(mxv != 0xFFFFFFFFu) || dead) {
          smem_a[w * 64 + lane] = a0; smem_a[(w + 4) * 64 + lane] = a1;
          if (w < 2) smem_a[(w + 8) * 64 + lane] = a2;
          break;
        }
        if (++spins > SPIN_CAP) { dead = true; if (lane == 0) atomicAdd(ctl, 1u); }
      }
      LDS_BARRIER();
      acc[0] = Z4; acc[1] = Z4; PIN_V(acc[0]); PIN_V(acc[1]);
      {
        u32x4 af[10];
#pragma unroll
        for (int i = 0; i < 10; ++i) af[i] = smem_a[i * 64 + lane];
        SCHED();
#pragma unroll
        for (int i = 0; i < 10; ++i) {
          acc[0] = smf(__builtin_bit_cast(bf16x8, af[i]), Wv[0][i], acc[0], idx);
          acc[1] = smf(__builtin_bit_cast(bf16x8, af[i]), Wv[1][i], acc[1], idx);
        }
      }
      STAMP(1);
      u16* vs = sq + (s & 1) * (8 * KS);
      {
        u16* dst = vs + (4 * (g & 1)) * KS + 32 * w + 16 * (lane >> 5) + r16;
        float v0 = comb(acc[0][0], acc[1][0]), v1 = comb(acc[0][1], acc[1][1]);
        float v2 = comb(acc[0][2], acc[1][2]), v3 = comb(acc[0][3], acc[1][3]);
        unsigned p01 = pk2(v0, v1), p23 = pk2(v2, v3);
        dst[0] = (u16)p01; dst[KS] = (u16)(p01 >> 16); dst[2 * KS] = (u16)p23; dst[3 * KS] = (u16)(p23 >> 16);
      }
      f32x4 c[10];
#pragma unroll
      for (int t2 = 0; t2 < 10; ++t2) { c[t2] = Z4; PIN_V(c[t2]); }
      LDS_BARRIER();
      STAMP(2);
      bf16x8 a0 = *(const bf16x8*)(vs + r * KS + 8 * (2 * g + par));
      bf16x8 a1 = *(const bf16x8*)(vs + r * KS + 64 + 8 * (2 * g + par));
#pragma unroll
      for (int t2 = 0; t2 < 10; ++t2) c[t2] = smf(a0, Wo[t2][0], c[t2], idx);
#pragma unroll
      for (int t2 = 0; t2 < 10; ++t2) c[t2] = smf(a1, Wo[t2][1], c[t2], idx);
#pragma unroll
      for (int pp = 0; pp < 5; ++pp) {
        float v0 = comb(c[2 * pp][0], c[2 * pp + 1][0]), v1 = comb(c[2 * pp][1], c[2 * pp + 1][1]);
        float v2 = comb(c[2 * pp][2], c[2 * pp + 1][2]), v3 = comb(c[2 * pp][3], c[2 * pp + 1][3]);
        u32x2 o = {nosent(pk2(v0, v1)), nosent(pk2(v2, v3))};
        st64(o, rs, uoff + s * STEPB + pp * 4 * 4096);
      }
#pragma unroll
      for (int pp = 0; pp < 5; ++pp) st64(u32x2{0xFFFFFFFFu, 0xFFFFFFFFu}, rs, uoff + (s + 2) * STEPB + pp * 4 * 4096);
      STAMP(3);
      STAMP_FLUSH();
    }
  } else if (b < 104) {
    const int c = b - 64;
    float* sred = (float*)smem;
    if (tid < 32) {
      int sh = tid >> 4, row = (tid >> 1) & 7, cp = tid & 1;
      u32x2 v = *(const u32x2*)(p.mem0 + row * 640 + c * 16 + sh * 8 + cp * 4);
      v.x = nosent(v.x); v.y = nosent(v.y);
      st64(v, rs, (int)OFF_MEM + c * 256 + tid * 8);
    }
    const int sl = 2 * c + par;
    for (int s = 0; s < NSEG - 1; ++s) {
      wait_flag(rs, CW_UXF + 16 * (8 * w + (lane & 7)), (unsigned)s, dead, ctl, lane);
      const int uxo = (int)OFF_UX + ((s * 80 + sl) * 32 + 8 * w) * 512 + r * 64 + 16 * g;
      u32x4 bx[8];
#pragma unroll
      for (int i = 0; i < 8; ++i) bx[i] = ld128(rs, uxo + i * 512);
      const int pb = (r16 < 8) ? (int)OFF_P + s * STEPB + (8 * w) * 640 + r * 16 : OOB;
      const int ub = (int)OFF_UM + s * STEPB + sl * 4096 + (8 * w) * 128 + r * 16;
      f32x4 acc0, acc1;
      unsigned spins = 0;
      STAMP(0);
      for (;;) {
        asm volatile("" ::: "memory");
        u32x4 pa[10], um[2];
#pragma unroll
        for (int i = 0; i < 2; ++i) um[i] = ld128(rs, ub + (4 * i + g) * 128);
#pragma unroll
        for (int i = 0; i < 2; ++i) pa[8 + i] = ld128(rs, pb + (4 * i + g) * 640);
#pragma unroll
        for (int i = 0; i < 8; ++i) pa[i] = ld128(rs, pb + i * 640 + 128 + 128 * g);
        asm volatile("" ::: "memory");
        unsigned mxv = umax3(um[0].x, um[0].z, 0u);
        mxv = umax3(mxv, um[1].x, um[1].z);
#pragma unroll
        for (int i = 0; i < 10; ++i) mxv = umax3(mxv, pa[i].x, pa[i].z);
        if (__all(mxv != 0xFFFFFFFFu) || dead) {
          acc0 = mf16(__builtin_bit_cast(bf16x8, pa[8]), __builtin_bit_cast(bf16x8, um[0]), Z4);
          acc1 = mf16(__builtin_bit_cast(bf16x8, pa[9]), __builtin_bit_cast(bf16x8, um[1]), Z4);
#pragma unroll
          for (int i = 0; i < 8; i += 2) {
            acc0 = mf16(__builtin_bit_cast(bf16x8, pa[i]), __builtin_bit_cast(bf16x8, bx[i]), acc0);
            acc1 = mf16(__builtin_bit_cast(bf16x8, pa[i + 1]), __builtin_bit_cast(bf16x8, bx[i + 1]), acc1);
          }
          break;
        }
        if (++spins > SPIN_CAP) { dead = true; if (lane == 0) atomicAdd(ctl, 1u); }
      }
      STAMP(1);
      if (g < 2) {
#pragma unroll
        for (int j = 0; j < 4; ++j) sred[w * 128 + par * 64 + (4 * g + j) * 8 + r] = acc0[j] + acc1[j];
      }
      LDS_BARRIER();
      STAMP(2);
      if (w == 0) {
        const f32x4* sr = (const f32x4*)sred;
        if (lane < 32) {
          f32x4 a = sr[lane], b2 = sr[32 + lane], c2 = sr[64 + lane], d2 = sr[96 + lane];
          f32x4 t = (a + b2) + (c2 + d2);
          u32x2 o = {nosent(pk2(t[0], t[1])), nosent(pk2(t[2], t[3]))};
          st64(o, rs, (int)OFF_MEM + (s + 1) * STEPB + c * 256 + lane * 8);
          st64(u32x2{0xFFFFFFFFu, 0xFFFFFFFFu}, rs, (int)OFF_MEM + (s + 3) * STEPB + c * 256 + lane * 8);
        }
      }
      STAMP(3);
      LDS_BARRIER();
      STAMP_FLUSH();
    }
  } else if (b < 136) {
    const int h = b - 104;
    bf16x8 Wk[2][20];
    {
      const u8* sk = (const u8*)(p.Wk + h * 128 + 32 * w);
#pragma unroll
      for (int i = 0; i < 10; ++i) dma_tile<64, 64>(sk + (size_t)i * 64 * 8192, 8192, wl + i * 4096, lane);
      WAIT_VM0();
#pragma unroll
      for (int ks = 0; ks < 20; ++ks)
#pragma unroll
        for (int tn = 0; tn < 2; ++tn) Wk[tn][ks] = frag_dense(wl, 64, 32 * ks, 16 * tn, lane);
      WAIT_LGKM0();
    }
#pragma unroll
    for (int tn = 0; tn < 2; ++tn)
#pragma unroll
      for (int ks = 0; ks < 20; ++ks) PIN_A(Wk[tn][ks]);
    LDS_BARRIER();
    u16* xs = (u16*)smem;
    u16* kb = (u16*)(smem + 41472);
    u32x4 xp[10];
    LDX(0);
    for (int t = 0; t < NSEG; ++t) {
      STAMP(0);
      STX();
      LDS_BARRIER();
      STAMP(1);
      LDX(t + 1 < NSEG ? t + 1 : 0);
      f32x4 acc[2][2];
      PROJ_GEMM(acc, Wk);
#pragma unroll
      for (int m = 0; m < 2; ++m)
#pragma unroll
        for (int tn = 0; tn < 2; ++tn) {
          u16* dst = kb + (8 * g + 4 * m) * KS + 32 * w + 16 * tn + r16;
          unsigned p01 = pk2(acc[m][tn][0], acc[m][tn][1]), p23 = pk2(acc[m][tn][2], acc[m][tn][3]);
          dst[0] = (u16)p01; dst[KS] = (u16)(p01 >> 16); dst[2 * KS] = (u16)p23; dst[3 * KS] = (u16)(p23 >> 16);
        }
      WAIT_VM0();
      LDS_BARRIER();
      if (tid == 0 && t > 1) st32((unsigned)(t - 1), rs, (CW_KXF + 16 * h) * 4);
      STAMP(2);
#pragma unroll
      for (int q = 0; q < 2; ++q) {
        int pc = q * 256 + tid, row = pc >> 4, ch = pc & 15;
        u32x4 o = *(const u32x4*)(kb + row * KS + ch * 8);
        st128(o, rs, (int)OFF_KX + (t * 32 + h) * 8192 + pc * 16);
      }
      if (t == 0 || t == NSEG - 1) {
        WAIT_VM0();
        LDS_BARRIER();
        if (tid == 0) st32((unsigned)t, rs, (CW_KXF + 16 * h) * 4);
      }
      STAMP(3);
    }
  } else if (b < 168) {
    const int h = b - 136;
    bf16x8 Wq[2][20];
    {
      const u8* sqp = (const u8*)(p.Wq + h * 128 + 32 * w);
#pragma unroll
      for (int i = 0; i < 10; ++i) dma_tile<64, 64>(sqp + (size_t)i * 64 * 8192, 8192, wl + i * 4096, lane);
      WAIT_VM0();
#pragma unroll
      for (int ks = 0; ks < 20; ++ks)
#pragma unroll
        for (int tn = 0; tn < 2; ++tn) Wq[tn][ks] = frag_dense(wl, 64, 32 * ks, 16 * tn, lane);
      WAIT_LGKM0();
    }
#pragma unroll
    for (int tn = 0; tn < 2; ++tn)
#pragma unroll
      for (int ks = 0; ks < 20; ++ks) PIN_A(Wq[tn][ks]);
    LDS_BARRIER();
    u16* xs = (u16*)smem;
    u16* kb = (u16*)(smem + 41472);
    u16* kmb = (u16*)(smem + 41472 + 8704);
    u8* pxb = smem + 41472 + 8704 + 2176;
    u32x4 xp[10];
    LDX(0);
    const float scale = 0.08838834764831845f;
    const float L2E = 1.4426950408889634f;
    for (int u = 0; u < NSEG; ++u) {
      STAMP(0);
      STX();
      LDS_BARRIER();
      STAMP(1);
      LDX(u + 1 < NSEG ? u + 1 : 0);
      f32x4 acc[2][2];
      PROJ_GEMM(acc, Wq);
#pragma unroll
      for (int m = 0; m < 2; ++m)
#pragma unroll
        for (int tn = 0; tn < 2; ++tn) {
          u16* dst = kb + (16 * m + 4 * g) * KS + 32 * w + 16 * tn + r16;
          unsigned p01 = pk2(acc[m][tn][0], acc[m][tn][1]), p23 = pk2(acc[m][tn][2], acc[m][tn][3]);
          dst[0] = (u16)p01; dst[KS] = (u16)(p01 >> 16); dst[2 * KS] = (u16)p23; dst[3 * KS] = (u16)(p23 >> 16);
        }
      u32x4 kxf[2][4];
      if (w < 2) {
        wait_flag(rs, CW_KXF + 16 * h, (unsigned)u, dead, ctl, lane);
        const int kxo = (int)OFF_KX + (u * 32 + h) * 8192 + r16 * 256 + 16 * g;
#pragma unroll
        for (int t2 = 0; t2 < 2; ++t2)
#pragma unroll
          for (int kk = 0; kk < 4; ++kk) kxf[t2][kk] = ld128(rs, kxo + t2 * 4096 + 64 * kk);
      }
      if (w == 3) {
        unsigned spins = 0;
        for (;;) {
          asm volatile("" ::: "memory");
          u32x4 k0 = ld128(rs, (int)OFF_KM + u * STEPB + h * 2048 + lane * 16);
          u32x4 k1 = ld128(rs, (int)OFF_KM + u * STEPB + h * 2048 + 1024 + lane * 16);
          asm volatile("" ::: "memory");
          unsigned mxv = umax3(umax3(k0.x, k0.z, k1.x), k1.z, 0u);
          if (__all(mxv != 0xFFFFFFFFu) || dead) {
            *(u32x4*)(kmb + (lane >> 4) * KS + (lane & 15) * 8) = k0;
            *(u32x4*)(kmb + (4 + (lane >> 4)) * KS + (lane & 15) * 8) = k1;
            break;
          }
          __builtin_amdgcn_s_sleep(8);
          if (++spins > SPIN_CAP) { dead = true; if (lane == 0) atomicAdd(ctl, 1u); }
        }
      }
      LDS_BARRIER();
      STAMP(2);
      if (w < 2) {
        f32x4 sc[3];
#pragma unroll
        for (int kk = 0; kk < 4; ++kk) {
          bf16x8 qf = *(const bf16x8*)(kb + (16 * w + r16) * KS + 32 * kk + 8 * g);
          bf16x8 kf = *(const bf16x8*)(kmb + r * KS + 32 * kk + 8 * g);
          sc[0] = mf16(kf, qf, kk == 0 ? Z4 : sc[0]);
          sc[1] = mf16(__builtin_bit_cast(bf16x8, kxf[0][kk]), qf, kk == 0 ? Z4 : sc[1]);
          sc[2] = mf16(__builtin_bit_cast(bf16x8, kxf[1][kk]), qf, kk == 0 ? Z4 : sc[2]);
        }
        const int tok = 16 * w + r16;
        float sv[12];
        float mx = -3.0e38f;
#pragma unroll
        for (int t2 = 0; t2 < 3; ++t2)
#pragma unroll
          for (int j = 0; j < 4; ++j) {
            float v = rbf(rbf(sc[t2][j]) * scale);
            if (t2 > 0) { int tk = 16 * (g & 1) + 4 * (2 * (t2 - 1) + (g >> 1)) + j; if (tk > tok) v = rbf(-1.0e9f); }
            else if (g >= 2) v = -3.0e38f;
            sv[t2 * 4 + j] = v;
            mx = fmaxf(mx, v);
          }
        mx = xmax32(xmax16(mx));
        float sum = 0.f;
#pragma unroll
        for (int q = 0; q < 12; ++q) {
          float e = __builtin_amdgcn_exp2f((sv[q] - mx) * L2E);
          sv[q] = e; sum += e;
        }
        sum = xadd32(xadd16(sum));
        float inv = __builtin_amdgcn_rcpf(sum);
        if (g < 2) {
          u32x2 o = {pk2(sv[0] * inv, sv[1] * inv), pk2(sv[2] * inv, sv[3] * inv)};
          *(u32x2*)(pxb + tok * 16 + g * 8) = o;
        }
#pragma unroll
        for (int t2 = 1; t2 < 3; ++t2) {
          u32x2 o = {pk2(sv[t2 * 4] * inv, sv[t2 * 4 + 1] * inv), pk2(sv[t2 * 4 + 2] * inv, sv[t2 * 4 + 3] * inv)};
          int kk = 16 * (t2 - 1) + 4 * g;
          *(u32x2*)(pxb + 512 + (kk >> 3) * 512 + tok * 16 + (kk & 7) * 2) = o;
        }
      }
      LDS_BARRIER();
      if (tid < 160) {
        u32x4 o = *(const u32x4*)(pxb + tid * 16);
        st128(o, rs, (int)OFF_PX + (u * 32 + h) * 2560 + tid * 16);
      }
      WAIT_VM0();
      LDS_BARRIER();
      if (tid == 0) st32((unsigned)u, rs, (CW_PXF + 16 * h) * 4);
    }
  } else if (b < 200) {
    const int h = b - 168;
    bf16x8 Wv[2][20], Wo[10][4];
    {
      const u8* sv = (const u8*)(p.Wv + h * 128 + 32 * w);
#pragma unroll
      for (int i = 0; i < 10; ++i) dma_tile<64, 64>(sv + (size_t)i * 64 * 8192, 8192, wl + i * 4096, lane);
      WAIT_VM0();
#pragma unroll
      for (int ks = 0; ks < 20; ++ks)
#pragma unroll
        for (int tn = 0; tn < 2; ++tn) Wv[tn][ks] = frag_dense(wl, 64, 32 * ks, 16 * tn, lane);
      WAIT_LGKM0();
      const u8* so = (const u8*)(p.Wo + (size_t)h * 128 * 640 + 160 * w);
#pragma unroll
      for (int i = 0; i < 2; ++i) dma_tile<64, 320>(so + (size_t)i * 64 * 1280, 1280, wl + i * 20480, lane);
      WAIT_VM0();
#pragma unroll
      for (int ks = 0; ks < 4; ++ks)
#pragma unroll
        for (int t2 = 0; t2 < 10; ++t2) Wo[t2][ks] = frag_dense(wl, 320, 32 * ks, 16 * t2, lane);
      WAIT_LGKM0();
    }
#pragma unroll
    for (int t2 = 0; t2 < 10; ++t2)
#pragma unroll
      for (int ks = 0; ks < 4; ++ks) PIN_A(Wo[t2][ks]);
#pragma unroll
    for (int tn = 0; tn < 2; ++tn)
#pragma unroll
      for (int ks = 0; ks < 8; ++ks) PIN_A(Wv[tn][ks]);
    LDS_BARRIER();
    u16* xs = (u16*)smem;
    u16* vb = (u16*)(smem + 41472);
    u32x4 xp[10];
    LDX(0);
    for (int t = 0; t < NSEG; ++t) {
      STX();
      LDS_BARRIER();
      LDX(t + 1 < NSEG ? t + 1 : 0);
      f32x4 acc[2][2];
      PROJ_GEMM(acc, Wv);
#pragma unroll
      for (int m = 0; m < 2; ++m)
#pragma unroll
        for (int tn = 0; tn < 2; ++tn) {
          u16* dst = vb + (16 * m + 4 * g) * KS + 32 * w + 16 * tn + r16;
          unsigned p01 = pk2(acc[m][tn][0], acc[m][tn][1]), p23 = pk2(acc[m][tn][2], acc[m][tn][3]);
          dst[0] = (u16)p01; dst[KS] = (u16)(p01 >> 16); dst[2 * KS] = (u16)p23; dst[3 * KS] = (u16)(p23 >> 16);
        }
      WAIT_VM0();
      LDS_BARRIER();
      if (tid == 0 && t > 1) st32((unsigned)(t - 1), rs, (CW_UXF + 16 * h) * 4);
      bf16x8 va[2][4];
#pragma unroll
      for (int m = 0; m < 2; ++m)
#pragma unroll
        for (int ks = 0; ks < 4; ++ks) va[m][ks] = *(const bf16x8*)(vb + (16 * m + r16) * KS + 32 * ks + 8 * g);
#pragma unroll
      for (int t2 = 0; t2 < 10; ++t2) {
        f32x4 c0 = Z4, c1 = Z4;
#pragma unroll
        for (int ks = 0; ks < 4; ++ks) { c0 = mf16(va[0][ks], Wo[t2][ks], c0); c1 = mf16(va[1][ks], Wo[t2][ks], c1); }
        int col = 160 * w + 16 * t2 + r16;
        u32x4 o = {pk2(c0[0], c0[1]), pk2(c0[2], c0[3]), pk2(c1[0], c1[1]), pk2(c1[2], c1[3])};
        st128(o, rs, (int)OFF_UX + ((t * 80 + (col >> 3)) * 32 + h) * 512 + (col & 7) * 64 + 16 * g);
      }
      if (t == 0 || t == NSEG - 1) {
        WAIT_VM0();
        LDS_BARRIER();
        if (tid == 0) st32((unsigned)t, rs, (CW_UXF + 16 * h) * 4);
      }
    }
  } else if (b < 232) {
    const int f = b - 200;
    u8* slab0 = smem;
    const unsigned ldsb = __builtin_amdgcn_readfirstlane((unsigned)(size_t)(LAS u8*)smem);
    u8* wdb = smem + 65536;
    u16* hb = (u16*)(smem + 65536 + 81920);
    bf16x8 A[20];
#pragma unroll
    for (int ks = 0; ks < 20; ++ks)
      A[ks] = *(const bf16x8*)(p.x + (size_t)(64 * f + 16 * w + r16) * 640 + 32 * ks + 8 * g);
#pragma unroll
    for (int ks = 0; ks < 16; ++ks) PIN_A(A[ks]);
    f32x4 acc2[4][10];
#pragma unroll
    for (int m = 0; m < 4; ++m)
#pragma unroll
      for (int n = 0; n < 10; ++n) acc2[m][n] = Z4;
    const int i16 = lane & 15;
    unsigned so[2], tb[4], wo2[2], tw;
#pragma unroll
    for (int q = 0; q < 2; ++q) {
      int row = 8 * (2 * w + q) + (lane >> 3);
      so[q] = (unsigned)(row * (DF * 2) + (((lane & 7) ^ swz<0>(row)) << 4));
      wo2[q] = (unsigned)((lane >> 4) * 1280 + (((lane & 15) ^ (2 * ((lane >> 4) | (q << 2)))) << 4));
    }
#pragma unroll
    for (int q = 0; q < 4; ++q) {
      int s0 = swz<0>(8 * g + (i16 >> 2));
      tb[q] = ldsb + (unsigned)((8 * g + (i16 >> 2)) * 128 + (((2 * q + ((i16 & 3) >> 1)) ^ s0) << 4) + (i16 & 1) * 8);
    }
    const int s1 = 2 * ((i16 >> 2) | ((g & 1) << 2));
    tw = ldsb + 65536 + (unsigned)((8 * g + (i16 >> 2)) * 256 + ((i16 & 3) >> 1) * 16 + (i16 & 1) * 8);
#define FFN_SLAB(c_, si_, slot_) do { \
      const u8* sg_ = (const u8*)(Wg_ + (size_t)(64 * (si_)) * DF + 64 * (c_)); \
      const u8* su_ = (const u8*)(Wu_ + (size_t)(64 * (si_)) * DF + 64 * (c_)); \
      unsigned bb_ = ldsb + (slot_) * 16384 + 2 * wq * 1024; \
      glds16_s(sg_, so[0], bb_); glds16_s(sg_, so[1], bb_ + 1024); \
      glds16_s(su_, so[0], bb_ + 8192); glds16_s(su_, so[1], bb_ + 8192 + 1024); } while (0)
#define FFN_WD(si_) ((si_) < 0 ? 0 : (si_) < 6 ? 3 : (si_) == 6 ? 2 : 0)
    WAIT_VM0();
    const u16* Wg_ = p.Wg; const u16* Wu_ = p.Wu; int wq = w;
    FFN_SLAB(0, 0, 0); FFN_SLAB(0, 1, 1); FFN_SLAB(0, 2, 2);
#pragma unroll 1
    for (int c = 0; c < 27; ++c) {
      const int cn = c + 1 < 27 ? c + 1 : 0;
      const int slotb = (c * 10) & 3;
      const u8* sd = (const u8*)(p.Wd + (size_t)(64 * c) * 640);
      f32x4 ag[4], au[4];
#pragma unroll
      for (int si = 0; si < 10; ++si) {
        asm volatile("s_waitcnt vmcnt(%0)" ::"n"(FFN_WD(si - 3) + 4 + FFN_WD(si - 2) + 4 + FFN_WD(si - 1)) : "memory");
        LDS_BARRIER();
        asm volatile("" : "+s"(Wg_), "+s"(Wu_), "+s"(wq));
        if (si + 3 < 10) FFN_SLAB(c, si + 3, (slotb + si + 3) & 3);
        else FFN_SLAB(cn, si + 3 - 10, (slotb + si + 3) & 3);
#pragma unroll
        for (int q = 0; q < FFN_WD(si); ++q) {
          const int P = 20 * wq + 3 * si + q;
          glds16_s(sd + (P & 15) * 4 * 1280 + (P >> 4) * 256, (P & 2) ? wo2[1] : wo2[0], ldsb + 65536 + P * 1024);
        }
        const unsigned sof = ((slotb + si) & 3) * 16384;
#pragma unroll
        for (int k2 = 0; k2 < 2; ++k2) {
          const int ks = si * 2 + k2;
          bf16x8 bg[4], bu[4];
#pragma unroll
          for (int ft = 0; ft < 4; ++ft) {
            const unsigned t0 = tb[ft] + sof + k2 * 4096;
            bg[ft] = trfrag(t0, t0 + 512);
            bu[ft] = trfrag(t0 + 8192, t0 + 8192 + 512);
          }
#pragma unroll
          for (int ft = 0; ft < 4; ++ft) {
            ag[ft] = mf16(A[ks], bg[ft], ks == 0 ? Z4 : ag[ft]);
            au[ft] = mf16(A[ks], bu[ft], ks == 0 ? Z4 : au[ft]);
          }
        }
      }
#pragma unroll
      for (int ft = 0; ft < 4; ++ft)
#pragma unroll
        for (int j = 0; j < 4; ++j) {
          float gv = rbf(ag[ft][j]), uv = rbf(au[ft][j]);
          hb[(16 * w + 4 * g + j) * 72 + 16 * ft + r16] = f2bf(rbf(gelu_tanh(gv)) * uv);
        }
      asm volatile("s_waitcnt vmcnt(12)" ::: "memory");
      LDS_BARRIER();
#pragma unroll
      for (int k2 = 0; k2 < 2; ++k2) {
        bf16x8 ha[4];
#pragma unroll
        for (int m = 0; m < 4; ++m) ha[m] = *(const bf16x8*)(hb + (16 * m + r16) * 72 + 32 * k2 + 8 * g);
#pragma unroll
        for (int n = 0; n < 10; ++n) {
          const int nc = 160 * wq + 16 * n;
          const unsigned ta = tw + (nc >> 7) * 16384 + ((((nc & 127) >> 3) ^ s1) << 4) + k2 * 8192;
          bf16x8 bd = trfrag(ta, ta + 1024);
#pragma unroll
          for (int m = 0; m < 4; ++m) acc2[m][n] = mf16(ha[m], bd, acc2[m][n]);
        }
      }
      LDS_BARRIER();
    }
    WAIT_VM0();
    LDS_BARRIER();
    {
      u16* ob = (u16*)smem;
#pragma unroll
      for (int m = 0; m < 4; ++m)
#pragma unroll
        for (int n = 0; n < 10; ++n)
#pragma unroll
          for (int j = 0; j < 4; ++j) ob[(16 * m + 4 * g + j) * XS + 160 * w + 16 * n + r16] = f2bf(acc2[m][n][j]);
      LDS_BARRIER();
      for (int pc = tid; pc < 64 * 80; pc += 256) {
        int row = pc / 80, cc = pc % 80;
        u32x4 o = *(const u32x4*)(ob + row * XS + cc * 8);
        st128(o, rs, (int)OFF_BO + (64 * f + row) * 1280 + cc * 16);
      }
      WAIT_VM0();
      LDS_BARRIER();
      if (tid == 0) st32(0u, rs, (CW_FF + 16 * f) * 4);
    }
  }

  if (b >= 104 && !(b >= 136 && b < 168)) {
    int tid = threadIdx.x;
    asm volatile("" : "+v"(tid));
    const int lane = tid & 63, r16 = lane & 15, g = lane >> 4;
    float* sg = (float*)smem;
    int* stask = (int*)(smem + 256);
    for (;;) {
      LDS_BARRIER();
      if (tid == 0) *stask = (int)atomicAdd(ctl + CW_QUEUE, 1u) + 1;
      LDS_BARRIER();
      const int task = *stask;
      if (task >= NTASK) break;
      const int t = task / NCH, cj = task % NCH;
      STAMP(0);
      {
        int tok = tid >> 3, part = tid & 7;
        float sacc = 0.f;
        const u16* xr = p.x + (size_t)(t * 32 + tok) * 640 + part * 8;
#pragma unroll
        for (int q = 0; q < 10; ++q) {
          u32x4 xv = *(const u32x4*)(xr + q * 64), gv = *(const u32x4*)(p.gw + part * 8 + q * 64);
#pragma unroll
          for (int e = 0; e < 4; ++e) {
            sacc += rbf(blo(xv[e]) * blo(gv[e]));
            sacc += rbf(bhi(xv[e]) * bhi(gv[e]));
          }
        }
        sacc += __shfl_xor(sacc, 1); sacc += __shfl_xor(sacc, 2); sacc += __shfl_xor(sacc, 4);
        if (part == 0) { float z = rbf(sacc); sg[tok] = rbf(1.0f / (1.0f + __expf(-z))); }
      }
      wait_flag(rs, (lane < 32 ? CW_PXF : CW_UXF) + 16 * (lane & 31), (unsigned)t, dead, ctl, lane);
      wait_flag(rs, CW_FF + 16 * (t >> 1), 0u, dead, ctl, lane);
      STAMP(1);
      const int col = cj * 64 + w * 16 + r16;
      const int uxo = (int)OFF_UX + (t * 80 + (col >> 3)) * 32 * 512 + (col & 7) * 64 + 16 * g;
      const int umo = (int)OFF_UM + t * STEPB + (col >> 3) * 4096 + (col & 7) * 16;
      u8* pxl = smem + 1024;
      {
        const u8* pxg = ws + OFF_PX + (size_t)t * 32 * 2560;
#pragma unroll
        for (int q = 0; q < 20; ++q)
          __builtin_amdgcn_global_load_lds((const unsigned*)(pxg + (20 * w + q) * 1024 + lane * 16), (unsigned*)(pxl + (20 * w + q) * 1024), 16, 0, AUX_SC1);
      }
      u32x4 bx[32];
#pragma unroll
      for (int h = 0; h < 32; ++h) bx[h] = ld128(rs, uxo + h * 512);
      u32x4 um[8];
      {
        unsigned spins = 0;
        for (;;) {
          asm volatile("" ::: "memory");
          unsigned mxv = 0;
#pragma unroll
          for (int hq = 0; hq < 8; ++hq) um[hq] = ld128(rs, umo + (4 * hq + g) * 128);
#pragma unroll
          for (int hq = 0; hq < 8; ++hq) mxv = umax3(mxv, um[hq].x, um[hq].z);
          if (__all(mxv != 0xFFFFFFFFu) || dead) break;
          __builtin_amdgcn_s_sleep(4);
          if (++spins > SPIN_CAP) { dead = true; if (lane == 0) atomicAdd(ctl, 1u); }
        }
      }
      u16 bo[8];
#pragma unroll
      for (int i = 0; i < 2; ++i)
#pragma unroll
        for (int q = 0; q < 4; ++q)
          bo[i * 4 + q] = (u16)__builtin_amdgcn_raw_buffer_load_b16(rs, (int)OFF_BO + ((t * 32 + 16 * i + 4 * g + q) * 640 + col) * 2, 0, AUX_SC1);
      WAIT_VM0();
      LDS_BARRIER();
      f32x4 acc0 = Z4, acc1 = Z4;
#pragma unroll
      for (int h = 0; h < 32; ++h) {
        bf16x8 a0 = *(const bf16x8*)(pxl + h * 2560 + 512 + g * 512 + r16 * 16);
        bf16x8 a1 = *(const bf16x8*)(pxl + h * 2560 + 512 + g * 512 + 256 + r16 * 16);
        acc0 = mf16(a0, __builtin_bit_cast(bf16x8, bx[h]), acc0);
        acc1 = mf16(a1, __builtin_bit_cast(bf16x8, bx[h]), acc1);
      }
#pragma unroll
      for (int hq = 0; hq < 8; ++hq) {
        bf16x8 a0 = *(const bf16x8*)(pxl + (4 * hq + g) * 2560 + r16 * 16);
        bf16x8 a1 = *(const bf16x8*)(pxl + (4 * hq + g) * 2560 + 256 + r16 * 16);
        acc0 = mf16(a0, __builtin_bit_cast(bf16x8, um[hq]), acc0);
        acc1 = mf16(a1, __builtin_bit_cast(bf16x8, um[hq]), acc1);
      }
      STAMP(2);
#pragma unroll
      for (int i = 0; i < 2; ++i)
#pragma unroll
        for (int q = 0; q < 4; ++q) {
          int tok = 16 * i + 4 * g + q;
          int o = (t * 32 + tok) * 640 + col;
          float gv = sg[tok];
          float a = rbf(i == 0 ? acc0[q] : acc1[q]);
          float bv = bf2f(bo[i * 4 + q]);
          float res = rbf(gv * a) + rbf(rbf(1.0f - gv) * bv);
          p.out[o] = f2bf(res);
        }
    }
  }
}

extern "C" void kernel_launch(void* const* d_in, const int* in_sizes, int n_in, void* d_out, int out_size, void* d_ws,
                              size_t ws_size, hipStream_t stream) {
  u8* ws = (u8*)d_ws;
  Params p;
  p.x = (const u16*)d_in[0]; p.mem0 = (const u16*)d_in[1]; p.Wq = (const u16*)d_in[2]; p.Wk = (const u16*)d_in[3];
  p.Wv = (const u16*)d_in[4]; p.Wo = (const u16*)d_in[5];
  p.Wg = (const u16*)d_in[8]; p.Wu = (const u16*)d_in[9]; p.Wd = (const u16*)d_in[10]; p.gw = (const u16*)d_in[11];
  p.ws = ws; p.out = (u16*)d_out;

  (void)hipFuncSetAttribute((const void*)k_main, hipFuncAttributeMaxDynamicSharedMemorySize, 163840);
  (void)hipMemsetAsync(ws, 0xFF, INIT_BYTES, stream);
  k_main<<<256, 256, 163840, stream>>>(p);
}
```

```cpp
#include <hip/hip_runtime.h>
#include <hip/hip_bf16.h>
#include <stdint.h>

typedef __bf16 bf16x8 __attribute__((ext_vector_type(8)));
typedef __bf16 bf16x16 __attribute__((ext_vector_type(16)));
typedef __bf16 bf16x2 __attribute__((ext_vector_type(2)));
typedef float f32x2 __attribute__((ext_vector_type(2)));
typedef float f32x4 __attribute__((ext_vector_type(4)));
typedef unsigned u32x4 __attribute__((ext_vector_type(4)));
typedef unsigned u32x2 __attribute__((ext_vector_type(2)));
typedef unsigned u32x8 __attribute__((ext_vector_type(8)));
typedef short s16x4 __attribute__((ext_vector_type(4)));
typedef unsigned short u16;
typedef unsigned char u8;

constexpr int SEQ = 2048, DM = 640, NSEG = 64, DF = 1728;
constexpr int NSL = 80;

constexpr size_t OFF_CTL = 0;
constexpr size_t CTL_BYTES = 16384;
constexpr int STEPB = 10240 + 20480 + 65536 + 327680;
constexpr size_t OFF_MEM = CTL_BYTES;
constexpr size_t OFF_P = OFF_MEM + 10240;
constexpr size_t OFF_KM = OFF_P + 20480;
constexpr size_t OFF_UM = OFF_KM + 65536;
constexpr size_t OFF_SENT_END = CTL_BYTES + (size_t)66 * STEPB;
constexpr size_t INIT_BYTES = CTL_BYTES + (size_t)3 * STEPB;
constexpr size_t OFF_KX = OFF_SENT_END;
constexpr size_t OFF_UX = OFF_KX + (size_t)SEQ * 4096 * 2;
constexpr size_t OFF_PX = OFF_UX + (size_t)64 * 80 * 32 * 8 * 32 * 2;
constexpr size_t OFF_BO = OFF_PX + (size_t)64 * 32 * 2560;
constexpr size_t OFF_GA = OFF_BO + (size_t)SEQ * DM * 2;
constexpr size_t OFF_END = OFF_GA + (size_t)SEQ * DM * 2;
constexpr int CW_TMO = 0, CW_QUEUE = 32, CW_KXF = 64, CW_UXF = 1024, CW_PXF = 2048, CW_FF = 3072, CW_MIX = 3584;

#define AUX_SC1 16
#define RSRC_W3 0x00020000
#define OOB 0x7FFF0000

__device__ __forceinline__ float bf2f(u16 v) { return __uint_as_float(((unsigned)v) << 16); }
__device__ __forceinline__ u16 f2bf(float f) { __bf16 b = (__bf16)f; return __builtin_bit_cast(u16, b); }
__device__ __forceinline__ float rbf(float f) { return (float)((__bf16)f); }
__device__ __forceinline__ unsigned pk2(float a, float b) {
  f32x2 v = {a, b};
  return __builtin_bit_cast(unsigned, __builtin_convertvector(v, bf16x2));
}
__device__ __forceinline__ unsigned nosent(unsigned x) { return x < 0xFFFFFFFEu ? x : 0xFFFFFFFEu; }
__device__ __forceinline__ f32x4 smf(bf16x8 a, bf16x16 b, f32x4 c, int idx) {
  return __builtin_amdgcn_smfmac_f32_16x16x64_bf16(a, b, c, idx, 0, 0);
}
__device__ __forceinline__ f32x4 mf16(bf16x8 a, bf16x8 b, f32x4 c) {
  return __builtin_amdgcn_mfma_f32_16x16x32_bf16(a, b, c, 0, 0, 0);
}
__device__ __forceinline__ float comb(float a, float b) {
  auto r = __builtin_amdgcn_permlane32_swap(__float_as_uint(a), __float_as_uint(b), false, false);
  return __uint_as_float(r[0]) + __uint_as_float(r[1]);
}
__device__ __forceinline__ u32x4 ld128(__amdgpu_buffer_rsrc_t rs, int off) {
  return __builtin_amdgcn_raw_buffer_load_b128(rs, off, 0, AUX_SC1);
}
__device__ __forceinline__ unsigned ld32(__amdgpu_buffer_rsrc_t rs, int off) {
  return __builtin_amdgcn_raw_buffer_load_b32(rs, off, 0, AUX_SC1);
}
__device__ __forceinline__ void st128(u32x4 v, __amdgpu_buffer_rsrc_t rs, int off) {
  __builtin_amdgcn_raw_buffer_store_b128(v, rs, off, 0, AUX_SC1);
}
__device__ __forceinline__ void st64(u32x2 v, __amdgpu_buffer_rsrc_t rs, int off) {
  __builtin_amdgcn_raw_buffer_store_b64(v, rs, off, 0, AUX_SC1);
}
__device__ __forceinline__ void st32(unsigned v, __amdgpu_buffer_rsrc_t rs, int off) {
  __builtin_amdgcn_raw_buffer_store_b32(v, rs, off, 0, AUX_SC1);
}

constexpr unsigned SPIN_CAP = 1u << 16;
#define STAMP(slot) do { } while (0)
#define STAMP_FLUSH() do { } while (0)
#define LDS_BARRIER() do { asm volatile("s_waitcnt lgkmcnt(0)" ::: "memory"); __builtin_amdgcn_s_barrier(); asm volatile("" ::: "memory"); } while (0)
#define WAIT_VM0() asm volatile("s_waitcnt vmcnt(0)" ::: "memory")
#define WAIT_LGKM0() asm volatile("s_waitcnt lgkmcnt(0)" ::: "memory")
#define PIN_A(x) asm volatile("" : "+a"(x))
#define PIN_V(x) asm volatile("" : "+v"(x))
#define LAS __attribute__((address_space(3)))

__device__ __forceinline__ float xmax8(float v) {
  int o = __builtin_amdgcn_update_dpp(0, __float_as_int(v), 0x128, 0xf, 0xf, false);
  return fmaxf(v, __int_as_float(o));
}
__device__ __forceinline__ float xadd8(float v) {
  int o = __builtin_amdgcn_update_dpp(0, __float_as_int(v), 0x128, 0xf, 0xf, false);
  return v + __int_as_float(o);
}
__device__ __forceinline__ float xmax16(float v) {
  auto r = __builtin_amdgcn_permlane16_swap(__float_as_uint(v), __float_as_uint(v), false, false);
  return fmaxf(__uint_as_float(r[0]), __uint_as_float(r[1]));
}
__device__ __forceinline__ float xadd16(float v) {
  auto r = __builtin_amdgcn_permlane16_swap(__float_as_uint(v), __float_as_uint(v), false, false);
  return __uint_as_float(r[0]) + __uint_as_float(r[1]);
}
__device__ __forceinline__ float xmax32(float v) {
  auto r = __builtin_amdgcn_permlane32_swap(__float_as_uint(v), __float_as_uint(v), false, false);
  return fmaxf(__uint_as_float(r[0]), __uint_as_float(r[1]));
}
__device__ __forceinline__ float xadd32(float v) {
  auto r = __builtin_amdgcn_permlane32_swap(__float_as_uint(v), __float_as_uint(v), false, false);
  return __uint_as_float(r[0]) + __uint_as_float(r[1]);
}
__device__ __forceinline__ unsigned umax3(unsigned a, unsigned b, unsigned c) { return max(max(a, b), c); }
__device__ __forceinline__ float blo(unsigned p) { return __uint_as_float(p << 16); }
__device__ __forceinline__ float bhi(unsigned p) { return __uint_as_float(p & 0xFFFF0000u); }

template <int R, int CB>
__device__ __forceinline__ void dma_tile(const u8* src, int ldb, u8* lds, int lane) {
  constexpr int CPR = CB / 16;
  constexpr int N = R * CPR / 64;
#pragma unroll
  for (int q = 0; q < N; ++q) {
    int idx = q * 64 + lane;
    int row = idx / CPR, ch = idx % CPR;
    __builtin_amdgcn_global_load_lds((const unsigned*)(src + (size_t)row * ldb + ch * 16), (unsigned*)(lds + q * 1024), 16, 0, 0);
  }
}
__device__ __forceinline__ u32x2 tr4(const u8* tile, int rsb, int k0, int n0, int lane) {
  int i = lane & 15;
  const u8* p = tile + (k0 + (i >> 2)) * rsb + (n0 + 4 * (i & 3)) * 2;
  s16x4 v = __builtin_amdgcn_ds_read_tr16_b64_v4i16((LAS s16x4*)p);
  return __builtin_bit_cast(u32x2, v);
}
__device__ __forceinline__ bf16x16 frag_sparse(const u8* blk, int rsb, int n0, int lane) {
  int gb = lane >> 4;
  int kb = 16 * (gb >> 1) + 4 * (gb & 1);
  u32x2 t00 = tr4(blk, rsb, kb, n0, lane), t01 = tr4(blk, rsb, kb + 8, n0, lane);
  u32x2 t10 = tr4(blk, rsb, kb + 32, n0, lane), t11 = tr4(blk, rsb, kb + 40, n0, lane);
  u32x8 d = {t00.x, t01.x, t00.y, t01.y, t10.x, t11.x, t10.y, t11.y};
  return __builtin_bit_cast(bf16x16, d);
}
__device__ __forceinline__ bf16x8 frag_dense(const u8* blk, int rsb, int k0, int n0, int lane) {
  int g = lane >> 4;
  u32x2 a = tr4(blk, rsb, k0 + 8 * g, n0, lane), b = tr4(blk, rsb, k0 + 8 * g + 4, n0, lane);
  u32x4 d = {a.x, a.y, b.x, b.y};
  return __builtin_bit_cast(bf16x8, d);
}

template <int MODE> __device__ __forceinline__ int swz(int row) {
  return MODE == 0 ? 2 * (((row >> 1) & 1) | (((row >> 3) & 1) << 1)) : 2 * ((row & 3) | (((row >> 3) & 1) << 2));
}
__device__ __forceinline__ void glds16_asm(const void* gsrc, unsigned lds_dst) {
  unsigned keep;
  asm volatile("s_mov_b32 %0, m0\n\ts_mov_b32 m0, %2\n\ts_nop 0\n\tglobal_load_lds_dwordx4 %1, off\n\ts_mov_b32 m0, %0"
               : "=&s"(keep) : "v"(gsrc), "s"(lds_dst) : "memory");
}
__device__ __forceinline__ void glds16_s(const void* sbase, unsigned voff, unsigned lds_dst) {
  unsigned keep;
  asm volatile("s_mov_b32 %0, m0\n\ts_mov_b32 m0, %3\n\ts_nop 0\n\tglobal_load_lds_dwordx4 %1, %2\n\ts_mov_b32 m0, %0"
               : "=&s"(keep) : "v"(voff), "s"(sbase), "s"(lds_dst) : "memory");
}
__device__ __forceinline__ u32x2 trr(unsigned addr) {
  s16x4 v = __builtin_amdgcn_ds_read_tr16_b64_v4i16((LAS s16x4*)(size_t)addr);
  return __builtin_bit_cast(u32x2, v);
}
__device__ __forceinline__ bf16x8 trfrag(unsigned a0, unsigned a1) {
  u32x2 a = trr(a0), b = trr(a1);
  u32x4 d = {a.x, a.y, b.x, b.y};
  return __builtin_bit_cast(bf16x8, d);
}
template <int MODE, int CB>
__device__ __forceinline__ void dma_swz(const u8* src, int ldb, unsigned lds, int q, int lane) {
  constexpr int CPR = CB / 16;
  int idx = q * 64 + lane;
  int row = idx / CPR, ch = (idx % CPR) ^ swz<MODE>(row);
  glds16_asm(src + (size_t)row * ldb + ch * 16, lds + q * 1024);
}
template <int MODE>
__device__ __forceinline__ u32x2 tr4s(const u8* tile, int rsb, int k0, int n0, int lane) {
  int i = lane & 15;
  int row = k0 + (i >> 2), n = n0 + 4 * (i & 3);
  const u8* p = tile + row * rsb + (((n >> 3) ^ swz<MODE>(row)) << 4) + (n & 7) * 2;
  s16x4 v = __builtin_amdgcn_ds_read_tr16_b64_v4i16((LAS s16x4*)p);
  return __builtin_bit_cast(u32x2, v);
}
template <int MODE>
__device__ __forceinline__ bf16x8 frag_dense_s(const u8* blk, int rsb, int k0, int n0, int lane) {
  int g = lane >> 4;
  u32x2 a = tr4s<MODE>(blk, rsb, k0 + 8 * g, n0, lane), b = tr4s<MODE>(blk, rsb, k0 + 8 * g + 4, n0, lane);
  u32x4 d = {a.x, a.y, b.x, b.y};
  return __builtin_bit_cast(bf16x8, d);
}
__device__ __forceinline__ float gelu_tanh(float x) {
  float u = 0.7978845608028654f * (x + 0.044715f * x * x * x);
  return x * __builtin_amdgcn_rcpf(1.0f + __builtin_amdgcn_exp2f(-2.8853900817779268f * u));
}

__device__ __forceinline__ float gate8(const u16* xrow, const u16* gw, int part) {
  float sacc = 0.f;
#pragma unroll
  for (int q = 0; q < 10; ++q) {
    u32x4 xv = *(const u32x4*)(xrow + part * 8 + q * 64), gv = *(const u32x4*)(gw + part * 8 + q * 64);
#pragma unroll
    for (int e = 0; e < 4; ++e) {
      sacc += rbf(blo(xv[e]) * blo(gv[e]));
      sacc += rbf(bhi(xv[e]) * bhi(gv[e]));
    }
  }
  sacc += __shfl_xor(sacc, 1); sacc += __shfl_xor(sacc, 2); sacc += __shfl_xor(sacc, 4);
  float z = rbf(sacc);
  return rbf(1.0f / (1.0f + __expf(-z)));
}

template <int SLP = 2>
__device__ __forceinline__ void wait_flag(__amdgpu_buffer_rsrc_t rs, int cw, unsigned v, unsigned* ctl, int lane) {
  for (unsigned spins = 0;; ++spins) {
    asm volatile("" ::: "memory");
    int f = (int)ld32(rs, cw * 4);
    if (__all(f >= (int)v)) break;
    if (spins > SPIN_CAP) { if (lane == 0) atomicAdd(ctl, 1u); break; }
    __builtin_amdgcn_s_sleep(SLP);
  }
  asm volatile("" ::: "memory");
}

#define ADAPT 7
#define AD_DEC 16
#define AD_INC 64
#define D1 4000
#define DV 4500
#define D2 5000
__device__ __forceinline__ void wait_until(unsigned long long t) {
  while (__builtin_amdgcn_s_memtime() < t) __builtin_amdgcn_s_sleep(1);
}
#define OUT_SLP 16
#define SCHED() __builtin_amdgcn_sched_barrier(0)
#define PROJ_GEMM(ACC, WF) do { \
    bf16x8 A0_[4], A1_[4]; \
    _Pragma("unroll") for (int ks = 0; ks < 3; ++ks) { \
      A0_[ks] = *(const bf16x8*)(xs + r16 * XS + 32 * ks + 8 * g); A1_[ks] = *(const bf16x8*)(xs + (16 + r16) * XS + 32 * ks + 8 * g); } \
    _Pragma("unroll") for (int ks = 0; ks < 20; ++ks) { \
      if (ks + 3 < 20) { A0_[(ks + 3) & 3] = *(const bf16x8*)(xs + r16 * XS + 32 * (ks + 3) + 8 * g); \
                         A1_[(ks + 3) & 3] = *(const bf16x8*)(xs + (16 + r16) * XS + 32 * (ks + 3) + 8 * g); } \
      SCHED(); \
      _Pragma("unroll") for (int tn = 0; tn < 2; ++tn) { \
        ACC[0][tn] = mf16(A0_[ks & 3], WF[tn][ks], ks == 0 ? Z4 : ACC[0][tn]); \
        ACC[1][tn] = mf16(A1_[ks & 3], WF[tn][ks], ks == 0 ? Z4 : ACC[1][tn]); } \
      SCHED(); \
    } } while (0)

#define LDX(t_) do { _Pragma("unroll") for (int q = 0; q < 10; ++q) { int pc = q * 256 + tid; int row = pc / 80, cc = pc % 80; \
      xp[q] = *(const u32x4*)(p.x + (size_t)((t_) * 32 + row) * 640 + cc * 8); } } while (0)
#define STX() do { _Pragma("unroll") for (int q = 0; q < 10; ++q) { int pc = q * 256 + tid; int row = pc / 80, cc = pc % 80; \
      *(u32x4*)(xs + row * XS + cc * 8) = xp[q]; } } while (0)
constexpr int XS = 648;
constexpr int KS = 136;

struct Params {
  const u16* x; const u16* mem0; const u16* Wq; const u16* Wk; const u16* Wv; const u16* Wo;
  const u16* Wg; const u16* Wu; const u16* Wd; const u16* gw; u8* ws; u16* out;
};
constexpr int NCH = 10;
constexpr int NTASK = NSEG * NCH;

__global__ void __launch_bounds__(256, 1) k_main(Params p) {
  extern __shared__ __attribute__((aligned(16))) u8 smem[];
  u8* ws = p.ws;
  const int tid = threadIdx.x, w = __builtin_amdgcn_readfirstlane(tid >> 6), lane = tid & 63;
  const int r16 = lane & 15, g = lane >> 4, par = r16 >> 3, r = r16 & 7;
  const int idx = par ? 0xEEEE : 0x4444;
  const int b = blockIdx.x;
  const __amdgpu_buffer_rsrc_t rs = __builtin_amdgcn_make_buffer_rsrc((void*)ws, 0, (int)OFF_END, RSRC_W3);
  unsigned* ctl = (unsigned*)(ws + OFF_CTL);
  const f32x4 Z4 = {0.f, 0.f, 0.f, 0.f};
  unsigned long long tanchor = 0;
  int dly = b < 32 ? D1 : b < 64 ? DV : D2;
#define AD_UPDATE() do { if (s > 0) { if (spins == 0) dly -= AD_DEC; else dly += AD_INC; if (dly < 0) dly = 0; } } while (0)
  const u32x4 PZ = {0xFFFFFFFFu, 0xFFFFFFFFu, 0xFFFFFFFFu, 0xFFFFFFFFu};
  u8* wl = smem + w * 40960;

  if (b < 32) {
    const int h = b;
    u16* sq = (u16*)smem;
    unsigned* spl = (unsigned*)(smem + 4608);
    u32x4* smem_a = (u32x4*)(smem + 8192);
    bf16x16 W[4][10];
    {
      const u8* src = (const u8*)((w < 2 ? p.Wq : p.Wk) + h * 128 + 64 * (w & 1));
#pragma unroll
      for (int rd = 0; rd < 2; ++rd) {
#pragma unroll
        for (int i = 0; i < 5; ++i) dma_tile<64, 128>(src + (size_t)(rd * 5 + i) * 64 * 8192, 8192, wl + i * 8192, lane);
        WAIT_VM0();
#pragma unroll
        for (int i = 0; i < 5; ++i)
#pragma unroll
          for (int tn = 0; tn < 4; ++tn) W[tn][rd * 5 + i] = frag_sparse(wl + i * 8192, 128, 16 * tn, lane);
        WAIT_LGKM0();
      }
    }
#pragma unroll
    for (int tn = 0; tn < 4; ++tn)
#pragma unroll
      for (int i = 0; i < (tn < 2 ? 8 : 7); ++i) PIN_A(W[tn][i]);
    LDS_BARRIER();
    const int moff = (int)OFF_MEM + (2 * g + par) * 128 + r * 16;
    for (int s = 0; s < NSEG; ++s) {
      u32x4 kxf[2][4];
      if (w == 0 && s < NSEG - 1) {
        wait_flag(rs, CW_KXF + 16 * h, (unsigned)s, ctl, lane);
        const int kxo = (int)OFF_KX + (s * 32 + h) * 8192 + r16 * 256 + 16 * g;
#pragma unroll
        for (int t2 = 0; t2 < 2; ++t2)
#pragma unroll
          for (int kk = 0; kk < 4; ++kk) kxf[t2][kk] = ld128(rs, kxo + t2 * 4096 + 64 * kk);
      }
      f32x4 acc[4];
      unsigned spins = 0;
      if ((ADAPT & 1) && s > 0) wait_until(tanchor + dly);
      STAMP(0);
      for (;;) {
        asm volatile("" ::: "memory");
        u32x4 a0 = ld128(rs, moff + s * STEPB + w * 1024);
        u32x4 a1 = ld128(rs, moff + s * STEPB + (w + 4) * 1024);
        u32x4 a2 = ld128(rs, w < 2 ? moff + s * STEPB + (w + 8) * 1024 : OOB);
        asm volatile("" ::: "memory");
        smem_a[w * 64 + lane] = a0; smem_a[(w + 4) * 64 + lane] = a1; smem_a[(w + 8) * 64 + lane] = a2;
        unsigned mxv = umax3(umax3(a0.x, a0.z, a1.x), a1.z, umax3(a2.x, a2.z, 0u));
        if (__all(mxv != 0xFFFFFFFFu) || ++spins > SPIN_CAP) break;
      }
      LDS_BARRIER();
#pragma unroll
      for (int tn = 0; tn < 4; ++tn) { acc[tn] = Z4; PIN_V(acc[tn]); }
      {
        u32x4 af[10];
#pragma unroll
        for (int i = 0; i < 10; ++i) af[i] = smem_a[i * 64 + lane];
        SCHED();
#pragma unroll
        for (int i = 0; i < 10; ++i)
#pragma unroll
          for (int tn = 0; tn < 4; ++tn) acc[tn] = smf(__builtin_bit_cast(bf16x8, af[i]), W[tn][i], acc[tn], idx);
      }
      STAMP(1);
      u16* dst = sq + (w >> 1) * (8 * KS) + (4 * (g & 1)) * KS + 64 * (w & 1) + 16 * (lane >> 5) + r16;
#pragma unroll
      for (int pp = 0; pp < 2; ++pp) {
        float v0 = comb(acc[2 * pp][0], acc[2 * pp + 1][0]), v1 = comb(acc[2 * pp][1], acc[2 * pp + 1][1]);
        float v2 = comb(acc[2 * pp][2], acc[2 * pp + 1][2]), v3 = comb(acc[2 * pp][3], acc[2 * pp + 1][3]);
        unsigned p01 = pk2(v0, v1), p23 = pk2(v2, v3);
        dst[32 * pp] = (u16)p01; dst[32 * pp + KS] = (u16)(p01 >> 16);
        dst[32 * pp + 2 * KS] = (u16)p23; dst[32 * pp + 3 * KS] = (u16)(p23 >> 16);
      }
      LDS_BARRIER();
      STAMP(2);
      if (w == 0 && s < NSEG - 1) {
        f32x4 sc[3] = {Z4, Z4, Z4};
#pragma unroll
        for (int kk = 0; kk < 4; ++kk) {
          bf16x8 qf = *(const bf16x8*)(sq + r * KS + 32 * kk + 8 * g);
          bf16x8 kf = *(const bf16x8*)(sq + 8 * KS + r * KS + 32 * kk + 8 * g);
          sc[1] = mf16(__builtin_bit_cast(bf16x8, kxf[0][kk]), qf, sc[1]);
          sc[2] = mf16(__builtin_bit_cast(bf16x8, kxf[1][kk]), qf, sc[2]);
          sc[0] = mf16(kf, qf, sc[0]);
        }
        const float NEG = -3.0e38f;
        float v[6];
        v[0] = par ? sc[1][2] : (g < 2 ? sc[0][0] : NEG);
        v[1] = par ? sc[1][3] : (g < 2 ? sc[0][1] : NEG);
        v[2] = par ? sc[2][0] : (g < 2 ? sc[0][2] : NEG);
        v[3] = par ? sc[2][1] : (g < 2 ? sc[0][3] : NEG);
        v[4] = par ? sc[2][2] : sc[1][0];
        v[5] = par ? sc[2][3] : sc[1][1];
        float mx = fmaxf(fmaxf(fmaxf(v[0], v[1]), fmaxf(v[2], v[3])), fmaxf(v[4], v[5]));
        mx = xmax32(xmax16(xmax8(mx)));
        const float scale = 0.08838834764831845f;
        const float L2E = 1.4426950408889634f;
        float mr = rbf(rbf(mx) * scale) * L2E;
        float e[6];
#pragma unroll
        for (int q = 0; q < 3; ++q) {
          unsigned p1 = pk2(v[2 * q], v[2 * q + 1]);
          unsigned p2 = pk2(blo(p1) * scale, bhi(p1) * scale);
          e[2 * q] = __builtin_amdgcn_exp2f(fmaf(blo(p2), L2E, -mr));
          e[2 * q + 1] = __builtin_amdgcn_exp2f(fmaf(bhi(p2), L2E, -mr));
        }
        float sum = ((e[0] + e[1]) + (e[2] + e[3])) + (e[4] + e[5]);
        sum = xadd32(xadd16(xadd8(sum)));
        float inv = __builtin_amdgcn_rcpf(sum);
        unsigned o0 = pk2(e[0] * inv, e[1] * inv), o1 = pk2(e[2] * inv, e[3] * inv), o2 = pk2(e[4] * inv, e[5] * inv);
        unsigned* pl = spl + r * 4;
        int dA = 32 + (g >> 1) * 32 + 2 * (g & 1), dB = dA + 64;
        int d0 = par ? dA + 1 : 2 * g, d1 = par ? dB : 2 * g + 1, d2 = par ? dB + 1 : dA;
        if (par || g < 2) { pl[d0] = o0; pl[d1] = o1; }
        pl[d2] = o2;
        WAIT_LGKM0();
        if (lane < 40) {
          u32x4 o = *(const u32x4*)(spl + lane * 4);
          o.x = nosent(o.x); o.y = nosent(o.y); o.z = nosent(o.z); o.w = nosent(o.w);
          st128(o, rs, (int)OFF_P + s * STEPB + h * 640 + lane * 16);
          st128(PZ, rs, (int)OFF_P + (s + 2) * STEPB + h * 640 + lane * 16);
        }
      }
      if (w == 1) {
#pragma unroll
        for (int q = 0; q < 2; ++q) {
          int pc = q * 64 + lane, row = pc >> 4, ch = pc & 15;
          u32x4 o = *(const u32x4*)(sq + 8 * KS + row * KS + ch * 8);
          o.x = nosent(o.x); o.y = nosent(o.y); o.z = nosent(o.z); o.w = nosent(o.w);
          st128(o, rs, (int)OFF_KM + s * STEPB + h * 2048 + pc * 16);
          st128(PZ, rs, (int)OFF_KM + (s + 2) * STEPB + h * 2048 + pc * 16);
        }
      }
      STAMP(3);
      LDS_BARRIER();
      AD_UPDATE();
      tanchor = __builtin_amdgcn_s_memtime();
      STAMP_FLUSH();
    }
  } else if (b < 64) {
    const int h = b - 32;
    u16* sq = (u16*)smem;
    u32x4* smem_a = (u32x4*)(smem + 8192);
    bf16x16 Wv[2][10], Wo[10][2];
    {
      const u8* src = (const u8*)(p.Wv + h * 128 + 32 * w);
#pragma unroll
      for (int i = 0; i < 10; ++i) dma_tile<64, 64>(src + (size_t)i * 64 * 8192, 8192, wl + i * 4096, lane);
      WAIT_VM0();
#pragma unroll
      for (int i = 0; i < 10; ++i)
#pragma unroll
        for (int tn = 0; tn < 2; ++tn) Wv[tn][i] = frag_sparse(wl + i * 4096, 64, 16 * tn, lane);
      WAIT_LGKM0();
      const u8* so = (const u8*)(p.Wo + (size_t)h * 128 * 640 + 160 * w);
#pragma unroll
      for (int i = 0; i < 2; ++i) dma_tile<64, 320>(so + (size_t)i * 64 * 1280, 1280, wl + i * 20480, lane);
      WAIT_VM0();
#pragma unroll
      for (int i = 0; i < 2; ++i)
#pragma unroll
        for (int t2 = 0; t2 < 10; ++t2) Wo[t2][i] = frag_sparse(wl + i * 20480, 320, 16 * t2, lane);
      WAIT_LGKM0();
    }
#pragma unroll
    for (int tn = 0; tn < 2; ++tn)
#pragma unroll
      for (int i = 0; i < 6; ++i) PIN_A(Wv[tn][i]);
#pragma unroll
    for (int t2 = 0; t2 < 10; ++t2)
#pragma unroll
      for (int i = 0; i < 2; ++i) PIN_A(Wo[t2][i]);
    LDS_BARRIER();
    const int moff = (int)OFF_MEM + (2 * g + par) * 128 + r * 16;
    const int ucol = 160 * w + 16 * (lane >> 5) + r16;
    const int uoff = (int)OFF_UM + h * 128 + (ucol >> 3) * 4096 + (ucol & 7) * 16 + 8 * (g & 1);
    for (int s = 0; s < NSEG; ++s) {
      f32x4 acc[2];
      unsigned spins = 0;
      if ((ADAPT & 2) && s > 0) wait_until(tanchor + dly);
      STAMP(0);
      for (;;) {
        asm volatile("" ::: "memory");
        u32x4 a0 = ld128(rs, moff + s * STEPB + w * 1024);
        u32x4 a1 = ld128(rs, moff + s * STEPB + (w + 4) * 1024);
        u32x4 a2 = ld128(rs, w < 2 ? moff + s * STEPB + (w + 8) * 1024 : OOB);
        asm volatile("" ::: "memory");
        smem_a[w * 64 + lane] = a0; smem_a[(w + 4) * 64 + lane] = a1; smem_a[(w + 8) * 64 + lane] = a2;
        unsigned mxv = umax3(umax3(a0.x, a0.z, a1.x), a1.z, umax3(a2.x, a2.z, 0u));
        if (__all(mxv != 0xFFFFFFFFu) || ++spins > SPIN_CAP) break;
      }
      LDS_BARRIER();
      acc[0] = Z4; acc[1] = Z4; PIN_V(acc[0]); PIN_V(acc[1]);
      {
        u32x4 af[10];
#pragma unroll
        for (int i = 0; i < 10; ++i) af[i] = smem_a[i * 64 + lane];
        SCHED();
#pragma unroll
        for (int i = 0; i < 10; ++i) {
          acc[0] = smf(__builtin_bit_cast(bf16x8, af[i]), Wv[0][i], acc[0], idx);
          acc[1] = smf(__builtin_bit_cast(bf16x8, af[i]), Wv[1][i], acc[1], idx);
        }
      }
      STAMP(1);
      u16* vs = sq + (s & 1) * (8 * KS);
      {
        u16* dst = vs + (4 * (g & 1)) * KS + 32 * w + 16 * (lane >> 5) + r16;
        float v0 = comb(acc[0][0], acc[1][0]), v1 = comb(acc[0][1], acc[1][1]);
        float v2 = comb(acc[0][2], acc[1][2]), v3 = comb(acc[0][3], acc[1][3]);
        unsigned p01 = pk2(v0, v1), p23 = pk2(v2, v3);
        dst[0] = (u16)p01; dst[KS] = (u16)(p01 >> 16); dst[2 * KS] = (u16)p23; dst[3 * KS] = (u16)(p23 >> 16);
      }
      f32x4 c[10];
#pragma unroll
      for (int t2 = 0; t2 < 10; ++t2) { c[t2] = Z4; PIN_V(c[t2]); }
      LDS_BARRIER();
      STAMP(2);
      bf16x8 a0 = *(const bf16x8*)(vs + r * KS + 8 * (2 * g + par));
      bf16x8 a1 = *(const bf16x8*)(vs + r * KS + 64 + 8 * (2 * g + par));
#pragma unroll
      for (int t2 = 0; t2 < 10; ++t2) c[t2] = smf(a0, Wo[t2][0], c[t2], idx);
#pragma unroll
      for (int t2 = 0; t2 < 10; ++t2) c[t2] = smf(a1, Wo[t2][1], c[t2], idx);
#pragma unroll
      for (int pp = 0; pp < 5; ++pp) {
        float v0 = comb(c[2 * pp][0], c[2 * pp + 1][0]), v1 = comb(c[2 * pp][1], c[2 * pp + 1][1]);
        float v2 = comb(c[2 * pp][2], c[2 * pp + 1][2]), v3 = comb(c[2 * pp][3], c[2 * pp + 1][3]);
        u32x2 o = {nosent(pk2(v0, v1)), nosent(pk2(v2, v3))};
        st64(o, rs, uoff + s * STEPB + pp * 4 * 4096);
      }
#pragma unroll
      for (int pp = 0; pp < 5; ++pp) st64(u32x2{0xFFFFFFFFu, 0xFFFFFFFFu}, rs, uoff + (s + 2) * STEPB + pp * 4 * 4096);
      STAMP(3);
      AD_UPDATE();
      tanchor = __builtin_amdgcn_s_memtime();
      STAMP_FLUSH();
    }
  } else if (b < 104) {
    const int c = b - 64;
    float* sred = (float*)smem;
    if (tid < 32) {
      int sh = tid >> 4, row = (tid >> 1) & 7, cp = tid & 1;
      u32x2 v = *(const u32x2*)(p.mem0 + row * 640 + c * 16 + sh * 8 + cp * 4);
      v.x = nosent(v.x); v.y = nosent(v.y);
      st64(v, rs, (int)OFF_MEM + c * 256 + tid * 8);
    }
    const int sl = 2 * c + par;
    for (int s = 0; s < NSEG - 1; ++s) {
      wait_flag(rs, CW_UXF + 16 * (8 * w + (lane & 7)), (unsigned)s, ctl, lane);
      const int uxo = (int)OFF_UX + ((s * 80 + sl) * 32 + 8 * w) * 512 + r * 64 + 16 * g;
      u32x4 bx[8];
#pragma unroll
      for (int i = 0; i < 8; ++i) bx[i] = ld128(rs, uxo + i * 512);
      const int pb = (r16 < 8) ? (int)OFF_P + s * STEPB + (8 * w) * 640 + r * 16 : OOB;
      const int ub = (int)OFF_UM + s * STEPB + sl * 4096 + (8 * w) * 128 + r * 16;
      f32x4 acc0, acc1;
      unsigned spins = 0;
      if ((ADAPT & 4) && s > 0) wait_until(tanchor + dly);
      STAMP(0);
      for (;;) {
        asm volatile("" ::: "memory");
        u32x4 pa[10], um[2];
#pragma unroll
        for (int i = 0; i < 2; ++i) um[i] = ld128(rs, ub + (4 * i + g) * 128);
#pragma unroll
        for (int i = 0; i < 2; ++i) pa[8 + i] = ld128(rs, pb + (4 * i + g) * 640);
#pragma unroll
        for (int i = 0; i < 8; ++i) pa[i] = ld128(rs, pb + i * 640 + 128 + 128 * g);
        asm volatile("" ::: "memory");
        unsigned mxv = umax3(um[0].x, um[0].z, 0u);
        mxv = umax3(mxv, um[1].x, um[1].z);
#pragma unroll
        for (int i = 0; i < 10; ++i) mxv = umax3(mxv, pa[i].x, pa[i].z);
        if (__all(mxv != 0xFFFFFFFFu) || spins > SPIN_CAP) {
          acc0 = mf16(__builtin_bit_cast(bf16x8, pa[8]), __builtin_bit_cast(bf16x8, um[0]), Z4);
          acc1 = mf16(__builtin_bit_cast(bf16x8, pa[9]), __builtin_bit_cast(bf16x8, um[1]), Z4);
#pragma unroll
          for (int i = 0; i < 8; i += 2) {
            acc0 = mf16(__builtin_bit_cast(bf16x8, pa[i]), __builtin_bit_cast(bf16x8, bx[i]), acc0);
            acc1 = mf16(__builtin_bit_cast(bf16x8, pa[i + 1]), __builtin_bit_cast(bf16x8, bx[i + 1]), acc1);
          }
          break;
        }
        ++spins;
      }
      STAMP(1);
      if (g < 2) {
#pragma unroll
        for (int j = 0; j < 4; ++j) sred[w * 128 + par * 64 + (4 * g + j) * 8 + r] = acc0[j] + acc1[j];
      }
      LDS_BARRIER();
      STAMP(2);
      if (w == 0) {
        const f32x4* sr = (const f32x4*)sred;
        if (lane < 32) {
          f32x4 a = sr[lane], b2 = sr[32 + lane], c2 = sr[64 + lane], d2 = sr[96 + lane];
          f32x4 t = (a + b2) + (c2 + d2);
          u32x2 o = {nosent(pk2(t[0], t[1])), nosent(pk2(t[2], t[3]))};
          st64(o, rs, (int)OFF_MEM + (s + 1) * STEPB + c * 256 + lane * 8);
          st64(u32x2{0xFFFFFFFFu, 0xFFFFFFFFu}, rs, (int)OFF_MEM + (s + 3) * STEPB + c * 256 + lane * 8);
        }
      }
      STAMP(3);
      LDS_BARRIER();
      AD_UPDATE();
      tanchor = __builtin_amdgcn_s_memtime();
      STAMP_FLUSH();
    }
  } else if (b < 136) {
    const int h = b - 104;
    bf16x8 Wk[2][20];
    {
      const u8* sk = (const u8*)(p.Wk + h * 128 + 32 * w);
#pragma unroll
      for (int i = 0; i < 10; ++i) dma_tile<64, 64>(sk + (size_t)i * 64 * 8192, 8192, wl + i * 4096, lane);
      WAIT_VM0();
#pragma unroll
      for (int ks = 0; ks < 20; ++ks)
#pragma unroll
        for (int tn = 0; tn < 2; ++tn) Wk[tn][ks] = frag_dense(wl, 64, 32 * ks, 16 * tn, lane);
      WAIT_LGKM0();
    }
#pragma unroll
    for (int tn = 0; tn < 2; ++tn)
#pragma unroll
      for (int ks = 0; ks < 20; ++ks) PIN_A(Wk[tn][ks]);
    LDS_BARRIER();
    u16* xs = (u16*)smem;
    u16* kb = (u16*)(smem + 41472);
    u32x4 xp[10];
    LDX(0);
    for (int t = 0; t < NSEG; ++t) {
      STAMP(0);
      STX();
      LDS_BARRIER();
      STAMP(1);
      LDX(t + 1 < NSEG ? t + 1 : 0);
      f32x4 acc[2][2];
      PROJ_GEMM(acc, Wk);
#pragma unroll
      for (int m = 0; m < 2; ++m)
#pragma unroll
        for (int tn = 0; tn < 2; ++tn) {
          u16* dst = kb + (8 * g + 4 * m) * KS + 32 * w + 16 * tn + r16;
          unsigned p01 = pk2(acc[m][tn][0], acc[m][tn][1]), p23 = pk2(acc[m][tn][2], acc[m][tn][3]);
          dst[0] = (u16)p01; dst[KS] = (u16)(p01 >> 16); dst[2 * KS] = (u16)p23; dst[3 * KS] = (u16)(p23 >> 16);
        }
      WAIT_VM0();
      LDS_BARRIER();
      if (tid == 0 && t > 1) st32((unsigned)(t - 1), rs, (CW_KXF + 16 * h) * 4);
      STAMP(2);
#pragma unroll
      for (int q = 0; q < 2; ++q) {
        int pc = q * 256 + tid, row = pc >> 4, ch = pc & 15;
        u32x4 o = *(const u32x4*)(kb + row * KS + ch * 8);
        st128(o, rs, (int)OFF_KX + (t * 32 + h) * 8192 + pc * 16);
      }
      if (t == 0 || t == NSEG - 1) {
        WAIT_VM0();
        LDS_BARRIER();
        if (tid == 0) st32((unsigned)t, rs, (CW_KXF + 16 * h) * 4);
      }
      STAMP(3);
    }
  } else if (b < 168) {
    const int h = b - 136;
    bf16x8 Wq[2][20];
    {
      const u8* sqp = (const u8*)(p.Wq + h * 128 + 32 * w);
#pragma unroll
      for (int i = 0; i < 10; ++i) dma_tile<64, 64>(sqp + (size_t)i * 64 * 8192, 8192, wl + i * 4096, lane);
      WAIT_VM0();
#pragma unroll
      for (int ks = 0; ks < 20; ++ks)
#pragma unroll
        for (int tn = 0; tn < 2; ++tn) Wq[tn][ks] = frag_dense(wl, 64, 32 * ks, 16 * tn, lane);
      WAIT_LGKM0();
    }
#pragma unroll
    for (int tn = 0; tn < 2; ++tn)
#pragma unroll
      for (int ks = 0; ks < 20; ++ks) PIN_A(Wq[tn][ks]);
    LDS_BARRIER();
    u16* xs = (u16*)smem;
    u16* kb = (u16*)(smem + 41472);
    u16* kmb = (u16*)(smem + 41472 + 8704);
    u8* pxb = smem + 41472 + 8704 + 2176;
    u32x4 xp[10];
    LDX(0);
    const float scale = 0.08838834764831845f;
    const float L2E = 1.4426950408889634f;
    for (int u = 0; u < NSEG; ++u) {
      STAMP(0);
      STX();
      LDS_BARRIER();
      STAMP(1);
      LDX(u + 1 < NSEG ? u + 1 : 0);
      f32x4 acc[2][2];
      PROJ_GEMM(acc, Wq);
#pragma unroll
      for (int m = 0; m < 2; ++m)
#pragma unroll
        for (int tn = 0; tn < 2; ++tn) {
          u16* dst = kb + (16 * m + 4 * g) * KS + 32 * w + 16 * tn + r16;
          unsigned p01 = pk2(acc[m][tn][0], acc[m][tn][1]), p23 = pk2(acc[m][tn][2], acc[m][tn][3]);
          dst[0] = (u16)p01; dst[KS] = (u16)(p01 >> 16); dst[2 * KS] = (u16)p23; dst[3 * KS] = (u16)(p23 >> 16);
        }
      u32x4 kxf[2][4];
      if (w < 2) {
        wait_flag(rs, CW_KXF + 16 * h, (unsigned)u, ctl, lane);
        const int kxo = (int)OFF_KX + (u * 32 + h) * 8192 + r16 * 256 + 16 * g;
#pragma unroll
        for (int t2 = 0; t2 < 2; ++t2)
#pragma unroll
          for (int kk = 0; kk < 4; ++kk) kxf[t2][kk] = ld128(rs, kxo + t2 * 4096 + 64 * kk);
      }
      if (w == 3) {
        unsigned spins = 0;
        for (;;) {
          asm volatile("" ::: "memory");
          u32x4 k0 = ld128(rs, (int)OFF_KM + u * STEPB + h * 2048 + lane * 16);
          u32x4 k1 = ld128(rs, (int)OFF_KM + u * STEPB + h * 2048 + 1024 + lane * 16);
          asm volatile("" ::: "memory");
          unsigned mxv = umax3(umax3(k0.x, k0.z, k1.x), k1.z, 0u);
          if (__all(mxv != 0xFFFFFFFFu) || spins > SPIN_CAP) {
          if (spins > SPIN_CAP && lane == 0) atomicAdd(ctl, 1u);
            *(u32x4*)(kmb + (lane >> 4) * KS + (lane & 15) * 8) = k0;
            *(u32x4*)(kmb + (4 + (lane >> 4)) * KS + (lane & 15) * 8) = k1;
            break;
          }
          __builtin_amdgcn_s_sleep(8);
          ++spins;
        }
      }
      LDS_BARRIER();
      STAMP(2);
      if (w < 2) {
        f32x4 sc[3];
#pragma unroll
        for (int kk = 0; kk < 4; ++kk) {
          bf16x8 qf = *(const bf16x8*)(kb + (16 * w + r16) * KS + 32 * kk + 8 * g);
          bf16x8 kf = *(const bf16x8*)(kmb + r * KS + 32 * kk + 8 * g);
          sc[0] = mf16(kf, qf, kk == 0 ? Z4 : sc[0]);
          sc[1] = mf16(__builtin_bit_cast(bf16x8, kxf[0][kk]), qf, kk == 0 ? Z4 : sc[1]);
          sc[2] = mf16(__builtin_bit_cast(bf16x8, kxf[1][kk]), qf, kk == 0 ? Z4 : sc[2]);
        }
        const int tok = 16 * w + r16;
        float sv[12];
        float mx = -3.0e38f;
#pragma unroll
        for (int t2 = 0; t2 < 3; ++t2)
#pragma unroll
          for (int j = 0; j < 4; ++j) {
            float v = rbf(rbf(sc[t2][j]) * scale);
            if (t2 > 0) { int tk = 16 * (g & 1) + 4 * (2 * (t2 - 1) + (g >> 1)) + j; if (tk > tok) v = rbf(-1.0e9f); }
            else if (g >= 2) v = -3.0e38f;
            sv[t2 * 4 + j] = v;
            mx = fmaxf(mx, v);
          }
        mx = xmax32(xmax16(mx));
        float sum = 0.f;
#pragma unroll
        for (int q = 0; q < 12; ++q) {
          float e = __builtin_amdgcn_exp2f((sv[q] - mx) * L2E);
          sv[q] = e; sum += e;
        }
        sum = xadd32(xadd16(sum));
        float inv = __builtin_amdgcn_rcpf(sum);
        if (g < 2) {
          u32x2 o = {pk2(sv[0] * inv, sv[1] * inv), pk2(sv[2] * inv, sv[3] * inv)};
          *(u32x2*)(pxb + tok * 16 + g * 8) = o;
        }
#pragma unroll
        for (int t2 = 1; t2 < 3; ++t2) {
          u32x2 o = {pk2(sv[t2 * 4] * inv, sv[t2 * 4 + 1] * inv), pk2(sv[t2 * 4 + 2] * inv, sv[t2 * 4 + 3] * inv)};
          int kk = 16 * (t2 - 1) + 4 * g;
          *(u32x2*)(pxb + 512 + (kk >> 3) * 512 + tok * 16 + (kk & 7) * 2) = o;
        }
      }
      LDS_BARRIER();
      if (tid < 160) {
        u32x4 o = *(const u32x4*)(pxb + tid * 16);
        st128(o, rs, (int)OFF_PX + (u * 32 + h) * 2560 + tid * 16);
      }
      WAIT_VM0();
      LDS_BARRIER();
      if (tid == 0) st32((unsigned)u, rs, (CW_PXF + 16 * h) * 4);
    }
  } else if (b < 200) {
    const int h = b - 168;
    bf16x8 Wv[2][20], Wo[10][4];
    {
      const u8* sv = (const u8*)(p.Wv + h * 128 + 32 * w);
#pragma unroll
      for (int i = 0; i < 10; ++i) dma_tile<64, 64>(sv + (size_t)i * 64 * 8192, 8192, wl + i * 4096, lane);
      WAIT_VM0();
#pragma unroll
      for (int ks = 0; ks < 20; ++ks)
#pragma unroll
        for (int tn = 0; tn < 2; ++tn) Wv[tn][ks] = frag_dense(wl, 64, 32 * ks, 16 * tn, lane);
      WAIT_LGKM0();
      const u8* so = (const u8*)(p.Wo + (size_t)h * 128 * 640 + 160 * w);
#pragma unroll
      for (int i = 0; i < 2; ++i) dma_tile<64, 320>(so + (size_t)i * 64 * 1280, 1280, wl + i * 20480, lane);
      WAIT_VM0();
#pragma unroll
      for (int ks = 0; ks < 4; ++ks)
#pragma unroll
        for (int t2 = 0; t2 < 10; ++t2) Wo[t2][ks] = frag_dense(wl, 320, 32 * ks, 16 * t2, lane);
      WAIT_LGKM0();
    }
#pragma unroll
    for (int t2 = 0; t2 < 10; ++t2)
#pragma unroll
      for (int ks = 0; ks < 4; ++ks) PIN_A(Wo[t2][ks]);
#pragma unroll
    for (int tn = 0; tn < 2; ++tn)
#pragma unroll
      for (int ks = 0; ks < 8; ++ks) PIN_A(Wv[tn][ks]);
    LDS_BARRIER();
    u16* xs = (u16*)smem;
    u16* vb = (u16*)(smem + 41472);
    u32x4 xp[10];
    LDX(0);
    for (int t = 0; t < NSEG; ++t) {
      STX();
      LDS_BARRIER();
      LDX(t + 1 < NSEG ? t + 1 : 0);
      f32x4 acc[2][2];
      PROJ_GEMM(acc, Wv);
#pragma unroll
      for (int m = 0; m < 2; ++m)
#pragma unroll
        for (int tn = 0; tn < 2; ++tn) {
          u16* dst = vb + (16 * m + 4 * g) * KS + 32 * w + 16 * tn + r16;
          unsigned p01 = pk2(acc[m][tn][0], acc[m][tn][1]), p23 = pk2(acc[m][tn][2], acc[m][tn][3]);
          dst[0] = (u16)p01; dst[KS] = (u16)(p01 >> 16); dst[2 * KS] = (u16)p23; dst[3 * KS] = (u16)(p23 >> 16);
        }
      WAIT_VM0();
      LDS_BARRIER();
      if (tid == 0 && t > 1) st32((unsigned)(t - 1), rs, (CW_UXF + 16 * h) * 4);
      bf16x8 va[2][4];
#pragma unroll
      for (int m = 0; m < 2; ++m)
#pragma unroll
        for (int ks = 0; ks < 4; ++ks) va[m][ks] = *(const bf16x8*)(vb + (16 * m + r16) * KS + 32 * ks + 8 * g);
#pragma unroll
      for (int t2 = 0; t2 < 10; ++t2) {
        f32x4 c0 = Z4, c1 = Z4;
#pragma unroll
        for (int ks = 0; ks < 4; ++ks) { c0 = mf16(va[0][ks], Wo[t2][ks], c0); c1 = mf16(va[1][ks], Wo[t2][ks], c1); }
        int col = 160 * w + 16 * t2 + r16;
        u32x4 o = {pk2(c0[0], c0[1]), pk2(c0[2], c0[3]), pk2(c1[0], c1[1]), pk2(c1[2], c1[3])};
        st128(o, rs, (int)OFF_UX + ((t * 80 + (col >> 3)) * 32 + h) * 512 + (col & 7) * 64 + 16 * g);
      }
      if (t == 0 || t == NSEG - 1) {
        WAIT_VM0();
        LDS_BARRIER();
        if (tid == 0) st32((unsigned)t, rs, (CW_UXF + 16 * h) * 4);
      }
    }
  } else if (b < 232) {
    const int f = b - 200;
    u8* slab0 = smem;
    const unsigned ldsb = __builtin_amdgcn_readfirstlane((unsigned)(size_t)(LAS u8*)smem);
    u8* wdb = smem + 65536;
    u16* hb = (u16*)(smem + 65536 + 81920);
    bf16x8 A[20];
#pragma unroll
    for (int ks = 0; ks < 20; ++ks)
      A[ks] = *(const bf16x8*)(p.x + (size_t)(64 * f + 16 * w + r16) * 640 + 32 * ks + 8 * g);
#pragma unroll
    for (int ks = 0; ks < 16; ++ks) PIN_A(A[ks]);
    f32x4 acc2[4][10];
#pragma unroll
    for (int m = 0; m < 4; ++m)
#pragma unroll
      for (int n = 0; n < 10; ++n) acc2[m][n] = Z4;
    const int i16 = lane & 15;
    unsigned so[2], tb[4], wo2[2], tw;
#pragma unroll
    for (int q = 0; q < 2; ++q) {
      int row = 8 * (2 * w + q) + (lane >> 3);
      so[q] = (unsigned)(row * (DF * 2) + (((lane & 7) ^ swz<0>(row)) << 4));
      wo2[q] = (unsigned)((lane >> 4) * 1280 + (((lane & 15) ^ (2 * ((lane >> 4) | (q << 2)))) << 4));
    }
#pragma unroll
    for (int q = 0; q < 4; ++q) {
      int s0 = swz<0>(8 * g + (i16 >> 2));
      tb[q] = ldsb + (unsigned)((8 * g + (i16 >> 2)) * 128 + (((2 * q + ((i16 & 3) >> 1)) ^ s0) << 4) + (i16 & 1) * 8);
    }
    const int s1 = 2 * ((i16 >> 2) | ((g & 1) << 2));
    tw = ldsb + 65536 + (unsigned)((8 * g + (i16 >> 2)) * 256 + ((i16 & 3) >> 1) * 16 + (i16 & 1) * 8);
#define FFN_SLAB(c_, si_, slot_) do { \
      const u8* sg_ = (const u8*)(Wg_ + (size_t)(64 * (si_)) * DF + 64 * (c_)); \
      const u8* su_ = (const u8*)(Wu_ + (size_t)(64 * (si_)) * DF + 64 * (c_)); \
      unsigned bb_ = ldsb + (slot_) * 16384 + 2 * wq * 1024; \
      glds16_s(sg_, so[0], bb_); glds16_s(sg_, so[1], bb_ + 1024); \
      glds16_s(su_, so[0], bb_ + 8192); glds16_s(su_, so[1], bb_ + 8192 + 1024); } while (0)
#define FFN_WD(si_) ((si_) < 0 ? 0 : (si_) < 6 ? 3 : (si_) == 6 ? 2 : 0)
    WAIT_VM0();
    const u16* Wg_ = p.Wg; const u16* Wu_ = p.Wu; int wq = w;
    const int c0 = f % 27;
    FFN_SLAB(c0, 0, 0); FFN_SLAB(c0, 1, 1); FFN_SLAB(c0, 2, 2);
#pragma unroll 1
    for (int ci = 0; ci < 27; ++ci) {
      const int c = c0 + ci < 27 ? c0 + ci : c0 + ci - 27;
      const int cn = c + 1 < 27 ? c + 1 : 0;
      const int slotb = (ci * 10) & 3;
      const u8* sd = (const u8*)(p.Wd + (size_t)(64 * c) * 640);
      f32x4 ag[4], au[4];
#pragma unroll
      for (int si = 0; si < 10; ++si) {
        asm volatile("s_waitcnt vmcnt(%0)" ::"n"(FFN_WD(si - 3) + 4 + FFN_WD(si - 2) + 4 + FFN_WD(si - 1)) : "memory");
        LDS_BARRIER();
        asm volatile("" : "+s"(Wg_), "+s"(Wu_), "+s"(wq));
        if (si + 3 < 10) FFN_SLAB(c, si + 3, (slotb + si + 3) & 3);
        else FFN_SLAB(cn, si + 3 - 10, (slotb + si + 3) & 3);
#pragma unroll
        for (int q = 0; q < FFN_WD(si); ++q) {
          const int P = 20 * wq + 3 * si + q;
          glds16_s(sd + (P & 15) * 4 * 1280 + (P >> 4) * 256, (P & 2) ? wo2[1] : wo2[0], ldsb + 65536 + P * 1024);
        }
        const unsigned sof = ((slotb + si) & 3) * 16384;
#pragma unroll
        for (int k2 = 0; k2 < 2; ++k2) {
          const int ks = si * 2 + k2;
          bf16x8 bg[4], bu[4];
#pragma unroll
          for (int ft = 0; ft < 4; ++ft) {
            const unsigned t0 = tb[ft] + sof + k2 * 4096;
            bg[ft] = trfrag(t0, t0 + 512);
            bu[ft] = trfrag(t0 + 8192, t0 + 8192 + 512);
          }
#pragma unroll
          for (int ft = 0; ft < 4; ++ft) {
            ag[ft] = mf16(A[ks], bg[ft], ks == 0 ? Z4 : ag[ft]);
            au[ft] = mf16(A[ks], bu[ft], ks == 0 ? Z4 : au[ft]);
          }
        }
      }
#pragma unroll
      for (int ft = 0; ft < 4; ++ft)
#pragma unroll
        for (int j = 0; j < 4; ++j) {
          float gv = rbf(ag[ft][j]), uv = rbf(au[ft][j]);
          hb[(16 * w + 4 * g + j) * 72 + 16 * ft + r16] = f2bf(rbf(gelu_tanh(gv)) * uv);
        }
      asm volatile("s_waitcnt vmcnt(12)" ::: "memory");
      LDS_BARRIER();
#pragma unroll
      for (int k2 = 0; k2 < 2; ++k2) {
        bf16x8 ha[4];
#pragma unroll
        for (int m = 0; m < 4; ++m) ha[m] = *(const bf16x8*)(hb + (16 * m + r16) * 72 + 32 * k2 + 8 * g);
#pragma unroll
        for (int n = 0; n < 10; ++n) {
          const int nc = 160 * wq + 16 * n;
          const unsigned ta = tw + (nc >> 7) * 16384 + ((((nc & 127) >> 3) ^ s1) << 4) + k2 * 8192;
          bf16x8 bd = trfrag(ta, ta + 1024);
#pragma unroll
          for (int m = 0; m < 4; ++m) acc2[m][n] = mf16(ha[m], bd, acc2[m][n]);
        }
      }
      LDS_BARRIER();
    }
    WAIT_VM0();
    LDS_BARRIER();
    {
      u16* ob = (u16*)smem;
#pragma unroll
      for (int m = 0; m < 4; ++m)
#pragma unroll
        for (int n = 0; n < 10; ++n)
#pragma unroll
          for (int j = 0; j < 4; ++j) ob[(16 * m + 4 * g + j) * XS + 160 * w + 16 * n + r16] = f2bf(acc2[m][n][j]);
      LDS_BARRIER();
      for (int pc = tid; pc < 64 * 80; pc += 256) {
        int row = pc / 80, cc = pc % 80;
        u32x4 o = *(const u32x4*)(ob + row * XS + cc * 8);
        st128(o, rs, (int)OFF_BO + (64 * f + row) * 1280 + cc * 16);
      }
      WAIT_VM0();
      LDS_BARRIER();
      unsigned* smx = (unsigned*)(smem + 90112);
      if (tid == 0) {
        st32(0u, rs, (CW_FF + 16 * f) * 4);
        *smx = atomicAnd(ctl + CW_MIX + 16 * f, 0x7FFFFFFFu);
      }
      LDS_BARRIER();
      const unsigned done = ~(*smx) & 0xFFFFFu;
      if (done) {
        const int row = tid >> 3, c8 = tid & 7;
        float omg[2];
#pragma unroll
        for (int hs = 0; hs < 2; ++hs) omg[hs] = rbf(1.0f - gate8(p.x + (size_t)(64 * f + 32 * hs + row) * 640, p.gw, c8));
        for (int k = 0; k < 20; ++k) {
          if (!((done >> k) & 1)) continue;
          const int hs = k / NCH, cj = k % NCH;
          const int tr = 32 * hs + row, cc = cj * 64 + c8 * 8;
          u32x4 ga = ld128(rs, (int)OFF_GA + ((64 * f + tr) * 640 + cc) * 2);
          u32x4 bv = *(const u32x4*)(ob + tr * XS + cc);
          const float om = hs ? omg[1] : omg[0];
          u32x4 o;
#pragma unroll
          for (int e = 0; e < 4; ++e)
            o[e] = pk2(blo(ga[e]) + rbf(om * blo(bv[e])), bhi(ga[e]) + rbf(om * bhi(bv[e])));
          *(u32x4*)(p.out + (size_t)(64 * f + tr) * 640 + cc) = o;
        }
      }
    }
  }

  if (b >= 104 && !(b >= 136 && b < 168)) {
    int tid = threadIdx.x;
    asm volatile("" : "+v"(tid));
    const int lane = tid & 63, r16 = lane & 15, g = lane >> 4;
    float* sg = (float*)smem;
    int* stask = (int*)(smem + 256);
    for (;;) {
      LDS_BARRIER();
      if (tid == 0) *stask = (int)atomicAdd(ctl + CW_QUEUE, 1u) + 1;
      LDS_BARRIER();
      const int task = *stask;
      if (task >= NTASK) break;
      const int t = task / NCH, cj = task % NCH;
      STAMP(0);
      {
        float gv = gate8(p.x + (size_t)(t * 32 + (tid >> 3)) * 640, p.gw, tid & 7);
        if ((tid & 7) == 0) sg[tid >> 3] = gv;
      }
      if (w == 0) wait_flag<OUT_SLP>(rs, (lane < 32 ? CW_PXF : CW_UXF) + 16 * (lane & 31), (unsigned)t, ctl, lane);
      LDS_BARRIER();
      STAMP(1);
      const int col = cj * 64 + w * 16 + r16;
      const int uxo = (int)OFF_UX + (t * 80 + (col >> 3)) * 32 * 512 + (col & 7) * 64 + 16 * g;
      const int umo = (int)OFF_UM + t * STEPB + (col >> 3) * 4096 + (col & 7) * 16;
      u8* pxl = smem + 1024;
      {
        const u8* pxg = ws + OFF_PX + (size_t)t * 32 * 2560;
#pragma unroll
        for (int q = 0; q < 20; ++q)
          __builtin_amdgcn_global_load_lds((const unsigned*)(pxg + (20 * w + q) * 1024 + lane * 16), (unsigned*)(pxl + (20 * w + q) * 1024), 16, 0, AUX_SC1);
      }
      u32x4 bx[32];
#pragma unroll
      for (int h = 0; h < 32; ++h) bx[h] = ld128(rs, uxo + h * 512);
      u32x4 um[8];
      {
        unsigned spins = 0;
        for (;;) {
          asm volatile("" ::: "memory");
          unsigned mxv = 0;
#pragma unroll
          for (int hq = 0; hq < 8; ++hq) um[hq] = ld128(rs, umo + (4 * hq + g) * 128);
#pragma unroll
          for (int hq = 0; hq < 8; ++hq) mxv = umax3(mxv, um[hq].x, um[hq].z);
          if (__all(mxv != 0xFFFFFFFFu) || spins > SPIN_CAP) break;
          __builtin_amdgcn_s_sleep(4);
          ++spins;
        }
      }
      WAIT_VM0();
      LDS_BARRIER();
      f32x4 acc0 = Z4, acc1 = Z4;
#pragma unroll
      for (int h = 0; h < 32; ++h) {
        bf16x8 a0 = *(const bf16x8*)(pxl + h * 2560 + 512 + g * 512 + r16 * 16);
        bf16x8 a1 = *(const bf16x8*)(pxl + h * 2560 + 512 + g * 512 + 256 + r16 * 16);
        acc0 = mf16(a0, __builtin_bit_cast(bf16x8, bx[h]), acc0);
        acc1 = mf16(a1, __builtin_bit_cast(bf16x8, bx[h]), acc1);
      }
#pragma unroll
      for (int hq = 0; hq < 8; ++hq) {
        bf16x8 a0 = *(const bf16x8*)(pxl + (4 * hq + g) * 2560 + r16 * 16);
        bf16x8 a1 = *(const bf16x8*)(pxl + (4 * hq + g) * 2560 + 256 + r16 * 16);
        acc0 = mf16(a0, __builtin_bit_cast(bf16x8, um[hq]), acc0);
        acc1 = mf16(a1, __builtin_bit_cast(bf16x8, um[hq]), acc1);
      }
      STAMP(2);
      u16* rt = (u16*)(smem + 512 + 83968);
#pragma unroll
      for (int i = 0; i < 2; ++i)
#pragma unroll
        for (int q = 0; q < 4; ++q) {
          int tok = 16 * i + 4 * g + q;
          rt[tok * 72 + w * 16 + r16] = f2bf(sg[tok] * rbf(i == 0 ? acc0[q] : acc1[q]));
        }
      if (tid == 0) stask[1] = (int)ld32(rs, (CW_FF + 16 * (t >> 1)) * 4);
      LDS_BARRIER();
      {
        const int row = tid >> 3, c8 = tid & 7;
        const int go = ((t * 32 + row) * 640 + cj * 64 + c8 * 8) * 2;
        u32x4 ga = *(const u32x4*)(rt + row * 72 + c8 * 8);
        bool mix = stask[1] >= 0;
        if (!mix) {
          st128(ga, rs, (int)OFF_GA + go);
          WAIT_VM0();
          LDS_BARRIER();
          if (tid == 0) stask[2] = (int)atomicAnd(ctl + CW_MIX + 16 * (t >> 1), ~(1u << ((t & 1) * NCH + cj)));
          LDS_BARRIER();
          mix = stask[2] >= 0;
        }
        if (mix) {
          u32x4 bv = ld128(rs, (int)OFF_BO + go);
          const float om = rbf(1.0f - sg[row]);
          u32x4 o;
#pragma unroll
          for (int e = 0; e < 4; ++e)
            o[e] = pk2(blo(ga[e]) + rbf(om * blo(bv[e])), bhi(ga[e]) + rbf(om * bhi(bv[e])));
          *(u32x4*)((u8*)p.out + go) = o;
        }
      }
    }
  }
}

extern "C" void kernel_launch(void* const* d_in, const int* in_sizes, int n_in, void* d_out, int out_size, void* d_ws,
                              size_t ws_size, hipStream_t stream) {
  u8* ws = (u8*)d_ws;
  Params p;
  p.x = (const u16*)d_in[0]; p.mem0 = (const u16*)d_in[1]; p.Wq = (const u16*)d_in[2]; p.Wk = (const u16*)d_in[3];
  p.Wv = (const u16*)d_in[4]; p.Wo = (const u16*)d_in[5];
  p.Wg = (const u16*)d_in[8]; p.Wu = (const u16*)d_in[9]; p.Wd = (const u16*)d_in[10]; p.gw = (const u16*)d_in[11];
  p.ws = ws; p.out = (u16*)d_out;

  (void)hipFuncSetAttribute((const void*)k_main, hipFuncAttributeMaxDynamicSharedMemorySize, 163840);
  (void)hipMemsetAsync(ws, 0xFF, INIT_BYTES, stream);
  k_main<<<256, 256, 163840, stream>>>(p);
}
```

```cpp
#include <hip/hip_runtime.h>
#include <hip/hip_bf16.h>
#include <stdint.h>

typedef __bf16 bf16x8 __attribute__((ext_vector_type(8)));
typedef __bf16 bf16x16 __attribute__((ext_vector_type(16)));
typedef __bf16 bf16x2 __attribute__((ext_vector_type(2)));
typedef float f32x2 __attribute__((ext_vector_type(2)));
typedef float f32x4 __attribute__((ext_vector_type(4)));
typedef unsigned u32x4 __attribute__((ext_vector_type(4)));
typedef unsigned u32x2 __attribute__((ext_vector_type(2)));
typedef unsigned u32x8 __attribute__((ext_vector_type(8)));
typedef short s16x4 __attribute__((ext_vector_type(4)));
typedef unsigned short u16;
typedef unsigned char u8;

constexpr int SEQ = 2048, DM = 640, NSEG = 64, DF = 1728;
constexpr int NSL = 80;

constexpr size_t OFF_CTL = 0;
constexpr size_t CTL_BYTES = 16384;
constexpr int STEPB = 10240 + 20480 + 65536 + 327680;
constexpr size_t OFF_MEM = CTL_BYTES;
constexpr size_t OFF_P = OFF_MEM + 10240;
constexpr size_t OFF_KM = OFF_P + 20480;
constexpr size_t OFF_UM = OFF_KM + 65536;
constexpr size_t OFF_SENT_END = CTL_BYTES + (size_t)66 * STEPB;
constexpr size_t INIT_BYTES = CTL_BYTES + (size_t)3 * STEPB;
constexpr size_t OFF_KX = OFF_SENT_END;
constexpr size_t OFF_UX = OFF_KX + (size_t)SEQ * 4096 * 2;
constexpr size_t OFF_PX = OFF_UX + (size_t)64 * 80 * 32 * 8 * 32 * 2;
constexpr size_t OFF_BO = OFF_PX + (size_t)64 * 32 * 2560;
constexpr size_t OFF_GA = OFF_BO + (size_t)SEQ * DM * 2;
constexpr size_t OFF_END = OFF_GA + (size_t)SEQ * DM * 2;
constexpr int CW_TMO = 0, CW_QUEUE = 32, CW_KXF = 64, CW_UXF = 1024, CW_PXF = 2048, CW_FF = 3072, CW_MIX = 3584;

#define AUX_SC1 16
#define RSRC_W3 0x00020000
#define OOB 0x7FFF0000

__device__ __forceinline__ float bf2f(u16 v) { return __uint_as_float(((unsigned)v) << 16); }
__device__ __forceinline__ u16 f2bf(float f) { __bf16 b = (__bf16)f; return __builtin_bit_cast(u16, b); }
__device__ __forceinline__ float rbf(float f) { return (float)((__bf16)f); }
__device__ __forceinline__ unsigned pk2(float a, float b) {
  f32x2 v = {a, b};
  return __builtin_bit_cast(unsigned, __builtin_convertvector(v, bf16x2));
}
__device__ __forceinline__ unsigned nosent(unsigned x) { return x < 0xFFFFFFFEu ? x : 0xFFFFFFFEu; }
__device__ __forceinline__ f32x4 smf(bf16x8 a, bf16x16 b, f32x4 c, int idx) {
  return __builtin_amdgcn_smfmac_f32_16x16x64_bf16(a, b, c, idx, 0, 0);
}
__device__ __forceinline__ f32x4 mf16(bf16x8 a, bf16x8 b, f32x4 c) {
  return __builtin_amdgcn_mfma_f32_16x16x32_bf16(a, b, c, 0, 0, 0);
}
__device__ __forceinline__ float comb(float a, float b) {
  auto r = __builtin_amdgcn_permlane32_swap(__float_as_uint(a), __float_as_uint(b), false, false);
  return __uint_as_float(r[0]) + __uint_as_float(r[1]);
}
__device__ __forceinline__ u32x4 ld128(__amdgpu_buffer_rsrc_t rs, int off) {
  return __builtin_amdgcn_raw_buffer_load_b128(rs, off, 0, AUX_SC1);
}
__device__ __forceinline__ unsigned ld32(__amdgpu_buffer_rsrc_t rs, int off) {
  return __builtin_amdgcn_raw_buffer_load_b32(rs, off, 0, AUX_SC1);
}
__device__ __forceinline__ void st128(u32x4 v, __amdgpu_buffer_rsrc_t rs, int off) {
  __builtin_amdgcn_raw_buffer_store_b128(v, rs, off, 0, AUX_SC1);
}
__device__ __forceinline__ void st64(u32x2 v, __amdgpu_buffer_rsrc_t rs, int off) {
  __builtin_amdgcn_raw_buffer_store_b64(v, rs, off, 0, AUX_SC1);
}
__device__ __forceinline__ void st32(unsigned v, __amdgpu_buffer_rsrc_t rs, int off) {
  __builtin_amdgcn_raw_buffer_store_b32(v, rs, off, 0, AUX_SC1);
}

constexpr unsigned SPIN_CAP = 1u << 16;
#define STAMP(slot) do { } while (0)
#define STAMP_FLUSH() do { } while (0)
#define LDS_BARRIER() do { asm volatile("s_waitcnt lgkmcnt(0)" ::: "memory"); __builtin_amdgcn_s_barrier(); asm volatile("" ::: "memory"); } while (0)
#define WAIT_VM0() asm volatile("s_waitcnt vmcnt(0)" ::: "memory")
#define WAIT_LGKM0() asm volatile("s_waitcnt lgkmcnt(0)" ::: "memory")
#define PIN_A(x) asm volatile("" : "+a"(x))
#define PIN_V(x) asm volatile("" : "+v"(x))
#define LAS __attribute__((address_space(3)))

__device__ __forceinline__ float rmax3(float a, float b, float c) {
  float r; asm("v_max3_f32 %0, %1, %2, %3\n\ts_nop 1" : "=v"(r) : "v"(a), "v"(b), "v"(c)); return r;
}
__device__ __forceinline__ float rmax2(float a, float b) {
  float r; asm("v_max_f32 %0, %1, %2\n\ts_nop 1" : "=v"(r) : "v"(a), "v"(b)); return r;
}
__device__ __forceinline__ float xmax8(float v) {
  int o = __builtin_amdgcn_update_dpp(0, __float_as_int(v), 0x128, 0xf, 0xf, false);
  return rmax2(v, __int_as_float(o));
}
__device__ __forceinline__ float xadd8(float v) {
  int o = __builtin_amdgcn_update_dpp(0, __float_as_int(v), 0x128, 0xf, 0xf, false);
  return v + __int_as_float(o);
}
__device__ __forceinline__ float xmax16(float v) {
  auto r = __builtin_amdgcn_permlane16_swap(__float_as_uint(v), __float_as_uint(v), false, false);
  return rmax2(__uint_as_float(r[0]), __uint_as_float(r[1]));
}
__device__ __forceinline__ float xadd16(float v) {
  auto r = __builtin_amdgcn_permlane16_swap(__float_as_uint(v), __float_as_uint(v), false, false);
  return __uint_as_float(r[0]) + __uint_as_float(r[1]);
}
__device__ __forceinline__ float xmax32(float v) {
  auto r = __builtin_amdgcn_permlane32_swap(__float_as_uint(v), __float_as_uint(v), false, false);
  return rmax2(__uint_as_float(r[0]), __uint_as_float(r[1]));
}
__device__ __forceinline__ float xadd32(float v) {
  auto r = __builtin_amdgcn_permlane32_swap(__float_as_uint(v), __float_as_uint(v), false, false);
  return __uint_as_float(r[0]) + __uint_as_float(r[1]);
}
__device__ __forceinline__ unsigned umax3(unsigned a, unsigned b, unsigned c) { return max(max(a, b), c); }
__device__ __forceinline__ float blo(unsigned p) { return __uint_as_float(p << 16); }
__device__ __forceinline__ float bhi(unsigned p) { return __uint_as_float(p & 0xFFFF0000u); }

__device__ __forceinline__ void glds16_asm(const void* gsrc, unsigned lds_dst) {
  unsigned keep;
  asm volatile("s_mov_b32 %0, m0\n\ts_mov_b32 m0, %2\n\ts_nop 0\n\tglobal_load_lds_dwordx4 %1, off\n\ts_mov_b32 m0, %0"
               : "=&s"(keep) : "v"(gsrc), "s"(lds_dst) : "memory");
}
template <int R, int CB>
__device__ __forceinline__ void dma_tile(const u8* src, int ldb, unsigned lds, int lane) {
  constexpr int CPR = CB / 16;
  constexpr int N = R * CPR / 64;
#pragma unroll
  for (int q = 0; q < N; ++q) {
    int idx = q * 64 + lane;
    int row = idx / CPR, ch = idx % CPR;
    glds16_asm(src + (size_t)row * ldb + ch * 16, lds + q * 1024);
  }
}
#define WAIT_VM(n) asm volatile("s_waitcnt vmcnt(%0)" ::"n"(n) : "memory")
__device__ __forceinline__ u32x2 tr4(const u8* tile, int rsb, int k0, int n0, int lane) {
  int i = lane & 15;
  const u8* p = tile + (k0 + (i >> 2)) * rsb + (n0 + 4 * (i & 3)) * 2;
  s16x4 v = __builtin_amdgcn_ds_read_tr16_b64_v4i16((LAS s16x4*)p);
  return __builtin_bit_cast(u32x2, v);
}
__device__ __forceinline__ bf16x16 frag_sparse(const u8* blk, int rsb, int n0, int lane) {
  int gb = lane >> 4;
  int kb = 16 * (gb >> 1) + 4 * (gb & 1);
  u32x2 t00 = tr4(blk, rsb, kb, n0, lane), t01 = tr4(blk, rsb, kb + 8, n0, lane);
  u32x2 t10 = tr4(blk, rsb, kb + 32, n0, lane), t11 = tr4(blk, rsb, kb + 40, n0, lane);
  u32x8 d = {t00.x, t01.x, t00.y, t01.y, t10.x, t11.x, t10.y, t11.y};
  return __builtin_bit_cast(bf16x16, d);
}
__device__ __forceinline__ bf16x8 frag_dense(const u8* blk, int rsb, int k0, int n0, int lane) {
  int g = lane >> 4;
  u32x2 a = tr4(blk, rsb, k0 + 8 * g, n0, lane), b = tr4(blk, rsb, k0 + 8 * g + 4, n0, lane);
  u32x4 d = {a.x, a.y, b.x, b.y};
  return __builtin_bit_cast(bf16x8, d);
}

template <int MODE> __device__ __forceinline__ int swz(int row) {
  return MODE == 0 ? 2 * (((row >> 1) & 1) | (((row >> 3) & 1) << 1)) : 2 * ((row & 3) | (((row >> 3) & 1) << 2));
}
__device__ __forceinline__ void glds16_s(const void* sbase, unsigned voff, unsigned lds_dst) {
  unsigned keep;
  asm volatile("s_mov_b32 %0, m0\n\ts_mov_b32 m0, %3\n\ts_nop 0\n\tglobal_load_lds_dwordx4 %1, %2\n\ts_mov_b32 m0, %0"
               : "=&s"(keep) : "v"(voff), "s"(sbase), "s"(lds_dst) : "memory");
}
__device__ __forceinline__ u32x2 trr(unsigned addr) {
  s16x4 v = __builtin_amdgcn_ds_read_tr16_b64_v4i16((LAS s16x4*)(size_t)addr);
  return __builtin_bit_cast(u32x2, v);
}
__device__ __forceinline__ bf16x8 trfrag(unsigned a0, unsigned a1) {
  u32x2 a = trr(a0), b = trr(a1);
  u32x4 d = {a.x, a.y, b.x, b.y};
  return __builtin_bit_cast(bf16x8, d);
}
template <int MODE, int CB>
__device__ __forceinline__ void dma_swz(const u8* src, int ldb, unsigned lds, int q, int lane) {
  constexpr int CPR = CB / 16;
  int idx = q * 64 + lane;
  int row = idx / CPR, ch = (idx % CPR) ^ swz<MODE>(row);
  glds16_asm(src + (size_t)row * ldb + ch * 16, lds + q * 1024);
}
template <int MODE>
__device__ __forceinline__ u32x2 tr4s(const u8* tile, int rsb, int k0, int n0, int lane) {
  int i = lane & 15;
  int row = k0 + (i >> 2), n = n0 + 4 * (i & 3);
  const u8* p = tile + row * rsb + (((n >> 3) ^ swz<MODE>(row)) << 4) + (n & 7) * 2;
  s16x4 v = __builtin_amdgcn_ds_read_tr16_b64_v4i16((LAS s16x4*)p);
  return __builtin_bit_cast(u32x2, v);
}
template <int MODE>
__device__ __forceinline__ bf16x8 frag_dense_s(const u8* blk, int rsb, int k0, int n0, int lane) {
  int g = lane >> 4;
  u32x2 a = tr4s<MODE>(blk, rsb, k0 + 8 * g, n0, lane), b = tr4s<MODE>(blk, rsb, k0 + 8 * g + 4, n0, lane);
  u32x4 d = {a.x, a.y, b.x, b.y};
  return __builtin_bit_cast(bf16x8, d);
}
__device__ __forceinline__ float gelu_tanh(float x) {
  float u = 0.7978845608028654f * (x + 0.044715f * x * x * x);
  return x * __builtin_amdgcn_rcpf(1.0f + __builtin_amdgcn_exp2f(-2.8853900817779268f * u));
}

__device__ __forceinline__ float gate8(const u16* xrow, const u16* gw, int part) {
  float sacc = 0.f;
#pragma unroll
  for (int q = 0; q < 10; ++q) {
    u32x4 xv = *(const u32x4*)(xrow + part * 8 + q * 64), gv = *(const u32x4*)(gw + part * 8 + q * 64);
#pragma unroll
    for (int e = 0; e < 4; ++e) {
      sacc += rbf(blo(xv[e]) * blo(gv[e]));
      sacc += rbf(bhi(xv[e]) * bhi(gv[e]));
    }
  }
  sacc += __shfl_xor(sacc, 1); sacc += __shfl_xor(sacc, 2); sacc += __shfl_xor(sacc, 4);
  float z = rbf(sacc);
  return rbf(1.0f / (1.0f + __expf(-z)));
}

template <int SLP = 2>
__device__ __forceinline__ void wait_flag(__amdgpu_buffer_rsrc_t rs, int cw, unsigned v, unsigned* ctl, int lane) {
  for (unsigned spins = 0;; ++spins) {
    asm volatile("" ::: "memory");
    int f = (int)ld32(rs, cw * 4);
    if (__all(f >= (int)v)) break;
    if (spins > SPIN_CAP) { if (lane == 0) atomicAdd(ctl, 1u); break; }
    __builtin_amdgcn_s_sleep(SLP);
  }
  asm volatile("" ::: "memory");
}

#define ADAPT 7
#define AD_DEC 16
#define AD_INC 64
#define D1 3800
#define DV 4300
#define D2 4800
__device__ __forceinline__ void wait_until(unsigned long long t) {
  while (__builtin_amdgcn_s_memtime() < t) __builtin_amdgcn_s_sleep(1);
}
#define START_DLY 8000
#define OUT_SLP 16
#define SCHED() __builtin_amdgcn_sched_barrier(0)
#define PROJ_GEMM(ACC, WF) do { \
    bf16x8 A0_[4], A1_[4]; \
    _Pragma("unroll") for (int ks = 0; ks < 3; ++ks) { \
      A0_[ks] = *(const bf16x8*)(xs + r16 * XS + 32 * ks + 8 * g); A1_[ks] = *(const bf16x8*)(xs + (16 + r16) * XS + 32 * ks + 8 * g); } \
    _Pragma("unroll") for (int ks = 0; ks < 20; ++ks) { \
      if (ks + 3 < 20) { A0_[(ks + 3) & 3] = *(const bf16x8*)(xs + r16 * XS + 32 * (ks + 3) + 8 * g); \
                         A1_[(ks + 3) & 3] = *(const bf16x8*)(xs + (16 + r16) * XS + 32 * (ks + 3) + 8 * g); } \
      SCHED(); \
      _Pragma("unroll") for (int tn = 0; tn < 2; ++tn) { \
        ACC[0][tn] = mf16(A0_[ks & 3], WF[tn][ks], ks == 0 ? Z4 : ACC[0][tn]); \
        ACC[1][tn] = mf16(A1_[ks & 3], WF[tn][ks], ks == 0 ? Z4 : ACC[1][tn]); } \
      SCHED(); \
    } } while (0)

#define LDX(t_) do { _Pragma("unroll") for (int q = 0; q < 10; ++q) { int pc = q * 256 + tid; int row = pc / 80, cc = pc % 80; \
      xp[q] = *(const u32x4*)(p.x + (size_t)((t_) * 32 + row) * 640 + cc * 8); } } while (0)
#define STX() do { _Pragma("unroll") for (int q = 0; q < 10; ++q) { int pc = q * 256 + tid; int row = pc / 80, cc = pc % 80; \
      *(u32x4*)(xs + row * XS + cc * 8) = xp[q]; } } while (0)
constexpr int XS = 648;
constexpr int KS = 136;

struct Params {
  const u16* x; const u16* mem0; const u16* Wq; const u16* Wk; const u16* Wv; const u16* Wo;
  const u16* Wg; const u16* Wu; const u16* Wd; const u16* gw; u8* ws; u16* out;
};
constexpr int NCH = 10;
constexpr int NTASK = NSEG * NCH;

__global__ void __launch_bounds__(256, 1) k_main(Params p) {
  extern __shared__ __attribute__((aligned(16))) u8 smem[];
  u8* ws = p.ws;
  const int tid = threadIdx.x, w = __builtin_amdgcn_readfirstlane(tid >> 6), lane = tid & 63;
  const int r16 = lane & 15, g = lane >> 4, par = r16 >> 3, r = r16 & 7;
  const int idx = par ? 0xEEEE : 0x4444;
#define XMAP 1
  int b;
  {
    const int bx = blockIdx.x, vi = (bx >> 3) * 4 + (bx & 3);
    if (XMAP == 0) b = bx;
    else if ((bx & 4) == 0) b = vi < 104 ? vi : 232 + (vi - 104);
    else b = 104 + vi;
  }
  const __amdgpu_buffer_rsrc_t rs = __builtin_amdgcn_make_buffer_rsrc((void*)ws, 0, (int)OFF_END, RSRC_W3);
  unsigned* ctl = (unsigned*)(ws + OFF_CTL);
  const f32x4 Z4 = {0.f, 0.f, 0.f, 0.f};
  unsigned long long tanchor = 0;
  int dly = b < 32 ? D1 : b < 64 ? DV : D2;
#define AD_UPDATE() do { if (s > 0) { if (spins == 0) dly -= AD_DEC; else dly += AD_INC; if (dly < 0) dly = 0; } } while (0)
  const u32x4 PZ = {0xFFFFFFFFu, 0xFFFFFFFFu, 0xFFFFFFFFu, 0xFFFFFFFFu};
  u8* wl = smem + w * 40960;
  const unsigned wla = __builtin_amdgcn_readfirstlane((unsigned)(size_t)(LAS u8*)smem) + w * 40960;

  if (b < 32) {
    const int h = b;
    u16* sq = (u16*)smem;
    unsigned* spl = (unsigned*)(smem + 4608);
    u32x4* smem_a = (u32x4*)(smem + 8192);
    bf16x16 W[4][10];
    {
      const u8* src = (const u8*)((w < 2 ? p.Wq : p.Wk) + h * 128 + 64 * (w & 1));
#pragma unroll
      for (int i = 0; i < 5; ++i) dma_tile<64, 128>(src + (size_t)i * 64 * 8192, 8192, wla + i * 8192, lane);
#pragma unroll
      for (int i = 0; i < 10; ++i) {
        WAIT_VM(8 * (i < 5 ? 4 : 9 - i));
#pragma unroll
        for (int tn = 0; tn < 4; ++tn) W[tn][i] = frag_sparse(wl + (i % 5) * 8192, 128, 16 * tn, lane);
        if (i < 5) {
          WAIT_LGKM0();
          dma_tile<64, 128>(src + (size_t)(i + 5) * 64 * 8192, 8192, wla + i * 8192, lane);
        }
      }
      WAIT_LGKM0();
    }
#pragma unroll
    for (int tn = 0; tn < 4; ++tn)
#pragma unroll
      for (int i = 0; i < (tn < 2 ? 8 : 7); ++i) PIN_A(W[tn][i]);
    LDS_BARRIER();
    const int moff = (int)OFF_MEM + (2 * g + par) * 128 + r * 16;
    for (int s = 0; s < NSEG; ++s) {
      u32x4 kxf[2][4];
      if (w == 0 && s < NSEG - 1) {
        wait_flag(rs, CW_KXF + 16 * h, (unsigned)s, ctl, lane);
        const int kxo = (int)OFF_KX + (s * 32 + h) * 8192 + r16 * 256 + 16 * g;
#pragma unroll
        for (int t2 = 0; t2 < 2; ++t2)
#pragma unroll
          for (int kk = 0; kk < 4; ++kk) kxf[t2][kk] = ld128(rs, kxo + t2 * 4096 + 64 * kk);
      }
      f32x4 acc[4];
      unsigned spins = 0;
      if ((ADAPT & 1) && s > 0) wait_until(tanchor + dly);
      STAMP(0);
      for (;;) {
        asm volatile("" ::: "memory");
        u32x4 a0 = ld128(rs, moff + s * STEPB + w * 1024);
        u32x4 a1 = ld128(rs, moff + s * STEPB + (w + 4) * 1024);
        u32x4 a2 = ld128(rs, w < 2 ? moff + s * STEPB + (w + 8) * 1024 : OOB);
        asm volatile("" ::: "memory");
        smem_a[w * 64 + lane] = a0; smem_a[(w + 4) * 64 + lane] = a1; smem_a[(w + 8) * 64 + lane] = a2;
        unsigned mxv = umax3(umax3(a0.x, a0.z, a1.x), a1.z, umax3(a2.x, a2.z, 0u));
        if (__all(mxv != 0xFFFFFFFFu) || ++spins > SPIN_CAP) break;
      }
      LDS_BARRIER();
#pragma unroll
      for (int tn = 0; tn < 4; ++tn) { acc[tn] = Z4; PIN_V(acc[tn]); }
      {
        u32x4 af[10];
#pragma unroll
        for (int i = 0; i < 10; ++i) af[i] = smem_a[i * 64 + lane];
        SCHED();
#pragma unroll
        for (int i = 0; i < 10; ++i)
#pragma unroll
          for (int tn = 0; tn < 4; ++tn) acc[tn] = smf(__builtin_bit_cast(bf16x8, af[i]), W[tn][i], acc[tn], idx);
      }
      STAMP(1);
      u16* dst = sq + (w >> 1) * (8 * KS) + (4 * (g & 1)) * KS + 64 * (w & 1) + 16 * (lane >> 5) + r16;
#pragma unroll
      for (int pp = 0; pp < 2; ++pp) {
        float v0 = comb(acc[2 * pp][0], acc[2 * pp + 1][0]), v1 = comb(acc[2 * pp][1], acc[2 * pp + 1][1]);
        float v2 = comb(acc[2 * pp][2], acc[2 * pp + 1][2]), v3 = comb(acc[2 * pp][3], acc[2 * pp + 1][3]);
        unsigned p01 = pk2(v0, v1), p23 = pk2(v2, v3);
        dst[32 * pp] = (u16)p01; dst[32 * pp + KS] = (u16)(p01 >> 16);
        dst[32 * pp + 2 * KS] = (u16)p23; dst[32 * pp + 3 * KS] = (u16)(p23 >> 16);
      }
      LDS_BARRIER();
      STAMP(2);
      if (w == 0 && s < NSEG - 1) {
        f32x4 sc[3] = {Z4, Z4, Z4};
        bf16x8 qf[4], kf[4];
#pragma unroll
        for (int kk = 0; kk < 4; ++kk) qf[kk] = *(const bf16x8*)(sq + r * KS + 32 * kk + 8 * g);
#pragma unroll
        for (int kk = 0; kk < 4; ++kk) kf[kk] = *(const bf16x8*)(sq + 8 * KS + r * KS + 32 * kk + 8 * g);
        SCHED();
#pragma unroll
        for (int kk = 0; kk < 4; ++kk) {
          sc[1] = mf16(__builtin_bit_cast(bf16x8, kxf[0][kk]), qf[kk], sc[1]);
          sc[2] = mf16(__builtin_bit_cast(bf16x8, kxf[1][kk]), qf[kk], sc[2]);
          sc[0] = mf16(kf[kk], qf[kk], sc[0]);
        }
        const float NEG = -3.0e38f;
        float v[6];
        v[0] = par ? sc[1][2] : (g < 2 ? sc[0][0] : NEG);
        v[1] = par ? sc[1][3] : (g < 2 ? sc[0][1] : NEG);
        v[2] = par ? sc[2][0] : (g < 2 ? sc[0][2] : NEG);
        v[3] = par ? sc[2][1] : (g < 2 ? sc[0][3] : NEG);
        v[4] = par ? sc[2][2] : sc[1][0];
        v[5] = par ? sc[2][3] : sc[1][1];
        float mx = rmax3(rmax3(v[0], v[1], v[2]), rmax3(v[3], v[4], v[5]), v[0]);
        mx = xmax32(xmax16(xmax8(mx)));
        const float scale = 0.08838834764831845f;
        const float L2E = 1.4426950408889634f;
        float mr = rbf(rbf(mx) * scale) * L2E;
        float e[6];
#pragma unroll
        for (int q = 0; q < 3; ++q) {
          unsigned p1 = pk2(v[2 * q], v[2 * q + 1]);
          unsigned p2 = pk2(blo(p1) * scale, bhi(p1) * scale);
          e[2 * q] = __builtin_amdgcn_exp2f(fmaf(blo(p2), L2E, -mr));
          e[2 * q + 1] = __builtin_amdgcn_exp2f(fmaf(bhi(p2), L2E, -mr));
        }
        float sum = ((e[0] + e[1]) + (e[2] + e[3])) + (e[4] + e[5]);
        sum = xadd32(xadd16(xadd8(sum)));
        float inv = __builtin_amdgcn_rcpf(sum);
        unsigned o0 = pk2(e[0] * inv, e[1] * inv), o1 = pk2(e[2] * inv, e[3] * inv), o2 = pk2(e[4] * inv, e[5] * inv);
        unsigned* pl = spl + r * 4;
        int dA = 32 + (g >> 1) * 32 + 2 * (g & 1), dB = dA + 64;
        int d0 = par ? dA + 1 : 2 * g, d1 = par ? dB : 2 * g + 1, d2 = par ? dB + 1 : dA;
        if (par || g < 2) { pl[d0] = o0; pl[d1] = o1; }
        pl[d2] = o2;
        WAIT_LGKM0();
        if (lane < 40) {
          u32x4 o = *(const u32x4*)(spl + lane * 4);
          o.x = nosent(o.x); o.y = nosent(o.y); o.z = nosent(o.z); o.w = nosent(o.w);
          st128(o, rs, (int)OFF_P + s * STEPB + h * 640 + lane * 16);
          st128(PZ, rs, (int)OFF_P + (s + 2) * STEPB + h * 640 + lane * 16);
        }
      }
      if (w == 1) {
#pragma unroll
        for (int q = 0; q < 2; ++q) {
          int pc = q * 64 + lane, row = pc >> 4, ch = pc & 15;
          u32x4 o = *(const u32x4*)(sq + 8 * KS + row * KS + ch * 8);
          o.x = nosent(o.x); o.y = nosent(o.y); o.z = nosent(o.z); o.w = nosent(o.w);
          st128(o, rs, (int)OFF_KM + s * STEPB + h * 2048 + pc * 16);
          st128(PZ, rs, (int)OFF_KM + (s + 2) * STEPB + h * 2048 + pc * 16);
        }
      }
      STAMP(3);
      LDS_BARRIER();
      AD_UPDATE();
      tanchor = __builtin_amdgcn_s_memtime();
      STAMP_FLUSH();
    }
  } else if (b < 64) {
    const int h = b - 32;
    u16* sq = (u16*)smem;
    u32x4* smem_a = (u32x4*)(smem + 8192);
    bf16x16 Wv[2][10], Wo[10][2];
    {
      const u8* src = (const u8*)(p.Wv + h * 128 + 32 * w);
      const u8* so = (const u8*)(p.Wo + (size_t)h * 128 * 640 + 160 * w);
#pragma unroll
      for (int i = 0; i < 10; ++i) dma_tile<64, 64>(src + (size_t)i * 64 * 8192, 8192, wla + i * 4096, lane);
      WAIT_VM(20);
#pragma unroll
      for (int i = 0; i < 5; ++i)
#pragma unroll
        for (int tn = 0; tn < 2; ++tn) Wv[tn][i] = frag_sparse(wl + i * 4096, 64, 16 * tn, lane);
      WAIT_LGKM0();
      dma_tile<64, 320>(so, 1280, wla, lane);
      WAIT_VM(20);
#pragma unroll
      for (int i = 5; i < 10; ++i)
#pragma unroll
        for (int tn = 0; tn < 2; ++tn) Wv[tn][i] = frag_sparse(wl + i * 4096, 64, 16 * tn, lane);
      WAIT_LGKM0();
      dma_tile<64, 320>(so + (size_t)64 * 1280, 1280, wla + 20480, lane);
      WAIT_VM(20);
#pragma unroll
      for (int t2 = 0; t2 < 10; ++t2) Wo[t2][0] = frag_sparse(wl, 320, 16 * t2, lane);
      WAIT_VM(0);
#pragma unroll
      for (int t2 = 0; t2 < 10; ++t2) Wo[t2][1] = frag_sparse(wl + 20480, 320, 16 * t2, lane);
      WAIT_LGKM0();
    }
#pragma unroll
    for (int tn = 0; tn < 2; ++tn)
#pragma unroll
      for (int i = 0; i < 6; ++i) PIN_A(Wv[tn][i]);
#pragma unroll
    for (int t2 = 0; t2 < 10; ++t2)
#pragma unroll
      for (int i = 0; i < 2; ++i) PIN_A(Wo[t2][i]);
    LDS_BARRIER();
    const int moff = (int)OFF_MEM + (2 * g + par) * 128 + r * 16;
    const int ucol = 160 * w + 16 * (lane >> 5) + r16;
    const int uoff = (int)OFF_UM + h * 128 + (ucol >> 3) * 4096 + (ucol & 7) * 16 + 8 * (g & 1);
    for (int s = 0; s < NSEG; ++s) {
      f32x4 acc[2];
      unsigned spins = 0;
      if ((ADAPT & 2) && s > 0) wait_until(tanchor + dly);
      STAMP(0);
      for (;;) {
        asm volatile("" ::: "memory");
        u32x4 a0 = ld128(rs, moff + s * STEPB + w * 1024);
        u32x4 a1 = ld128(rs, moff + s * STEPB + (w + 4) * 1024);
        u32x4 a2 = ld128(rs, w < 2 ? moff + s * STEPB + (w + 8) * 1024 : OOB);
        asm volatile("" ::: "memory");
        smem_a[w * 64 + lane] = a0; smem_a[(w + 4) * 64 + lane] = a1; smem_a[(w + 8) * 64 + lane] = a2;
        unsigned mxv = umax3(umax3(a0.x, a0.z, a1.x), a1.z, umax3(a2.x, a2.z, 0u));
        if (__all(mxv != 0xFFFFFFFFu) || ++spins > SPIN_CAP) break;
      }
      LDS_BARRIER();
      acc[0] = Z4; acc[1] = Z4; PIN_V(acc[0]); PIN_V(acc[1]);
      {
        u32x4 af[10];
#pragma unroll
        for (int i = 0; i < 10; ++i) af[i] = smem_a[i * 64 + lane];
        SCHED();
#pragma unroll
        for (int i = 0; i < 10; ++i) {
          acc[0] = smf(__builtin_bit_cast(bf16x8, af[i]), Wv[0][i], acc[0], idx);
          acc[1] = smf(__builtin_bit_cast(bf16x8, af[i]), Wv[1][i], acc[1], idx);
        }
      }
      STAMP(1);
      u16* vs = sq + (s & 1) * (8 * KS);
      {
        u16* dst = vs + (4 * (g & 1)) * KS + 32 * w + 16 * (lane >> 5) + r16;
        float v0 = comb(acc[0][0], acc[1][0]), v1 = comb(acc[0][1], acc[1][1]);
        float v2 = comb(acc[0][2], acc[1][2]), v3 = comb(acc[0][3], acc[1][3]);
        unsigned p01 = pk2(v0, v1), p23 = pk2(v2, v3);
        dst[0] = (u16)p01; dst[KS] = (u16)(p01 >> 16); dst[2 * KS] = (u16)p23; dst[3 * KS] = (u16)(p23 >> 16);
      }
      f32x4 c[10];
#pragma unroll
      for (int t2 = 0; t2 < 10; ++t2) { c[t2] = Z4; PIN_V(c[t2]); }
      LDS_BARRIER();
      STAMP(2);
      bf16x8 a0 = *(const bf16x8*)(vs + r * KS + 8 * (2 * g + par));
      bf16x8 a1 = *(const bf16x8*)(vs + r * KS + 64 + 8 * (2 * g + par));
#pragma unroll
      for (int t2 = 0; t2 < 10; ++t2) c[t2] = smf(a0, Wo[t2][0], c[t2], idx);
#pragma unroll
      for (int t2 = 0; t2 < 10; ++t2) c[t2] = smf(a1, Wo[t2][1], c[t2], idx);
#pragma unroll
      for (int pp = 0; pp < 5; ++pp) {
        float v0 = comb(c[2 * pp][0], c[2 * pp + 1][0]), v1 = comb(c[2 * pp][1], c[2 * pp + 1][1]);
        float v2 = comb(c[2 * pp][2], c[2 * pp + 1][2]), v3 = comb(c[2 * pp][3], c[2 * pp + 1][3]);
        u32x2 o = {nosent(pk2(v0, v1)), nosent(pk2(v2, v3))};
        st64(o, rs, uoff + s * STEPB + pp * 4 * 4096);
      }
#pragma unroll
      for (int pp = 0; pp < 5; ++pp) st64(u32x2{0xFFFFFFFFu, 0xFFFFFFFFu}, rs, uoff + (s + 2) * STEPB + pp * 4 * 4096);
      STAMP(3);
      AD_UPDATE();
      tanchor = __builtin_amdgcn_s_memtime();
      STAMP_FLUSH();
    }
  } else if (b < 104) {
    const int c = b - 64;
    float* sred = (float*)smem;
    if (tid < 32) {
      int sh = tid >> 4, row = (tid >> 1) & 7, cp = tid & 1;
      u32x2 v = *(const u32x2*)(p.mem0 + row * 640 + c * 16 + sh * 8 + cp * 4);
      v.x = nosent(v.x); v.y = nosent(v.y);
      st64(v, rs, (int)OFF_MEM + c * 256 + tid * 8);
    }
    const int sl = 2 * c + par;
    for (int s = 0; s < NSEG - 1; ++s) {
      wait_flag(rs, CW_UXF + 16 * (8 * w + (lane & 7)), (unsigned)s, ctl, lane);
      const int uxo = (int)OFF_UX + ((s * 80 + sl) * 32 + 8 * w) * 512 + r * 64 + 16 * g;
      u32x4 bx[8];
#pragma unroll
      for (int i = 0; i < 8; ++i) bx[i] = ld128(rs, uxo + i * 512);
      const int pb = (r16 < 8) ? (int)OFF_P + s * STEPB + (8 * w) * 640 + r * 16 : OOB;
      const int ub = (int)OFF_UM + s * STEPB + sl * 4096 + (8 * w) * 128 + r * 16;
      f32x4 acc0, acc1;
      unsigned spins = 0;
      if ((ADAPT & 4) && s > 0) wait_until(tanchor + dly);
      STAMP(0);
      for (;;) {
        asm volatile("" ::: "memory");
        u32x4 pa[10], um[2];
#pragma unroll
        for (int i = 0; i < 2; ++i) um[i] = ld128(rs, ub + (4 * i + g) * 128);
#pragma unroll
        for (int i = 0; i < 2; ++i) pa[8 + i] = ld128(rs, pb + (4 * i + g) * 640);
#pragma unroll
        for (int i = 0; i < 8; ++i) pa[i] = ld128(rs, pb + i * 640 + 128 + 128 * g);
        asm volatile("" ::: "memory");
        unsigned mxv = umax3(um[0].x, um[0].z, 0u);
        mxv = umax3(mxv, um[1].x, um[1].z);
#pragma unroll
        for (int i = 0; i < 10; ++i) mxv = umax3(mxv, pa[i].x, pa[i].z);
        if (__all(mxv != 0xFFFFFFFFu) || spins > SPIN_CAP) {
          acc0 = mf16(__builtin_bit_cast(bf16x8, pa[8]), __builtin_bit_cast(bf16x8, um[0]), Z4);
          acc1 = mf16(__builtin_bit_cast(bf16x8, pa[9]), __builtin_bit_cast(bf16x8, um[1]), Z4);
#pragma unroll
          for (int i = 0; i < 8; i += 2) {
            acc0 = mf16(__builtin_bit_cast(bf16x8, pa[i]), __builtin_bit_cast(bf16x8, bx[i]), acc0);
            acc1 = mf16(__builtin_bit_cast(bf16x8, pa[i + 1]), __builtin_bit_cast(bf16x8, bx[i + 1]), acc1);
          }
          break;
        }
        ++spins;
      }
      STAMP(1);
      if (g < 2) {
#pragma unroll
        for (int j = 0; j < 4; ++j) sred[w * 128 + par * 64 + (4 * g + j) * 8 + r] = acc0[j] + acc1[j];
      }
      LDS_BARRIER();
      STAMP(2);
      if (w == 0) {
        const f32x4* sr = (const f32x4*)sred;
        if (lane < 32) {
          f32x4 a = sr[lane], b2 = sr[32 + lane], c2 = sr[64 + lane], d2 = sr[96 + lane];
          f32x4 t = (a + b2) + (c2 + d2);
          u32x2 o = {nosent(pk2(t[0], t[1])), nosent(pk2(t[2], t[3]))};
          st64(o, rs, (int)OFF_MEM + (s + 1) * STEPB + c * 256 + lane * 8);
          st64(u32x2{0xFFFFFFFFu, 0xFFFFFFFFu}, rs, (int)OFF_MEM + (s + 3) * STEPB + c * 256 + lane * 8);
        }
      }
      STAMP(3);
      LDS_BARRIER();
      AD_UPDATE();
      tanchor = __builtin_amdgcn_s_memtime();
      STAMP_FLUSH();
    }
  } else if (b < 136) {
    const int h = b - 104;
    bf16x8 Wk[2][20];
    u32x4 xp[10];
    LDX(0);
    asm volatile("" ::: "memory");
    {
      const u8* sk = (const u8*)(p.Wk + h * 128 + 32 * w);
#pragma unroll
      for (int i = 0; i < 10; ++i) dma_tile<64, 64>(sk + (size_t)i * 64 * 8192, 8192, wla + i * 4096, lane);
      WAIT_VM0();
#pragma unroll
      for (int ks = 0; ks < 20; ++ks)
#pragma unroll
        for (int tn = 0; tn < 2; ++tn) Wk[tn][ks] = frag_dense(wl, 64, 32 * ks, 16 * tn, lane);
      WAIT_LGKM0();
    }
#pragma unroll
    for (int tn = 0; tn < 2; ++tn)
#pragma unroll
      for (int ks = 0; ks < 20; ++ks) PIN_A(Wk[tn][ks]);
    LDS_BARRIER();
    u16* xs = (u16*)smem;
    u16* kb = (u16*)(smem + 41472);
    for (int t = 0; t < NSEG; ++t) {
      STAMP(0);
      STX();
      LDS_BARRIER();
      STAMP(1);
      LDX(t + 1 < NSEG ? t + 1 : 0);
      f32x4 acc[2][2];
      PROJ_GEMM(acc, Wk);
#pragma unroll
      for (int m = 0; m < 2; ++m)
#pragma unroll
        for (int tn = 0; tn < 2; ++tn) {
          u16* dst = kb + (8 * g + 4 * m) * KS + 32 * w + 16 * tn + r16;
          unsigned p01 = pk2(acc[m][tn][0], acc[m][tn][1]), p23 = pk2(acc[m][tn][2], acc[m][tn][3]);
          dst[0] = (u16)p01; dst[KS] = (u16)(p01 >> 16); dst[2 * KS] = (u16)p23; dst[3 * KS] = (u16)(p23 >> 16);
        }
      WAIT_VM(10);
      LDS_BARRIER();
      if (tid == 0 && t > 1) st32((unsigned)(t - 1), rs, (CW_KXF + 16 * h) * 4);
      STAMP(2);
#pragma unroll
      for (int q = 0; q < 2; ++q) {
        int pc = q * 256 + tid, row = pc >> 4, ch = pc & 15;
        u32x4 o = *(const u32x4*)(kb + row * KS + ch * 8);
        st128(o, rs, (int)OFF_KX + (t * 32 + h) * 8192 + pc * 16);
      }
      if (t == 0 || t == NSEG - 1) {
        WAIT_VM0();
        LDS_BARRIER();
        if (tid == 0) st32((unsigned)t, rs, (CW_KXF + 16 * h) * 4);
      }
      STAMP(3);
    }
  } else if (b < 168) {
    const int h = b - 136;
    wait_until(__builtin_amdgcn_s_memtime() + START_DLY);
    bf16x8 Wq[2][20];
    {
      const u8* sqp = (const u8*)(p.Wq + h * 128 + 32 * w);
#pragma unroll
      for (int i = 0; i < 10; ++i) dma_tile<64, 64>(sqp + (size_t)i * 64 * 8192, 8192, wla + i * 4096, lane);
      WAIT_VM0();
#pragma unroll
      for (int ks = 0; ks < 20; ++ks)
#pragma unroll
        for (int tn = 0; tn < 2; ++tn) Wq[tn][ks] = frag_dense(wl, 64, 32 * ks, 16 * tn, lane);
      WAIT_LGKM0();
    }
#pragma unroll
    for (int tn = 0; tn < 2; ++tn)
#pragma unroll
      for (int ks = 0; ks < 20; ++ks) PIN_A(Wq[tn][ks]);
    LDS_BARRIER();
    u16* xs = (u16*)smem;
    u16* kb = (u16*)(smem + 41472);
    u16* kmb = (u16*)(smem + 41472 + 8704);
    u8* pxb = smem + 41472 + 8704 + 2176;
    u32x4 xp[10];
    LDX(0);
    const float scale = 0.08838834764831845f;
    const float L2E = 1.4426950408889634f;
    int kflag = -1;
    u16* kxb = (u16*)(smem + 57344);
    {
      wait_flag(rs, CW_KXF + 16 * h, 0u, ctl, lane);
      u32x4 a = ld128(rs, (int)OFF_KX + h * 8192 + tid * 16), c2 = ld128(rs, (int)OFF_KX + h * 8192 + 4096 + tid * 16);
      *(u32x4*)(kxb + (tid >> 4) * KS + (tid & 15) * 8) = a;
      *(u32x4*)(kxb + (16 + (tid >> 4)) * KS + (tid & 15) * 8) = c2;
    }
    for (int u = 0; u < NSEG; ++u) {
      STAMP(0);
      STX();
      LDS_BARRIER();
      STAMP(1);
      const int un = u + 1 < NSEG ? u + 1 : u;
      if (kflag < un) {
        wait_flag(rs, CW_KXF + 16 * h, (unsigned)un, ctl, lane);
        kflag = __builtin_amdgcn_readfirstlane((int)ld32(rs, (CW_KXF + 16 * h) * 4));
      }
      u32x4 kn0 = ld128(rs, (int)OFF_KX + (un * 32 + h) * 8192 + tid * 16);
      u32x4 kn1 = ld128(rs, (int)OFF_KX + (un * 32 + h) * 8192 + 4096 + tid * 16);
      u32x4 k0 = ld128(rs, w == 3 ? (int)OFF_KM + u * STEPB + h * 2048 + lane * 16 : OOB);
      u32x4 k1 = ld128(rs, w == 3 ? (int)OFF_KM + u * STEPB + h * 2048 + 1024 + lane * 16 : OOB);
      LDX(u + 1 < NSEG ? u + 1 : 0);
      f32x4 acc[2][2];
      PROJ_GEMM(acc, Wq);
#pragma unroll
      for (int m = 0; m < 2; ++m)
#pragma unroll
        for (int tn = 0; tn < 2; ++tn) {
          u16* dst = kb + (16 * m + 4 * g) * KS + 32 * w + 16 * tn + r16;
          unsigned p01 = pk2(acc[m][tn][0], acc[m][tn][1]), p23 = pk2(acc[m][tn][2], acc[m][tn][3]);
          dst[0] = (u16)p01; dst[KS] = (u16)(p01 >> 16); dst[2 * KS] = (u16)p23; dst[3 * KS] = (u16)(p23 >> 16);
        }
      WAIT_VM(14);
      LDS_BARRIER();
      if (tid == 0 && u > 0 && u < NSEG - 1) st32((unsigned)(u - 1), rs, (CW_PXF + 16 * h) * 4);
      if (w == 3) {
        unsigned spins = 0;
        for (;;) {
          unsigned mxv = umax3(umax3(k0.x, k0.z, k1.x), k1.z, 0u);
          if (__all(mxv != 0xFFFFFFFFu) || spins > SPIN_CAP) {
            *(u32x4*)(kmb + (lane >> 4) * KS + (lane & 15) * 8) = k0;
            *(u32x4*)(kmb + (4 + (lane >> 4)) * KS + (lane & 15) * 8) = k1;
            break;
          }
          if (spins) __builtin_amdgcn_s_sleep(6);
          ++spins;
          asm volatile("" ::: "memory");
          k0 = ld128(rs, (int)OFF_KM + u * STEPB + h * 2048 + lane * 16);
          k1 = ld128(rs, (int)OFF_KM + u * STEPB + h * 2048 + 1024 + lane * 16);
          asm volatile("" ::: "memory");
        }
      }
      LDS_BARRIER();
      STAMP(2);
      {
        f32x4 sc[3];
        bf16x8 qf[4], kf[4];
#pragma unroll
        for (int kk = 0; kk < 4; ++kk) qf[kk] = *(const bf16x8*)(kb + (8 * w + r) * KS + 32 * kk + 8 * g);
#pragma unroll
        for (int kk = 0; kk < 4; ++kk) kf[kk] = *(const bf16x8*)(kmb + r * KS + 32 * kk + 8 * g);
        bf16x8 kxf[2][4];
        const u16* kxc = kxb + (u & 1) * (32 * KS);
#pragma unroll
        for (int t2 = 0; t2 < 2; ++t2)
#pragma unroll
          for (int kk = 0; kk < 4; ++kk) kxf[t2][kk] = *(const bf16x8*)(kxc + (16 * t2 + r16) * KS + 32 * kk + 8 * g);
        SCHED();
#pragma unroll
        for (int kk = 0; kk < 4; ++kk) {
          sc[1] = mf16(kxf[0][kk], qf[kk], kk == 0 ? Z4 : sc[1]);
          sc[2] = mf16(kxf[1][kk], qf[kk], kk == 0 ? Z4 : sc[2]);
          sc[0] = mf16(kf[kk], qf[kk], kk == 0 ? Z4 : sc[0]);
        }
        const int tok = 8 * w + r;
        const int tkA = 16 * (g & 1) + 4 * (g >> 1), tkB = tkA + 8;
        float v[6];
        int tk[6];
        v[0] = par ? sc[1][2] : sc[0][0]; tk[0] = par ? tkA + 2 : -1;
        v[1] = par ? sc[1][3] : sc[0][1]; tk[1] = par ? tkA + 3 : -1;
        v[2] = par ? sc[2][0] : sc[0][2]; tk[2] = par ? tkB : -1;
        v[3] = par ? sc[2][1] : sc[0][3]; tk[3] = par ? tkB + 1 : -1;
        v[4] = par ? sc[2][2] : sc[1][0]; tk[4] = par ? tkB + 2 : tkA;
        v[5] = par ? sc[2][3] : sc[1][1]; tk[5] = par ? tkB + 3 : tkA + 1;
        const bool nomem = !par && g >= 2;
        float sv[6];
#pragma unroll
        for (int q = 0; q < 3; ++q) {
          unsigned p1 = pk2(v[2 * q], v[2 * q + 1]);
          unsigned p2 = pk2(blo(p1) * scale, bhi(p1) * scale);
          sv[2 * q] = blo(p2); sv[2 * q + 1] = bhi(p2);
        }
        const float MASKV = rbf(-1.0e9f);
#pragma unroll
        for (int q = 0; q < 6; ++q) {
          if (tk[q] > tok) sv[q] = MASKV;
          if (q < 4 && nomem) sv[q] = -3.0e38f;
        }
        float mx = fmaxf(fmaxf(fmaxf(sv[0], sv[1]), fmaxf(sv[2], sv[3])), fmaxf(sv[4], sv[5]));
        mx = xmax32(xmax16(xmax8(mx)));
        float e[6];
#pragma unroll
        for (int q = 0; q < 6; ++q) e[q] = __builtin_amdgcn_exp2f((sv[q] - mx) * L2E);
        float sum = ((e[0] + e[1]) + (e[2] + e[3])) + (e[4] + e[5]);
        sum = xadd32(xadd16(xadd8(sum)));
        float inv = __builtin_amdgcn_rcpf(sum);
        unsigned o0 = pk2(e[0] * inv, e[1] * inv), o1 = pk2(e[2] * inv, e[3] * inv), o2 = pk2(e[4] * inv, e[5] * inv);
        unsigned* pl = (unsigned*)pxb + tok * 4;
        int dA = 128 + (g >> 1) * 128 + 2 * (g & 1), dB = dA + 256;
        int d0 = par ? dA + 1 : 2 * g, d1 = par ? dB : 2 * g + 1, d2 = par ? dB + 1 : dA;
        if (!nomem) { pl[d0] = o0; pl[d1] = o1; }
        pl[d2] = o2;
      }
      {
        u16* kxn = kxb + ((u + 1) & 1) * (32 * KS);
        *(u32x4*)(kxn + (tid >> 4) * KS + (tid & 15) * 8) = kn0;
        *(u32x4*)(kxn + (16 + (tid >> 4)) * KS + (tid & 15) * 8) = kn1;
      }
      LDS_BARRIER();
      if (tid < 160) {
        u32x4 o = *(const u32x4*)(pxb + tid * 16);
        st128(o, rs, (int)OFF_PX + (u * 32 + h) * 2560 + tid * 16);
      }
      if (u >= NSEG - 2) {
        WAIT_VM0();
        LDS_BARRIER();
        if (tid == 0) st32((unsigned)u, rs, (CW_PXF + 16 * h) * 4);
      }
    }
  } else if (b < 200) {
    const int h = b - 168;
    bf16x8 Wv[2][20], Wo[10][4];
    u32x4 xp[10];
    LDX(0);
    asm volatile("" ::: "memory");
    {
      const u8* sv = (const u8*)(p.Wv + h * 128 + 32 * w);
      const u8* so = (const u8*)(p.Wo + (size_t)h * 128 * 640 + 160 * w);
#pragma unroll
      for (int i = 0; i < 10; ++i) dma_tile<64, 64>(sv + (size_t)i * 64 * 8192, 8192, wla + i * 4096, lane);
      WAIT_VM(20);
#pragma unroll
      for (int ks = 0; ks < 10; ++ks)
#pragma unroll
        for (int tn = 0; tn < 2; ++tn) Wv[tn][ks] = frag_dense(wl, 64, 32 * ks, 16 * tn, lane);
      WAIT_LGKM0();
      dma_tile<64, 320>(so, 1280, wla, lane);
      WAIT_VM(20);
#pragma unroll
      for (int ks = 10; ks < 20; ++ks)
#pragma unroll
        for (int tn = 0; tn < 2; ++tn) Wv[tn][ks] = frag_dense(wl, 64, 32 * ks, 16 * tn, lane);
      WAIT_LGKM0();
      dma_tile<64, 320>(so + (size_t)64 * 1280, 1280, wla + 20480, lane);
      WAIT_VM(20);
#pragma unroll
      for (int ks = 0; ks < 2; ++ks)
#pragma unroll
        for (int t2 = 0; t2 < 10; ++t2) Wo[t2][ks] = frag_dense(wl, 320, 32 * ks, 16 * t2, lane);
      WAIT_VM(0);
#pragma unroll
      for (int ks = 2; ks < 4; ++ks)
#pragma unroll
        for (int t2 = 0; t2 < 10; ++t2) Wo[t2][ks] = frag_dense(wl, 320, 32 * ks, 16 * t2, lane);
      WAIT_LGKM0();
    }
#pragma unroll
    for (int t2 = 0; t2 < 10; ++t2)
#pragma unroll
      for (int ks = 0; ks < 4; ++ks) PIN_A(Wo[t2][ks]);
#pragma unroll
    for (int tn = 0; tn < 2; ++tn)
#pragma unroll
      for (int ks = 0; ks < 8; ++ks) PIN_A(Wv[tn][ks]);
    LDS_BARRIER();
    u16* xs = (u16*)smem;
    u16* vb = (u16*)(smem + 41472);
    for (int t = 0; t < NSEG; ++t) {
      STX();
      LDS_BARRIER();
      LDX(t + 1 < NSEG ? t + 1 : 0);
      f32x4 acc[2][2];
      PROJ_GEMM(acc, Wv);
#pragma unroll
      for (int m = 0; m < 2; ++m)
#pragma unroll
        for (int tn = 0; tn < 2; ++tn) {
          u16* dst = vb + (16 * m + 4 * g) * KS + 32 * w + 16 * tn + r16;
          unsigned p01 = pk2(acc[m][tn][0], acc[m][tn][1]), p23 = pk2(acc[m][tn][2], acc[m][tn][3]);
          dst[0] = (u16)p01; dst[KS] = (u16)(p01 >> 16); dst[2 * KS] = (u16)p23; dst[3 * KS] = (u16)(p23 >> 16);
        }
      WAIT_VM(10);
      LDS_BARRIER();
      if (tid == 0 && t > 1) st32((unsigned)(t - 1), rs, (CW_UXF + 16 * h) * 4);
      bf16x8 va[2][4];
#pragma unroll
      for (int m = 0; m < 2; ++m)
#pragma unroll
        for (int ks = 0; ks < 4; ++ks) va[m][ks] = *(const bf16x8*)(vb + (16 * m + r16) * KS + 32 * ks + 8 * g);
#pragma unroll
      for (int t2 = 0; t2 < 10; ++t2) {
        f32x4 c0 = Z4, c1 = Z4;
#pragma unroll
        for (int ks = 0; ks < 4; ++ks) { c0 = mf16(va[0][ks], Wo[t2][ks], c0); c1 = mf16(va[1][ks], Wo[t2][ks], c1); }
        int col = 160 * w + 16 * t2 + r16;
        u32x4 o = {pk2(c0[0], c0[1]), pk2(c0[2], c0[3]), pk2(c1[0], c1[1]), pk2(c1[2], c1[3])};
        st128(o, rs, (int)OFF_UX + ((t * 80 + (col >> 3)) * 32 + h) * 512 + (col & 7) * 64 + 16 * g);
      }
      if (t == 0 || t == NSEG - 1) {
        WAIT_VM0();
        LDS_BARRIER();
        if (tid == 0) st32((unsigned)t, rs, (CW_UXF + 16 * h) * 4);
      }
    }
  } else if (b < 232) {
    const int f = b - 200;
    wait_until(__builtin_amdgcn_s_memtime() + START_DLY);
    u8* slab0 = smem;
    const unsigned ldsb = __builtin_amdgcn_readfirstlane((unsigned)(size_t)(LAS u8*)smem);
    u8* wdb = smem + 65536;
    u16* hb = (u16*)(smem + 65536 + 81920);
    bf16x8 A[20];
#pragma unroll
    for (int ks = 0; ks < 20; ++ks)
      A[ks] = *(const bf16x8*)(p.x + (size_t)(64 * f + 16 * w + r16) * 640 + 32 * ks + 8 * g);
#pragma unroll
    for (int ks = 0; ks < 16; ++ks) PIN_A(A[ks]);
    f32x4 acc2[4][10];
#pragma unroll
    for (int m = 0; m < 4; ++m)
#pragma unroll
      for (int n = 0; n < 10; ++n) acc2[m][n] = Z4;
    const int i16 = lane & 15;
    unsigned so[2], tb[4], wo2[2], tw;
#pragma unroll
    for (int q = 0; q < 2; ++q) {
      int row = 8 * (2 * w + q) + (lane >> 3);
      so[q] = (unsigned)(row * (DF * 2) + (((lane & 7) ^ swz<0>(row)) << 4));
      wo2[q] = (unsigned)((lane >> 4) * 1280 + (((lane & 15) ^ (2 * ((lane >> 4) | (q << 2)))) << 4));
    }
#pragma unroll
    for (int q = 0; q < 4; ++q) {
      int s0 = swz<0>(8 * g + (i16 >> 2));
      tb[q] = ldsb + (unsigned)((8 * g + (i16 >> 2)) * 128 + (((2 * q + ((i16 & 3) >> 1)) ^ s0) << 4) + (i16 & 1) * 8);
    }
    const int s1 = 2 * ((i16 >> 2) | ((g & 1) << 2));
    tw = ldsb + 65536 + (unsigned)((8 * g + (i16 >> 2)) * 256 + ((i16 & 3) >> 1) * 16 + (i16 & 1) * 8);
#define FFN_SLAB(c_, si_, slot_) do { \
      const u8* sg_ = (const u8*)(Wg_ + (size_t)(64 * (si_)) * DF + 64 * (c_)); \
      const u8* su_ = (const u8*)(Wu_ + (size_t)(64 * (si_)) * DF + 64 * (c_)); \
      unsigned bb_ = ldsb + (slot_) * 16384 + 2 * wq * 1024; \
      glds16_s(sg_, so[0], bb_); glds16_s(sg_, so[1], bb_ + 1024); \
      glds16_s(su_, so[0], bb_ + 8192); glds16_s(su_, so[1], bb_ + 8192 + 1024); } while (0)
#define FFN_WD(si_) ((si_) < 0 ? 0 : (si_) < 6 ? 3 : (si_) == 6 ? 2 : 0)
    WAIT_VM0();
    const u16* Wg_ = p.Wg; const u16* Wu_ = p.Wu; int wq = w;
    const int c0 = f % 27;
    FFN_SLAB(c0, 0, 0); FFN_SLAB(c0, 1, 1); FFN_SLAB(c0, 2, 2);
#pragma unroll 1
    for (int ci = 0; ci < 27; ++ci) {
      const int c = c0 + ci < 27 ? c0 + ci : c0 + ci - 27;
      const int cn = c + 1 < 27 ? c + 1 : 0;
      const int slotb = (ci * 10) & 3;
      const u8* sd = (const u8*)(p.Wd + (size_t)(64 * c) * 640);
      f32x4 ag[4], au[4];
#pragma unroll
      for (int si = 0; si < 10; ++si) {
        asm volatile("s_waitcnt vmcnt(%0)" ::"n"(FFN_WD(si - 3) + 4 + FFN_WD(si - 2) + 4 + FFN_WD(si - 1)) : "memory");
        LDS_BARRIER();
        asm volatile("" : "+s"(Wg_), "+s"(Wu_), "+s"(wq));
        if (si + 3 < 10) FFN_SLAB(c, si + 3, (slotb + si + 3) & 3);
        else FFN_SLAB(cn, si + 3 - 10, (slotb + si + 3) & 3);
#pragma unroll
        for (int q = 0; q < FFN_WD(si); ++q) {
          const int P = 20 * wq + 3 * si + q;
          glds16_s(sd + (P & 15) * 4 * 1280 + (P >> 4) * 256, (P & 2) ? wo2[1] : wo2[0], ldsb + 65536 + P * 1024);
        }
        const unsigned sof = ((slotb + si) & 3) * 16384;
#pragma unroll
        for (int k2 = 0; k2 < 2; ++k2) {
          const int ks = si * 2 + k2;
          bf16x8 bg[4], bu[4];
#pragma unroll
          for (int ft = 0; ft < 4; ++ft) {
            const unsigned t0 = tb[ft] + sof + k2 * 4096;
            bg[ft] = trfrag(t0, t0 + 512);
            bu[ft] = trfrag(t0 + 8192, t0 + 8192 + 512);
          }
#pragma unroll
          for (int ft = 0; ft < 4; ++ft) {
            ag[ft] = mf16(A[ks], bg[ft], ks == 0 ? Z4 : ag[ft]);
            au[ft] = mf16(A[ks], bu[ft], ks == 0 ? Z4 : au[ft]);
          }
        }
      }
#pragma unroll
      for (int ft = 0; ft < 4; ++ft)
#pragma unroll
        for (int j = 0; j < 4; ++j) {
          float gv = rbf(ag[ft][j]), uv = rbf(au[ft][j]);
          hb[(16 * w + 4 * g + j) * 72 + 16 * ft + r16] = f2bf(rbf(gelu_tanh(gv)) * uv);
        }
      asm volatile("s_waitcnt vmcnt(12)" ::: "memory");
      LDS_BARRIER();
#pragma unroll
      for (int k2 = 0; k2 < 2; ++k2) {
        bf16x8 ha[4];
#pragma unroll
        for (int m = 0; m < 4; ++m) ha[m] = *(const bf16x8*)(hb + (16 * m + r16) * 72 + 32 * k2 + 8 * g);
#pragma unroll
        for (int n = 0; n < 10; ++n) {
          const int nc = 160 * wq + 16 * n;
          const unsigned ta = tw + (nc >> 7) * 16384 + ((((nc & 127) >> 3) ^ s1) << 4) + k2 * 8192;
          bf16x8 bd = trfrag(ta, ta + 1024);
#pragma unroll
          for (int m = 0; m < 4; ++m) acc2[m][n] = mf16(ha[m], bd, acc2[m][n]);
        }
      }
      LDS_BARRIER();
    }
    WAIT_VM0();
    LDS_BARRIER();
    {
      u16* ob = (u16*)smem;
#pragma unroll
      for (int m = 0; m < 4; ++m)
#pragma unroll
        for (int n = 0; n < 10; ++n)
#pragma unroll
          for (int j = 0; j < 4; ++j) ob[(16 * m + 4 * g + j) * XS + 160 * w + 16 * n + r16] = f2bf(acc2[m][n][j]);
      LDS_BARRIER();
      for (int pc = tid; pc < 64 * 80; pc += 256) {
        int row = pc / 80, cc = pc % 80;
        u32x4 o = *(const u32x4*)(ob + row * XS + cc * 8);
        st128(o, rs, (int)OFF_BO + (64 * f + row) * 1280 + cc * 16);
      }
      WAIT_VM0();
      LDS_BARRIER();
      unsigned* smx = (unsigned*)(smem + 90112);
      if (tid == 0) {
        st32(0u, rs, (CW_FF + 16 * f) * 4);
        *smx = atomicAnd(ctl + CW_MIX + 16 * f, 0x7FFFFFFFu);
      }
      LDS_BARRIER();
      const unsigned done = ~(*smx) & 0xFFFFFu;
      if (done) {
        const int row = tid >> 3, c8 = tid & 7;
        float omg[2];
#pragma unroll
        for (int hs = 0; hs < 2; ++hs) omg[hs] = rbf(1.0f - gate8(p.x + (size_t)(64 * f + 32 * hs + row) * 640, p.gw, c8));
        for (int k = 0; k < 20; ++k) {
          if (!((done >> k) & 1)) continue;
          const int hs = k / NCH, cj = k % NCH;
          const int tr = 32 * hs + row, cc = cj * 64 + c8 * 8;
          u32x4 ga = ld128(rs, (int)OFF_GA + ((64 * f + tr) * 640 + cc) * 2);
          u32x4 bv = *(const u32x4*)(ob + tr * XS + cc);
          const float om = hs ? omg[1] : omg[0];
          u32x4 o;
#pragma unroll
          for (int e = 0; e < 4; ++e)
            o[e] = pk2(blo(ga[e]) + rbf(om * blo(bv[e])), bhi(ga[e]) + rbf(om * bhi(bv[e])));
          *(u32x4*)(p.out + (size_t)(64 * f + tr) * 640 + cc) = o;
        }
      }
    }
  }

  if (b >= 104 && !(b >= 136 && b < 168)) {
    int tid = threadIdx.x;
    asm volatile("" : "+v"(tid));
    const int lane = tid & 63, r16 = lane & 15, g = lane >> 4;
    float* sg = (float*)smem;
    int* stask = (int*)(smem + 256);
    for (;;) {
      LDS_BARRIER();
      if (tid == 0) *stask = (int)atomicAdd(ctl + CW_QUEUE, 1u) + 1;
      LDS_BARRIER();
      const int task = *stask;
      if (task >= NTASK) break;
      const int t = task / NCH, cj = task % NCH;
      STAMP(0);
      {
        float gv = gate8(p.x + (size_t)(t * 32 + (tid >> 3)) * 640, p.gw, tid & 7);
        if ((tid & 7) == 0) sg[tid >> 3] = gv;
      }
      if (w == 0) wait_flag<OUT_SLP>(rs, CW_UXF + 16 * (lane & 31), (unsigned)t, ctl, lane);
      LDS_BARRIER();
      STAMP(1);
      const int col = cj * 64 + w * 16 + r16;
      const int uxo = (int)OFF_UX + (t * 80 + (col >> 3)) * 32 * 512 + (col & 7) * 64 + 16 * g;
      const int umo = (int)OFF_UM + t * STEPB + (col >> 3) * 4096 + (col & 7) * 16;
      u32x4 bx[32];
#pragma unroll
      for (int h = 0; h < 32; ++h) bx[h] = ld128(rs, uxo + h * 512);
      if (w == 0) wait_flag<OUT_SLP>(rs, CW_PXF + 16 * (lane & 31), (unsigned)t, ctl, lane);
      LDS_BARRIER();
      u8* pxl = smem + 1024;
      {
        const u8* pxg = ws + OFF_PX + (size_t)t * 32 * 2560;
#pragma unroll
        for (int q = 0; q < 20; ++q)
          __builtin_amdgcn_global_load_lds((const unsigned*)(pxg + (20 * w + q) * 1024 + lane * 16), (unsigned*)(pxl + (20 * w + q) * 1024), 16, 0, AUX_SC1);
      }
      u32x4 um[8];
      {
        unsigned spins = 0;
        for (;;) {
          asm volatile("" ::: "memory");
          unsigned mxv = 0;
#pragma unroll
          for (int hq = 0; hq < 8; ++hq) um[hq] = ld128(rs, umo + (4 * hq + g) * 128);
#pragma unroll
          for (int hq = 0; hq < 8; ++hq) mxv = umax3(mxv, um[hq].x, um[hq].z);
          if (__all(mxv != 0xFFFFFFFFu) || spins > SPIN_CAP) break;
          __builtin_amdgcn_s_sleep(4);
          ++spins;
        }
      }
      WAIT_VM0();
      LDS_BARRIER();
      f32x4 acc0 = Z4, acc1 = Z4;
#pragma unroll
      for (int h = 0; h < 32; ++h) {
        bf16x8 a0 = *(const bf16x8*)(pxl + h * 2560 + 512 + g * 512 + r16 * 16);
        bf16x8 a1 = *(const bf16x8*)(pxl + h * 2560 + 512 + g * 512 + 256 + r16 * 16);
        acc0 = mf16(a0, __builtin_bit_cast(bf16x8, bx[h]), acc0);
        acc1 = mf16(a1, __builtin_bit_cast(bf16x8, bx[h]), acc1);
      }
#pragma unroll
      for (int hq = 0; hq < 8; ++hq) {
        bf16x8 a0 = *(const bf16x8*)(pxl + (4 * hq + g) * 2560 + r16 * 16);
        bf16x8 a1 = *(const bf16x8*)(pxl + (4 * hq + g) * 2560 + 256 + r16 * 16);
        acc0 = mf16(a0, __builtin_bit_cast(bf16x8, um[hq]), acc0);
        acc1 = mf16(a1, __builtin_bit_cast(bf16x8, um[hq]), acc1);
      }
      STAMP(2);
      u16* rt = (u16*)(smem + 512 + 83968);
#pragma unroll
      for (int i = 0; i < 2; ++i)
#pragma unroll
        for (int q = 0; q < 4; ++q) {
          int tok = 16 * i + 4 * g + q;
          rt[tok * 72 + w * 16 + r16] = f2bf(sg[tok] * rbf(i == 0 ? acc0[q] : acc1[q]));
        }
      if (tid == 0) stask[1] = (int)ld32(rs, (CW_FF + 16 * (t >> 1)) * 4);
      LDS_BARRIER();
      {
        const int row = tid >> 3, c8 = tid & 7;
        const int go = ((t * 32 + row) * 640 + cj * 64 + c8 * 8) * 2;
        u32x4 ga = *(const u32x4*)(rt + row * 72 + c8 * 8);
        bool mix = stask[1] >= 0;
        if (!mix) {
          st128(ga, rs, (int)OFF_GA + go);
          WAIT_VM0();
          LDS_BARRIER();
          if (tid == 0) stask[2] = (int)atomicAnd(ctl + CW_MIX + 16 * (t >> 1), ~(1u << ((t & 1) * NCH + cj)));
          LDS_BARRIER();
          mix = stask[2] >= 0;
        }
        if (mix) {
          u32x4 bv = ld128(rs, (int)OFF_BO + go);
          const float om = rbf(1.0f - sg[row]);
          u32x4 o;
#pragma unroll
          for (int e = 0; e < 4; ++e)
            o[e] = pk2(blo(ga[e]) + rbf(om * blo(bv[e])), bhi(ga[e]) + rbf(om * bhi(bv[e])));
          *(u32x4*)((u8*)p.out + go) = o;
        }
      }
    }
  }
}

extern "C" void kernel_launch(void* const* d_in, const int* in_sizes, int n_in, void* d_out, int out_size, void* d_ws,
                              size_t ws_size, hipStream_t stream) {
  u8* ws = (u8*)d_ws;
  Params p;
  p.x = (const u16*)d_in[0]; p.mem0 = (const u16*)d_in[1]; p.Wq = (const u16*)d_in[2]; p.Wk = (const u16*)d_in[3];
  p.Wv = (const u16*)d_in[4]; p.Wo = (const u16*)d_in[5];
  p.Wg = (const u16*)d_in[8]; p.Wu = (const u16*)d_in[9]; p.Wd = (const u16*)d_in[10]; p.gw = (const u16*)d_in[11];
  p.ws = ws; p.out = (u16*)d_out;

  (void)hipFuncSetAttribute((const void*)k_main, hipFuncAttributeMaxDynamicSharedMemorySize, 163840);
  (void)hipMemsetAsync(ws, 0xFF, INIT_BYTES, stream);
  k_main<<<256, 256, 163840, stream>>>(p);
}
```

```cpp
#include <hip/hip_runtime.h>
#include <hip/hip_bf16.h>
#include <stdint.h>

typedef __bf16 bf16x8 __attribute__((ext_vector_type(8)));
typedef __bf16 bf16x16 __attribute__((ext_vector_type(16)));
typedef __bf16 bf16x2 __attribute__((ext_vector_type(2)));
typedef float f32x2 __attribute__((ext_vector_type(2)));
typedef float f32x4 __attribute__((ext_vector_type(4)));
typedef unsigned u32x4 __attribute__((ext_vector_type(4)));
typedef unsigned u32x2 __attribute__((ext_vector_type(2)));
typedef unsigned u32x8 __attribute__((ext_vector_type(8)));
typedef short s16x4 __attribute__((ext_vector_type(4)));
typedef unsigned short u16;
typedef unsigned char u8;

constexpr int SEQ = 2048, DM = 640, NSEG = 64, DF = 1728;

constexpr size_t OFF_CTL = 0;
constexpr size_t CTL_BYTES = 16384;
constexpr int STEPB = 10240 + 20480 + 65536 + 327680;
constexpr size_t OFF_MEM = CTL_BYTES;
constexpr size_t OFF_P = OFF_MEM + 10240;
constexpr size_t OFF_KM = OFF_P + 20480;
constexpr size_t OFF_UM = OFF_KM + 65536;
constexpr size_t OFF_SENT_END = CTL_BYTES + (size_t)66 * STEPB;
constexpr size_t INIT_BYTES = CTL_BYTES + (size_t)3 * STEPB;
constexpr size_t OFF_KX = OFF_SENT_END;
constexpr size_t OFF_UX = OFF_KX + (size_t)SEQ * 4096 * 2;
constexpr size_t OFF_PX = OFF_UX + (size_t)64 * 80 * 32 * 8 * 32 * 2;
constexpr size_t OFF_BO = OFF_PX + (size_t)64 * 32 * 2560;
constexpr size_t OFF_GA = OFF_BO + (size_t)SEQ * DM * 2;
constexpr size_t OFF_END = OFF_GA + (size_t)SEQ * DM * 2;
constexpr int CW_TMO = 0, CW_QUEUE = 32, CW_KXF = 64, CW_UXF = 1024, CW_PXF = 2048, CW_FF = 3072, CW_MIX = 3584;

#define AUX_SC1 16
#define RSRC_W3 0x00020000
#define OOB 0x7FFF0000

__device__ __forceinline__ float bf2f(u16 v) { return __uint_as_float(((unsigned)v) << 16); }
__device__ __forceinline__ u16 f2bf(float f) { __bf16 b = (__bf16)f; return __builtin_bit_cast(u16, b); }
__device__ __forceinline__ float rbf(float f) { return (float)((__bf16)f); }
__device__ __forceinline__ unsigned pk2(float a, float b) {
  f32x2 v = {a, b};
  return __builtin_bit_cast(unsigned, __builtin_convertvector(v, bf16x2));
}
__device__ __forceinline__ unsigned nosent(unsigned x) { return x < 0xFFFFFFFEu ? x : 0xFFFFFFFEu; }
__device__ __forceinline__ f32x4 smf(bf16x8 a, bf16x16 b, f32x4 c, int idx) {
  return __builtin_amdgcn_smfmac_f32_16x16x64_bf16(a, b, c, idx, 0, 0);
}
__device__ __forceinline__ f32x4 mf16(bf16x8 a, bf16x8 b, f32x4 c) {
  return __builtin_amdgcn_mfma_f32_16x16x32_bf16(a, b, c, 0, 0, 0);
}
__device__ __forceinline__ float comb(float a, float b) {
  auto r = __builtin_amdgcn_permlane32_swap(__float_as_uint(a), __float_as_uint(b), false, false);
  return __uint_as_float(r[0]) + __uint_as_float(r[1]);
}
__device__ __forceinline__ u32x4 ld128(__amdgpu_buffer_rsrc_t rs, int off) {
  return __builtin_amdgcn_raw_buffer_load_b128(rs, off, 0, AUX_SC1);
}
__device__ __forceinline__ unsigned ld32(__amdgpu_buffer_rsrc_t rs, int off) {
  return __builtin_amdgcn_raw_buffer_load_b32(rs, off, 0, AUX_SC1);
}
__device__ __forceinline__ void st128(u32x4 v, __amdgpu_buffer_rsrc_t rs, int off) {
  __builtin_amdgcn_raw_buffer_store_b128(v, rs, off, 0, AUX_SC1);
}
__device__ __forceinline__ void st64(u32x2 v, __amdgpu_buffer_rsrc_t rs, int off) {
  __builtin_amdgcn_raw_buffer_store_b64(v, rs, off, 0, AUX_SC1);
}
__device__ __forceinline__ void st32(unsigned v, __amdgpu_buffer_rsrc_t rs, int off) {
  __builtin_amdgcn_raw_buffer_store_b32(v, rs, off, 0, AUX_SC1);
}

constexpr unsigned SPIN_CAP = 1u << 16;
#define STAMP(slot) do { } while (0)
#define STAMP_FLUSH() do { } while (0)
#define LDS_BARRIER() do { asm volatile("s_waitcnt lgkmcnt(0)" ::: "memory"); __builtin_amdgcn_s_barrier(); asm volatile("" ::: "memory"); } while (0)
#define WAIT_VM0() asm volatile("s_waitcnt vmcnt(0)" ::: "memory")
#define WAIT_LGKM0() asm volatile("s_waitcnt lgkmcnt(0)" ::: "memory")
#define PIN_A(x) asm volatile("" : "+a"(x))
#define PIN_V(x) asm volatile("" : "+v"(x))
#define LAS __attribute__((address_space(3)))

__device__ __forceinline__ float rmax3(float a, float b, float c) {
  float r; asm("v_max3_f32 %0, %1, %2, %3\n\ts_nop 1" : "=v"(r) : "v"(a), "v"(b), "v"(c)); return r;
}
__device__ __forceinline__ float rmax2(float a, float b) {
  float r; asm("v_max_f32 %0, %1, %2\n\ts_nop 1" : "=v"(r) : "v"(a), "v"(b)); return r;
}
__device__ __forceinline__ float xmax8(float v) {
  int o = __builtin_amdgcn_update_dpp(0, __float_as_int(v), 0x128, 0xf, 0xf, false);
  return rmax2(v, __int_as_float(o));
}
__device__ __forceinline__ float xadd8(float v) {
  int o = __builtin_amdgcn_update_dpp(0, __float_as_int(v), 0x128, 0xf, 0xf, false);
  return v + __int_as_float(o);
}
__device__ __forceinline__ float xmax16(float v) {
  auto r = __builtin_amdgcn_permlane16_swap(__float_as_uint(v), __float_as_uint(v), false, false);
  return rmax2(__uint_as_float(r[0]), __uint_as_float(r[1]));
}
__device__ __forceinline__ float xadd16(float v) {
  auto r = __builtin_amdgcn_permlane16_swap(__float_as_uint(v), __float_as_uint(v), false, false);
  return __uint_as_float(r[0]) + __uint_as_float(r[1]);
}
__device__ __forceinline__ float xmax32(float v) {
  auto r = __builtin_amdgcn_permlane32_swap(__float_as_uint(v), __float_as_uint(v), false, false);
  return rmax2(__uint_as_float(r[0]), __uint_as_float(r[1]));
}
__device__ __forceinline__ float xadd32(float v) {
  auto r = __builtin_amdgcn_permlane32_swap(__float_as_uint(v), __float_as_uint(v), false, false);
  return __uint_as_float(r[0]) + __uint_as_float(r[1]);
}
__device__ __forceinline__ unsigned umax3(unsigned a, unsigned b, unsigned c) { return max(max(a, b), c); }
__device__ __forceinline__ float blo(unsigned p) { return __uint_as_float(p << 16); }
__device__ __forceinline__ float bhi(unsigned p) { return __uint_as_float(p & 0xFFFF0000u); }

__device__ __forceinline__ void glds16_asm(const void* gsrc, unsigned lds_dst) {
  unsigned keep;
  asm volatile("s_mov_b32 %0, m0\n\ts_mov_b32 m0, %2\n\ts_nop 0\n\tglobal_load_lds_dwordx4 %1, off\n\ts_mov_b32 m0, %0"
               : "=&s"(keep) : "v"(gsrc), "s"(lds_dst) : "memory");
}
template <int R, int CB>
__device__ __forceinline__ void dma_tile(const u8* src, int ldb, unsigned lds, int lane) {
  constexpr int CPR = CB / 16;
  constexpr int N = R * CPR / 64;
#pragma unroll
  for (int q = 0; q < N; ++q) {
    int idx = q * 64 + lane;
    int row = idx / CPR, ch = idx % CPR;
    glds16_asm(src + (size_t)row * ldb + ch * 16, lds + q * 1024);
  }
}
#define WAIT_VM(n) asm volatile("s_waitcnt vmcnt(%0)" ::"n"(n) : "memory")
__device__ __forceinline__ u32x2 tr4(const u8* tile, int rsb, int k0, int n0, int lane) {
  int i = lane & 15;
  const u8* p = tile + (k0 + (i >> 2)) * rsb + (n0 + 4 * (i & 3)) * 2;
  s16x4 v = __builtin_amdgcn_ds_read_tr16_b64_v4i16((LAS s16x4*)p);
  return __builtin_bit_cast(u32x2, v);
}
__device__ __forceinline__ bf16x16 frag_sparse(const u8* blk, int rsb, int n0, int lane) {
  int gb = lane >> 4;
  int kb = 16 * (gb >> 1) + 4 * (gb & 1);
  u32x2 t00 = tr4(blk, rsb, kb, n0, lane), t01 = tr4(blk, rsb, kb + 8, n0, lane);
  u32x2 t10 = tr4(blk, rsb, kb + 32, n0, lane), t11 = tr4(blk, rsb, kb + 40, n0, lane);
  u32x8 d = {t00.x, t01.x, t00.y, t01.y, t10.x, t11.x, t10.y, t11.y};
  return __builtin_bit_cast(bf16x16, d);
}
__device__ __forceinline__ bf16x8 frag_dense(const u8* blk, int rsb, int k0, int n0, int lane) {
  int g = lane >> 4;
  u32x2 a = tr4(blk, rsb, k0 + 8 * g, n0, lane), b = tr4(blk, rsb, k0 + 8 * g + 4, n0, lane);
  u32x4 d = {a.x, a.y, b.x, b.y};
  return __builtin_bit_cast(bf16x8, d);
}

template <int MODE> __device__ __forceinline__ int swz(int row) {
  return MODE == 0 ? 2 * (((row >> 1) & 1) | (((row >> 3) & 1) << 1)) : 2 * ((row & 3) | (((row >> 3) & 1) << 2));
}
__device__ __forceinline__ void glds16_s(const void* sbase, unsigned voff, unsigned lds_dst) {
  unsigned keep;
  asm volatile("s_mov_b32 %0, m0\n\ts_mov_b32 m0, %3\n\ts_nop 0\n\tglobal_load_lds_dwordx4 %1, %2\n\ts_mov_b32 m0, %0"
               : "=&s"(keep) : "v"(voff), "s"(sbase), "s"(lds_dst) : "memory");
}
__device__ __forceinline__ u32x2 trr(unsigned addr) {
  s16x4 v = __builtin_amdgcn_ds_read_tr16_b64_v4i16((LAS s16x4*)(size_t)addr);
  return __builtin_bit_cast(u32x2, v);
}
__device__ __forceinline__ bf16x8 trfrag(unsigned a0, unsigned a1) {
  u32x2 a = trr(a0), b = trr(a1);
  u32x4 d = {a.x, a.y, b.x, b.y};
  return __builtin_bit_cast(bf16x8, d);
}
__device__ __forceinline__ float gelu_tanh(float x) {
  float u = 0.7978845608028654f * (x + 0.044715f * x * x * x);
  return x * __builtin_amdgcn_rcpf(1.0f + __builtin_amdgcn_exp2f(-2.8853900817779268f * u));
}

__device__ __forceinline__ float gate8(const u16* xrow, const u16* gw, int part) {
  float sacc = 0.f;
#pragma unroll
  for (int q = 0; q < 10; ++q) {
    u32x4 xv = *(const u32x4*)(xrow + part * 8 + q * 64), gv = *(const u32x4*)(gw + part * 8 + q * 64);
#pragma unroll
    for (int e = 0; e < 4; ++e) {
      sacc += rbf(blo(xv[e]) * blo(gv[e]));
      sacc += rbf(bhi(xv[e]) * bhi(gv[e]));
    }
  }
  sacc += __shfl_xor(sacc, 1); sacc += __shfl_xor(sacc, 2); sacc += __shfl_xor(sacc, 4);
  float z = rbf(sacc);
  return rbf(1.0f / (1.0f + __expf(-z)));
}

template <int SLP = 2>
__device__ __forceinline__ void wait_flag(__amdgpu_buffer_rsrc_t rs, int cw, unsigned v, unsigned* ctl, int lane) {
  for (unsigned spins = 0;; ++spins) {
    asm volatile("" ::: "memory");
    int f = (int)ld32(rs, cw * 4);
    if (__all(f >= (int)v)) break;
    if (spins > SPIN_CAP) { if (lane == 0) atomicAdd(ctl, 1u); break; }
    __builtin_amdgcn_s_sleep(SLP);
  }
  asm volatile("" ::: "memory");
}

#define ADAPT 7
#define AD_DEC 16
#define AD_INC 64
#define D1 3800
#define DV 4300
#define D2 4800
__device__ __forceinline__ void wait_until(unsigned long long t) {
  while (__builtin_amdgcn_s_memtime() < t) __builtin_amdgcn_s_sleep(1);
}
#define START_DLY 8000
#define OUT_SLP 16
#define SCHED() __builtin_amdgcn_sched_barrier(0)
#define PROJ_GEMM(ACC, WF) do { \
    bf16x8 A0_[4], A1_[4]; \
    _Pragma("unroll") for (int ks = 0; ks < 3; ++ks) { \
      A0_[ks] = *(const bf16x8*)(xs + r16 * XS + 32 * ks + 8 * g); A1_[ks] = *(const bf16x8*)(xs + (16 + r16) * XS + 32 * ks + 8 * g); } \
    _Pragma("unroll") for (int ks = 0; ks < 20; ++ks) { \
      if (ks + 3 < 20) { A0_[(ks + 3) & 3] = *(const bf16x8*)(xs + r16 * XS + 32 * (ks + 3) + 8 * g); \
                         A1_[(ks + 3) & 3] = *(const bf16x8*)(xs + (16 + r16) * XS + 32 * (ks + 3) + 8 * g); } \
      SCHED(); \
      _Pragma("unroll") for (int tn = 0; tn < 2; ++tn) { \
        ACC[0][tn] = mf16(A0_[ks & 3], WF[tn][ks], ks == 0 ? Z4 : ACC[0][tn]); \
        ACC[1][tn] = mf16(A1_[ks & 3], WF[tn][ks], ks == 0 ? Z4 : ACC[1][tn]); } \
      SCHED(); \
    } } while (0)

#define LDX(t_) do { _Pragma("unroll") for (int q = 0; q < 10; ++q) { int pc = q * 256 + tid; int row = pc / 80, cc = pc % 80; \
      xp[q] = *(const u32x4*)(p.x + (size_t)((t_) * 32 + row) * 640 + cc * 8); } } while (0)
#define STX() do { _Pragma("unroll") for (int q = 0; q < 10; ++q) { int pc = q * 256 + tid; int row = pc / 80, cc = pc % 80; \
      *(u32x4*)(xs + row * XS + cc * 8) = xp[q]; } } while (0)
constexpr int XS = 648;
constexpr int KS = 136;

struct Params {
  const u16* x; const u16* mem0; const u16* Wq; const u16* Wk; const u16* Wv; const u16* Wo;
  const u16* Wg; const u16* Wu; const u16* Wd; const u16* gw; u8* ws; u16* out;
};
constexpr int NCH = 10;
constexpr int NTASK = NSEG * NCH;

__global__ void __launch_bounds__(256, 1) k_main(Params p) {
  extern __shared__ __attribute__((aligned(16))) u8 smem[];
  u8* ws = p.ws;
  const int tid = threadIdx.x, w = __builtin_amdgcn_readfirstlane(tid >> 6), lane = tid & 63;
  const int r16 = lane & 15, g = lane >> 4, par = r16 >> 3, r = r16 & 7;
  const int idx = par ? 0xEEEE : 0x4444;
#define XMAP 2
  int b;
  {
    const int bx = blockIdx.x, vi = (bx >> 3) * 4 + (bx & 3);
    if (XMAP == 0) b = bx;
    else if (XMAP == 1) {
      if ((bx & 4) == 0) b = vi < 104 ? vi : 232 + (vi - 104);
      else b = 104 + vi;
    } else if (XMAP == 2) {
      if ((bx & 4) == 0) b = vi < 84 ? vi : vi < 108 ? 232 + (vi - 84) : 104 + (vi - 108);
      else b = vi < 20 ? 84 + vi : 124 + (vi - 20);
    } else {
      if ((bx & 4) == 0) b = vi < 32 ? vi : vi < 72 ? 64 + (vi - 32) : vi < 96 ? 232 + (vi - 72) : 104 + (vi - 96);
      else b = vi < 32 ? 32 + vi : 136 + (vi - 32);
    }
  }
  const __amdgpu_buffer_rsrc_t rs = __builtin_amdgcn_make_buffer_rsrc((void*)ws, 0, (int)OFF_END, RSRC_W3);
  unsigned* ctl = (unsigned*)(ws + OFF_CTL);
  const f32x4 Z4 = {0.f, 0.f, 0.f, 0.f};
  unsigned long long tanchor = 0;
  int dly = b < 32 ? D1 : b < 64 ? DV : D2;
#define AD_UPDATE() do { if (s > 0) { if (spins == 0) dly -= AD_DEC; else dly += AD_INC; if (dly < 0) dly = 0; } } while (0)
  const u32x4 PZ = {0xFFFFFFFFu, 0xFFFFFFFFu, 0xFFFFFFFFu, 0xFFFFFFFFu};
  u8* wl = smem + w * 40960;
  const unsigned wla = __builtin_amdgcn_readfirstlane((unsigned)(size_t)(LAS u8*)smem) + w * 40960;

  if (b < 32) {
    const int h = b;
    u16* sq = (u16*)smem;
    unsigned* spl = (unsigned*)(smem + 4608);
    u32x4* smem_a = (u32x4*)(smem + 8192);
    bf16x16 W[4][10];
    {
      const u8* src = (const u8*)((w < 2 ? p.Wq : p.Wk) + h * 128 + 64 * (w & 1));
#pragma unroll
      for (int i = 0; i < 5; ++i) dma_tile<64, 128>(src + (size_t)i * 64 * 8192, 8192, wla + i * 8192, lane);
#pragma unroll
      for (int i = 0; i < 10; ++i) {
        WAIT_VM(8 * (i < 5 ? 4 : 9 - i));
#pragma unroll
        for (int tn = 0; tn < 4; ++tn) W[tn][i] = frag_sparse(wl + (i % 5) * 8192, 128, 16 * tn, lane);
        if (i < 5) {
          WAIT_LGKM0();
          dma_tile<64, 128>(src + (size_t)(i + 5) * 64 * 8192, 8192, wla + i * 8192, lane);
        }
      }
      WAIT_LGKM0();
    }
#pragma unroll
    for (int tn = 0; tn < 4; ++tn)
#pragma unroll
      for (int i = 0; i < (tn < 2 ? 8 : 7); ++i) PIN_A(W[tn][i]);
    LDS_BARRIER();
    const int moff = (int)OFF_MEM + (2 * g + par) * 128 + r * 16;
    for (int s = 0; s < NSEG; ++s) {
      u32x4 kxf[2][4];
      if (w == 0 && s < NSEG - 1) {
        wait_flag(rs, CW_KXF + 16 * h, (unsigned)s, ctl, lane);
        const int kxo = (int)OFF_KX + (s * 32 + h) * 8192 + r16 * 256 + 16 * g;
#pragma unroll
        for (int t2 = 0; t2 < 2; ++t2)
#pragma unroll
          for (int kk = 0; kk < 4; ++kk) kxf[t2][kk] = ld128(rs, kxo + t2 * 4096 + 64 * kk);
      }
      f32x4 acc[4];
      unsigned spins = 0;
      if ((ADAPT & 1) && s > 0) wait_until(tanchor + dly);
      STAMP(0);
      for (;;) {
        asm volatile("" ::: "memory");
        u32x4 a0 = ld128(rs, moff + s * STEPB + w * 1024);
        u32x4 a1 = ld128(rs, moff + s * STEPB + (w + 4) * 1024);
        u32x4 a2 = ld128(rs, w < 2 ? moff + s * STEPB + (w + 8) * 1024 : OOB);
        asm volatile("" ::: "memory");
        smem_a[w * 64 + lane] = a0; smem_a[(w + 4) * 64 + lane] = a1; smem_a[(w + 8) * 64 + lane] = a2;
        unsigned mxv = umax3(umax3(a0.x, a0.z, a1.x), a1.z, umax3(a2.x, a2.z, 0u));
        if (__all(mxv != 0xFFFFFFFFu) || ++spins > SPIN_CAP) break;
      }
      LDS_BARRIER();
#pragma unroll
      for (int tn = 0; tn < 4; ++tn) { acc[tn] = Z4; PIN_V(acc[tn]); }
      {
        u32x4 af[10];
#pragma unroll
        for (int i = 0; i < 10; ++i) af[i] = smem_a[i * 64 + lane];
        SCHED();
#pragma unroll
        for (int i = 0; i < 10; ++i)
#pragma unroll
          for (int tn = 0; tn < 4; ++tn) acc[tn] = smf(__builtin_bit_cast(bf16x8, af[i]), W[tn][i], acc[tn], idx);
      }
      STAMP(1);
      u16* dst = sq + (w >> 1) * (8 * KS) + (4 * (g & 1)) * KS + 64 * (w & 1) + 16 * (lane >> 5) + r16;
#pragma unroll
      for (int pp = 0; pp < 2; ++pp) {
        float v0 = comb(acc[2 * pp][0], acc[2 * pp + 1][0]), v1 = comb(acc[2 * pp][1], acc[2 * pp + 1][1]);
        float v2 = comb(acc[2 * pp][2], acc[2 * pp + 1][2]), v3 = comb(acc[2 * pp][3], acc[2 * pp + 1][3]);
        unsigned p01 = pk2(v0, v1), p23 = pk2(v2, v3);
        dst[32 * pp] = (u16)p01; dst[32 * pp + KS] = (u16)(p01 >> 16);
        dst[32 * pp + 2 * KS] = (u16)p23; dst[32 * pp + 3 * KS] = (u16)(p23 >> 16);
      }
      LDS_BARRIER();
      STAMP(2);
      if (w == 0 && s < NSEG - 1) {
        f32x4 sc[3] = {Z4, Z4, Z4};
        bf16x8 qf[4], kf[4];
#pragma unroll
        for (int kk = 0; kk < 4; ++kk) qf[kk] = *(const bf16x8*)(sq + r * KS + 32 * kk + 8 * g);
#pragma unroll
        for (int kk = 0; kk < 4; ++kk) kf[kk] = *(const bf16x8*)(sq + 8 * KS + r * KS + 32 * kk + 8 * g);
        SCHED();
#pragma unroll
        for (int kk = 0; kk < 4; ++kk) {
          sc[1] = mf16(__builtin_bit_cast(bf16x8, kxf[0][kk]), qf[kk], sc[1]);
          sc[2] = mf16(__builtin_bit_cast(bf16x8, kxf[1][kk]), qf[kk], sc[2]);
          sc[0] = mf16(kf[kk], qf[kk], sc[0]);
        }
        const float NEG = -3.0e38f;
        float v[6];
        v[0] = par ? sc[1][2] : (g < 2 ? sc[0][0] : NEG);
        v[1] = par ? sc[1][3] : (g < 2 ? sc[0][1] : NEG);
        v[2] = par ? sc[2][0] : (g < 2 ? sc[0][2] : NEG);
        v[3] = par ? sc[2][1] : (g < 2 ? sc[0][3] : NEG);
        v[4] = par ? sc[2][2] : sc[1][0];
        v[5] = par ? sc[2][3] : sc[1][1];
        float mx = rmax3(rmax3(v[0], v[1], v[2]), rmax3(v[3], v[4], v[5]), v[0]);
        mx = xmax32(xmax16(xmax8(mx)));
        const float scale = 0.08838834764831845f;
        const float L2E = 1.4426950408889634f;
        float mr = rbf(rbf(mx) * scale) * L2E;
        float e[6];
#pragma unroll
        for (int q = 0; q < 3; ++q) {
          unsigned p1 = pk2(v[2 * q], v[2 * q + 1]);
          unsigned p2 = pk2(blo(p1) * scale, bhi(p1) * scale);
          e[2 * q] = __builtin_amdgcn_exp2f(fmaf(blo(p2), L2E, -mr));
          e[2 * q + 1] = __builtin_amdgcn_exp2f(fmaf(bhi(p2), L2E, -mr));
        }
        float sum = ((e[0] + e[1]) + (e[2] + e[3])) + (e[4] + e[5]);
        sum = xadd32(xadd16(xadd8(sum)));
        float inv = __builtin_amdgcn_rcpf(sum);
        unsigned o0 = pk2(e[0] * inv, e[1] * inv), o1 = pk2(e[2] * inv, e[3] * inv), o2 = pk2(e[4] * inv, e[5] * inv);
        unsigned* pl = spl + r * 4;
        int dA = 32 + (g >> 1) * 32 + 2 * (g & 1), dB = dA + 64;
        int d0 = par ? dA + 1 : 2 * g, d1 = par ? dB : 2 * g + 1, d2 = par ? dB + 1 : dA;
        if (par || g < 2) { pl[d0] = o0; pl[d1] = o1; }
        pl[d2] = o2;
        WAIT_LGKM0();
        if (lane < 40) {
          u32x4 o = *(const u32x4*)(spl + lane * 4);
          o.x = nosent(o.x); o.y = nosent(o.y); o.z = nosent(o.z); o.w = nosent(o.w);
          st128(o, rs, (int)OFF_P + s * STEPB + h * 640 + lane * 16);
          st128(PZ, rs, (int)OFF_P + (s + 2) * STEPB + h * 640 + lane * 16);
        }
      }
      if (w == 1) {
#pragma unroll
        for (int q = 0; q < 2; ++q) {
          int pc = q * 64 + lane, row = pc >> 4, ch = pc & 15;
          u32x4 o = *(const u32x4*)(sq + 8 * KS + row * KS + ch * 8);
          o.x = nosent(o.x); o.y = nosent(o.y); o.z = nosent(o.z); o.w = nosent(o.w);
          st128(o, rs, (int)OFF_KM + s * STEPB + h * 2048 + pc * 16);
          st128(PZ, rs, (int)OFF_KM + (s + 2) * STEPB + h * 2048 + pc * 16);
        }
      }
      STAMP(3);
      LDS_BARRIER();
      AD_UPDATE();
      tanchor = __builtin_amdgcn_s_memtime();
      STAMP_FLUSH();
    }
  } else if (b < 64) {
    const int h = b - 32;
    u16* sq = (u16*)smem;
    u32x4* smem_a = (u32x4*)(smem + 8192);
    bf16x16 Wv[2][10], Wo[10][2];
    {
      const u8* src = (const u8*)(p.Wv + h * 128 + 32 * w);
      const u8* so = (const u8*)(p.Wo + (size_t)h * 128 * 640 + 160 * w);
#pragma unroll
      for (int i = 0; i < 10; ++i) dma_tile<64, 64>(src + (size_t)i * 64 * 8192, 8192, wla + i * 4096, lane);
      WAIT_VM(20);
#pragma unroll
      for (int i = 0; i < 5; ++i)
#pragma unroll
        for (int tn = 0; tn < 2; ++tn) Wv[tn][i] = frag_sparse(wl + i * 4096, 64, 16 * tn, lane);
      WAIT_LGKM0();
      dma_tile<64, 320>(so, 1280, wla, lane);
      WAIT_VM(20);
#pragma unroll
      for (int i = 5; i < 10; ++i)
#pragma unroll
        for (int tn = 0; tn < 2; ++tn) Wv[tn][i] = frag_sparse(wl + i * 4096, 64, 16 * tn, lane);
      WAIT_LGKM0();
      dma_tile<64, 320>(so + (size_t)64 * 1280, 1280, wla + 20480, lane);
      WAIT_VM(20);
#pragma unroll
      for (int t2 = 0; t2 < 10; ++t2) Wo[t2][0] = frag_sparse(wl, 320, 16 * t2, lane);
      WAIT_VM(0);
#pragma unroll
      for (int t2 = 0; t2 < 10; ++t2) Wo[t2][1] = frag_sparse(wl + 20480, 320, 16 * t2, lane);
      WAIT_LGKM0();
    }
#pragma unroll
    for (int tn = 0; tn < 2; ++tn)
#pragma unroll
      for (int i = 0; i < 6; ++i) PIN_A(Wv[tn][i]);
#pragma unroll
    for (int t2 = 0; t2 < 10; ++t2)
#pragma unroll
      for (int i = 0; i < 2; ++i) PIN_A(Wo[t2][i]);
    LDS_BARRIER();
    const int moff = (int)OFF_MEM + (2 * g + par) * 128 + r * 16;
    const int ucol = 160 * w + 16 * (lane >> 5) + r16;
    const int uoff = (int)OFF_UM + h * 128 + (ucol >> 3) * 4096 + (ucol & 7) * 16 + 8 * (g & 1);
    for (int s = 0; s < NSEG; ++s) {
      f32x4 acc[2];
      unsigned spins = 0;
      if ((ADAPT & 2) && s > 0) wait_until(tanchor + dly);
      STAMP(0);
      for (;;) {
        asm volatile("" ::: "memory");
        u32x4 a0 = ld128(rs, moff + s * STEPB + w * 1024);
        u32x4 a1 = ld128(rs, moff + s * STEPB + (w + 4) * 1024);
        u32x4 a2 = ld128(rs, w < 2 ? moff + s * STEPB + (w + 8) * 1024 : OOB);
        asm volatile("" ::: "memory");
        smem_a[w * 64 + lane] = a0; smem_a[(w + 4) * 64 + lane] = a1; smem_a[(w + 8) * 64 + lane] = a2;
        unsigned mxv = umax3(umax3(a0.x, a0.z, a1.x), a1.z, umax3(a2.x, a2.z, 0u));
        if (__all(mxv != 0xFFFFFFFFu) || ++spins > SPIN_CAP) break;
      }
      LDS_BARRIER();
      acc[0] = Z4; acc[1] = Z4; PIN_V(acc[0]); PIN_V(acc[1]);
      {
        u32x4 af[10];
#pragma unroll
        for (int i = 0; i < 10; ++i) af[i] = smem_a[i * 64 + lane];
        SCHED();
#pragma unroll
        for (int i = 0; i < 10; ++i) {
          acc[0] = smf(__builtin_bit_cast(bf16x8, af[i]), Wv[0][i], acc[0], idx);
          acc[1] = smf(__builtin_bit_cast(bf16x8, af[i]), Wv[1][i], acc[1], idx);
        }
      }
      STAMP(1);
      u16* vs = sq + (s & 1) * (8 * KS);
      {
        u16* dst = vs + (4 * (g & 1)) * KS + 32 * w + 16 * (lane >> 5) + r16;
        float v0 = comb(acc[0][0], acc[1][0]), v1 = comb(acc[0][1], acc[1][1]);
        float v2 = comb(acc[0][2], acc[1][2]), v3 = comb(acc[0][3], acc[1][3]);
        unsigned p01 = pk2(v0, v1), p23 = pk2(v2, v3);
        dst[0] = (u16)p01; dst[KS] = (u16)(p01 >> 16); dst[2 * KS] = (u16)p23; dst[3 * KS] = (u16)(p23 >> 16);
      }
      f32x4 c[10];
#pragma unroll
      for (int t2 = 0; t2 < 10; ++t2) { c[t2] = Z4; PIN_V(c[t2]); }
      LDS_BARRIER();
      STAMP(2);
      bf16x8 a0 = *(const bf16x8*)(vs + r * KS + 8 * (2 * g + par));
      bf16x8 a1 = *(const bf16x8*)(vs + r * KS + 64 + 8 * (2 * g + par));
#pragma unroll
      for (int t2 = 0; t2 < 10; ++t2) c[t2] = smf(a0, Wo[t2][0], c[t2], idx);
#pragma unroll
      for (int t2 = 0; t2 < 10; ++t2) c[t2] = smf(a1, Wo[t2][1], c[t2], idx);
#pragma unroll
      for (int pp = 0; pp < 5; ++pp) {
        float v0 = comb(c[2 * pp][0], c[2 * pp + 1][0]), v1 = comb(c[2 * pp][1], c[2 * pp + 1][1]);
        float v2 = comb(c[2 * pp][2], c[2 * pp + 1][2]), v3 = comb(c[2 * pp][3], c[2 * pp + 1][3]);
        u32x2 o = {nosent(pk2(v0, v1)), nosent(pk2(v2, v3))};
        st64(o, rs, uoff + s * STEPB + pp * 4 * 4096);
      }
#pragma unroll
      for (int pp = 0; pp < 5; ++pp) st64(u32x2{0xFFFFFFFFu, 0xFFFFFFFFu}, rs, uoff + (s + 2) * STEPB + pp * 4 * 4096);
      STAMP(3);
      AD_UPDATE();
      tanchor = __builtin_amdgcn_s_memtime();
      STAMP_FLUSH();
    }
  } else if (b < 104) {
    const int c = b - 64;
    float* sred = (float*)smem;
    if (tid < 32) {
      int sh = tid >> 4, row = (tid >> 1) & 7, cp = tid & 1;
      u32x2 v = *(const u32x2*)(p.mem0 + row * 640 + c * 16 + sh * 8 + cp * 4);
      v.x = nosent(v.x); v.y = nosent(v.y);
      st64(v, rs, (int)OFF_MEM + c * 256 + tid * 8);
    }
    const int sl = 2 * c + par;
    for (int s = 0; s < NSEG - 1; ++s) {
      wait_flag(rs, CW_UXF + 16 * (8 * w + (lane & 7)), (unsigned)s, ctl, lane);
      const int uxo = (int)OFF_UX + ((s * 80 + sl) * 32 + 8 * w) * 512 + r * 64 + 16 * g;
      u32x4 bx[8];
#pragma unroll
      for (int i = 0; i < 8; ++i) bx[i] = ld128(rs, uxo + i * 512);
      const int pb = (r16 < 8) ? (int)OFF_P + s * STEPB + (8 * w) * 640 + r * 16 : OOB;
      const int ub = (int)OFF_UM + s * STEPB + sl * 4096 + (8 * w) * 128 + r * 16;
      f32x4 acc0, acc1;
      unsigned spins = 0;
      if ((ADAPT & 4) && s > 0) wait_until(tanchor + dly);
      STAMP(0);
      for (;;) {
        asm volatile("" ::: "memory");
        u32x4 pa[10], um[2];
#pragma unroll
        for (int i = 0; i < 2; ++i) um[i] = ld128(rs, ub + (4 * i + g) * 128);
#pragma unroll
        for (int i = 0; i < 2; ++i) pa[8 + i] = ld128(rs, pb + (4 * i + g) * 640);
#pragma unroll
        for (int i = 0; i < 8; ++i) pa[i] = ld128(rs, pb + i * 640 + 128 + 128 * g);
        asm volatile("" ::: "memory");
        unsigned mxv = umax3(um[0].x, um[0].z, 0u);
        mxv = umax3(mxv, um[1].x, um[1].z);
#pragma unroll
        for (int i = 0; i < 10; ++i) mxv = umax3(mxv, pa[i].x, pa[i].z);
        if (__all(mxv != 0xFFFFFFFFu) || spins > SPIN_CAP) {
          acc0 = mf16(__builtin_bit_cast(bf16x8, pa[8]), __builtin_bit_cast(bf16x8, um[0]), Z4);
          acc1 = mf16(__builtin_bit_cast(bf16x8, pa[9]), __builtin_bit_cast(bf16x8, um[1]), Z4);
#pragma unroll
          for (int i = 0; i < 8; i += 2) {
            acc0 = mf16(__builtin_bit_cast(bf16x8, pa[i]), __builtin_bit_cast(bf16x8, bx[i]), acc0);
            acc1 = mf16(__builtin_bit_cast(bf16x8, pa[i + 1]), __builtin_bit_cast(bf16x8, bx[i + 1]), acc1);
          }
          break;
        }
        ++spins;
      }
      STAMP(1);
      if (g < 2) {
#pragma unroll
        for (int j = 0; j < 4; ++j) sred[w * 128 + par * 64 + (4 * g + j) * 8 + r] = acc0[j] + acc1[j];
      }
      LDS_BARRIER();
      STAMP(2);
      if (w == 0) {
        const f32x4* sr = (const f32x4*)sred;
        if (lane < 32) {
          f32x4 a = sr[lane], b2 = sr[32 + lane], c2 = sr[64 + lane], d2 = sr[96 + lane];
          f32x4 t = (a + b2) + (c2 + d2);
          u32x2 o = {nosent(pk2(t[0], t[1])), nosent(pk2(t[2], t[3]))};
          st64(o, rs, (int)OFF_MEM + (s + 1) * STEPB + c * 256 + lane * 8);
          st64(u32x2{0xFFFFFFFFu, 0xFFFFFFFFu}, rs, (int)OFF_MEM + (s + 3) * STEPB + c * 256 + lane * 8);
        }
      }
      STAMP(3);
      LDS_BARRIER();
      AD_UPDATE();
      tanchor = __builtin_amdgcn_s_memtime();
      STAMP_FLUSH();
    }
  } else if (b < 136) {
    const int h = b - 104;
    bf16x8 Wk[2][20];
    u32x4 xp[10];
    LDX(0);
    asm volatile("" ::: "memory");
    {
      const u8* sk = (const u8*)(p.Wk + h * 128 + 32 * w);
#pragma unroll
      for (int i = 0; i < 10; ++i) dma_tile<64, 64>(sk + (size_t)i * 64 * 8192, 8192, wla + i * 4096, lane);
      WAIT_VM0();
#pragma unroll
      for (int ks = 0; ks < 20; ++ks)
#pragma unroll
        for (int tn = 0; tn < 2; ++tn) Wk[tn][ks] = frag_dense(wl, 64, 32 * ks, 16 * tn, lane);
      WAIT_LGKM0();
    }
#pragma unroll
    for (int tn = 0; tn < 2; ++tn)
#pragma unroll
      for (int ks = 0; ks < 20; ++ks) PIN_A(Wk[tn][ks]);
    LDS_BARRIER();
    u16* xs = (u16*)smem;
    u16* kb = (u16*)(smem + 41472);
    for (int t = 0; t < NSEG; ++t) {
      STAMP(0);
      STX();
      LDS_BARRIER();
      STAMP(1);
      LDX(t + 1 < NSEG ? t + 1 : 0);
      f32x4 acc[2][2];
      PROJ_GEMM(acc, Wk);
#pragma unroll
      for (int m = 0; m < 2; ++m)
#pragma unroll
        for (int tn = 0; tn < 2; ++tn) {
          u16* dst = kb + (8 * g + 4 * m) * KS + 32 * w + 16 * tn + r16;
          unsigned p01 = pk2(acc[m][tn][0], acc[m][tn][1]), p23 = pk2(acc[m][tn][2], acc[m][tn][3]);
          dst[0] = (u16)p01; dst[KS] = (u16)(p01 >> 16); dst[2 * KS] = (u16)p23; dst[3 * KS] = (u16)(p23 >> 16);
        }
      WAIT_VM(10);
      LDS_BARRIER();
      if (tid == 0 && t > 1) st32((unsigned)(t - 1), rs, (CW_KXF + 16 * h) * 4);
      STAMP(2);
#pragma unroll
      for (int q = 0; q < 2; ++q) {
        int pc = q * 256 + tid, row = pc >> 4, ch = pc & 15;
        u32x4 o = *(const u32x4*)(kb + row * KS + ch * 8);
        st128(o, rs, (int)OFF_KX + (t * 32 + h) * 8192 + pc * 16);
      }
      if (t == 0 || t == NSEG - 1) {
        WAIT_VM0();
        LDS_BARRIER();
        if (tid == 0) st32((unsigned)t, rs, (CW_KXF + 16 * h) * 4);
      }
      STAMP(3);
    }
  } else if (b < 168) {
    const int h = b - 136;
    wait_until(__builtin_amdgcn_s_memtime() + START_DLY);
    bf16x8 Wq[2][20];
    {
      const u8* sqp = (const u8*)(p.Wq + h * 128 + 32 * w);
#pragma unroll
      for (int i = 0; i < 10; ++i) dma_tile<64, 64>(sqp + (size_t)i * 64 * 8192, 8192, wla + i * 4096, lane);
      WAIT_VM0();
#pragma unroll
      for (int ks = 0; ks < 20; ++ks)
#pragma unroll
        for (int tn = 0; tn < 2; ++tn) Wq[tn][ks] = frag_dense(wl, 64, 32 * ks, 16 * tn, lane);
      WAIT_LGKM0();
    }
#pragma unroll
    for (int tn = 0; tn < 2; ++tn)
#pragma unroll
      for (int ks = 0; ks < 20; ++ks) PIN_A(Wq[tn][ks]);
    LDS_BARRIER();
    u16* xs = (u16*)smem;
    u16* kb = (u16*)(smem + 41472);
    u16* kmb = (u16*)(smem + 41472 + 8704);
    u8* pxb = smem + 41472 + 8704 + 2176;
    u32x4 xp[10];
    LDX(0);
    const float scale = 0.08838834764831845f;
    const float L2E = 1.4426950408889634f;
    int kflag = -1;
    u16* kxb = (u16*)(smem + 57344);
    {
      wait_flag(rs, CW_KXF + 16 * h, 0u, ctl, lane);
      u32x4 a = ld128(rs, (int)OFF_KX + h * 8192 + tid * 16), c2 = ld128(rs, (int)OFF_KX + h * 8192 + 4096 + tid * 16);
      *(u32x4*)(kxb + (tid >> 4) * KS + (tid & 15) * 8) = a;
      *(u32x4*)(kxb + (16 + (tid >> 4)) * KS + (tid & 15) * 8) = c2;
    }
    for (int u = 0; u < NSEG; ++u) {
      STAMP(0);
      STX();
      LDS_BARRIER();
      STAMP(1);
      const int un = u + 1 < NSEG ? u + 1 : u;
      if (kflag < un) {
        wait_flag(rs, CW_KXF + 16 * h, (unsigned)un, ctl, lane);
        kflag = __builtin_amdgcn_readfirstlane((int)ld32(rs, (CW_KXF + 16 * h) * 4));
      }
      u32x4 kn0 = ld128(rs, (int)OFF_KX + (un * 32 + h) * 8192 + tid * 16);
      u32x4 kn1 = ld128(rs, (int)OFF_KX + (un * 32 + h) * 8192 + 4096 + tid * 16);
      u32x4 k0 = ld128(rs, w == 3 ? (int)OFF_KM + u * STEPB + h * 2048 + lane * 16 : OOB);
      u32x4 k1 = ld128(rs, w == 3 ? (int)OFF_KM + u * STEPB + h * 2048 + 1024 + lane * 16 : OOB);
      LDX(u + 1 < NSEG ? u + 1 : 0);
      f32x4 acc[2][2];
      PROJ_GEMM(acc, Wq);
#pragma unroll
      for (int m = 0; m < 2; ++m)
#pragma unroll
        for (int tn = 0; tn < 2; ++tn) {
          u16* dst = kb + (16 * m + 4 * g) * KS + 32 * w + 16 * tn + r16;
          unsigned p01 = pk2(acc[m][tn][0], acc[m][tn][1]), p23 = pk2(acc[m][tn][2], acc[m][tn][3]);
          dst[0] = (u16)p01; dst[KS] = (u16)(p01 >> 16); dst[2 * KS] = (u16)p23; dst[3 * KS] = (u16)(p23 >> 16);
        }
      WAIT_VM(14);
      LDS_BARRIER();
      if (tid == 0 && u > 0 && u < NSEG - 1) st32((unsigned)(u - 1), rs, (CW_PXF + 16 * h) * 4);
      if (w == 3) {
        unsigned spins = 0;
        for (;;) {
          unsigned mxv = umax3(umax3(k0.x, k0.z, k1.x), k1.z, 0u);
          if (__all(mxv != 0xFFFFFFFFu) || spins > SPIN_CAP) {
            *(u32x4*)(kmb + (lane >> 4) * KS + (lane & 15) * 8) = k0;
            *(u32x4*)(kmb + (4 + (lane >> 4)) * KS + (lane & 15) * 8) = k1;
            break;
          }
          if (spins) __builtin_amdgcn_s_sleep(6);
          ++spins;
          asm volatile("" ::: "memory");
          k0 = ld128(rs, (int)OFF_KM + u * STEPB + h * 2048 + lane * 16);
          k1 = ld128(rs, (int)OFF_KM + u * STEPB + h * 2048 + 1024 + lane * 16);
          asm volatile("" ::: "memory");
        }
      }
      LDS_BARRIER();
      STAMP(2);
      {
        f32x4 sc[3];
        bf16x8 qf[4], kf[4];
#pragma unroll
        for (int kk = 0; kk < 4; ++kk) qf[kk] = *(const bf16x8*)(kb + (8 * w + r) * KS + 32 * kk + 8 * g);
#pragma unroll
        for (int kk = 0; kk < 4; ++kk) kf[kk] = *(const bf16x8*)(kmb + r * KS + 32 * kk + 8 * g);
        bf16x8 kxf[2][4];
        const u16* kxc = kxb + (u & 1) * (32 * KS);
#pragma unroll
        for (int t2 = 0; t2 < 2; ++t2)
#pragma unroll
          for (int kk = 0; kk < 4; ++kk) kxf[t2][kk] = *(const bf16x8*)(kxc + (16 * t2 + r16) * KS + 32 * kk + 8 * g);
        SCHED();
#pragma unroll
        for (int kk = 0; kk < 4; ++kk) {
          sc[1] = mf16(kxf[0][kk], qf[kk], kk == 0 ? Z4 : sc[1]);
          sc[2] = mf16(kxf[1][kk], qf[kk], kk == 0 ? Z4 : sc[2]);
          sc[0] = mf16(kf[kk], qf[kk], kk == 0 ? Z4 : sc[0]);
        }
        const int tok = 8 * w + r;
        const int tkA = 16 * (g & 1) + 4 * (g >> 1), tkB = tkA + 8;
        float v[6];
        int tk[6];
        v[0] = par ? sc[1][2] : sc[0][0]; tk[0] = par ? tkA + 2 : -1;
        v[1] = par ? sc[1][3] : sc[0][1]; tk[1] = par ? tkA + 3 : -1;
        v[2] = par ? sc[2][0] : sc[0][2]; tk[2] = par ? tkB : -1;
        v[3] = par ? sc[2][1] : sc[0][3]; tk[3] = par ? tkB + 1 : -1;
        v[4] = par ? sc[2][2] : sc[1][0]; tk[4] = par ? tkB + 2 : tkA;
        v[5] = par ? sc[2][3] : sc[1][1]; tk[5] = par ? tkB + 3 : tkA + 1;
        const bool nomem = !par && g >= 2;
        float sv[6];
#pragma unroll
        for (int q = 0; q < 3; ++q) {
          unsigned p1 = pk2(v[2 * q], v[2 * q + 1]);
          unsigned p2 = pk2(blo(p1) * scale, bhi(p1) * scale);
          sv[2 * q] = blo(p2); sv[2 * q + 1] = bhi(p2);
        }
        const float MASKV = rbf(-1.0e9f);
#pragma unroll
        for (int q = 0; q < 6; ++q) {
          if (tk[q] > tok) sv[q] = MASKV;
          if (q < 4 && nomem) sv[q] = -3.0e38f;
        }
        float mx = fmaxf(fmaxf(fmaxf(sv[0], sv[1]), fmaxf(sv[2], sv[3])), fmaxf(sv[4], sv[5]));
        mx = xmax32(xmax16(xmax8(mx)));
        float e[6];
#pragma unroll
        for (int q = 0; q < 6; ++q) e[q] = __builtin_amdgcn_exp2f((sv[q] - mx) * L2E);
        float sum = ((e[0] + e[1]) + (e[2] + e[3])) + (e[4] + e[5]);
        sum = xadd32(xadd16(xadd8(sum)));
        float inv = __builtin_amdgcn_rcpf(sum);
        unsigned o0 = pk2(e[0] * inv, e[1] * inv), o1 = pk2(e[2] * inv, e[3] * inv), o2 = pk2(e[4] * inv, e[5] * inv);
        unsigned* pl = (unsigned*)pxb + tok * 4;
        int dA = 128 + (g >> 1) * 128 + 2 * (g & 1), dB = dA + 256;
        int d0 = par ? dA + 1 : 2 * g, d1 = par ? dB : 2 * g + 1, d2 = par ? dB + 1 : dA;
        if (!nomem) { pl[d0] = o0; pl[d1] = o1; }
        pl[d2] = o2;
      }
      {
        u16* kxn = kxb + ((u + 1) & 1) * (32 * KS);
        *(u32x4*)(kxn + (tid >> 4) * KS + (tid & 15) * 8) = kn0;
        *(u32x4*)(kxn + (16 + (tid >> 4)) * KS + (tid & 15) * 8) = kn1;
      }
      LDS_BARRIER();
      if (tid < 160) {
        u32x4 o = *(const u32x4*)(pxb + tid * 16);
        st128(o, rs, (int)OFF_PX + (u * 32 + h) * 2560 + tid * 16);
      }
      if (u >= NSEG - 2) {
        WAIT_VM0();
        LDS_BARRIER();
        if (tid == 0) st32((unsigned)u, rs, (CW_PXF + 16 * h) * 4);
      }
    }
  } else if (b < 200) {
    const int h = b - 168;
    bf16x8 Wv[2][20], Wo[10][4];
    u32x4 xp[10];
    LDX(0);
    asm volatile("" ::: "memory");
    {
      const u8* sv = (const u8*)(p.Wv + h * 128 + 32 * w);
      const u8* so = (const u8*)(p.Wo + (size_t)h * 128 * 640 + 160 * w);
#pragma unroll
      for (int i = 0; i < 10; ++i) dma_tile<64, 64>(sv + (size_t)i * 64 * 8192, 8192, wla + i * 4096, lane);
      WAIT_VM(20);
#pragma unroll
      for (int ks = 0; ks < 10; ++ks)
#pragma unroll
        for (int tn = 0; tn < 2; ++tn) Wv[tn][ks] = frag_dense(wl, 64, 32 * ks, 16 * tn, lane);
      WAIT_LGKM0();
      dma_tile<64, 320>(so, 1280, wla, lane);
      WAIT_VM(20);
#pragma unroll
      for (int ks = 10; ks < 20; ++ks)
#pragma unroll
        for (int tn = 0; tn < 2; ++tn) Wv[tn][ks] = frag_dense(wl, 64, 32 * ks, 16 * tn, lane);
      WAIT_LGKM0();
      dma_tile<64, 320>(so + (size_t)64 * 1280, 1280, wla + 20480, lane);
      WAIT_VM(20);
#pragma unroll
      for (int ks = 0; ks < 2; ++ks)
#pragma unroll
        for (int t2 = 0; t2 < 10; ++t2) Wo[t2][ks] = frag_dense(wl, 320, 32 * ks, 16 * t2, lane);
      WAIT_VM(0);
#pragma unroll
      for (int ks = 2; ks < 4; ++ks)
#pragma unroll
        for (int t2 = 0; t2 < 10; ++t2) Wo[t2][ks] = frag_dense(wl, 320, 32 * ks, 16 * t2, lane);
      WAIT_LGKM0();
    }
#pragma unroll
    for (int t2 = 0; t2 < 10; ++t2)
#pragma unroll
      for (int ks = 0; ks < 4; ++ks) PIN_A(Wo[t2][ks]);
#pragma unroll
    for (int tn = 0; tn < 2; ++tn)
#pragma unroll
      for (int ks = 0; ks < 8; ++ks) PIN_A(Wv[tn][ks]);
    LDS_BARRIER();
    u16* xs = (u16*)smem;
    u16* vb = (u16*)(smem + 41472);
    for (int t = 0; t < NSEG; ++t) {
      STX();
      LDS_BARRIER();
      LDX(t + 1 < NSEG ? t + 1 : 0);
      f32x4 acc[2][2];
      PROJ_GEMM(acc, Wv);
#pragma unroll
      for (int m = 0; m < 2; ++m)
#pragma unroll
        for (int tn = 0; tn < 2; ++tn) {
          u16* dst = vb + (16 * m + 4 * g) * KS + 32 * w + 16 * tn + r16;
          unsigned p01 = pk2(acc[m][tn][0], acc[m][tn][1]), p23 = pk2(acc[m][tn][2], acc[m][tn][3]);
          dst[0] = (u16)p01; dst[KS] = (u16)(p01 >> 16); dst[2 * KS] = (u16)p23; dst[3 * KS] = (u16)(p23 >> 16);
        }
      WAIT_VM(10);
      LDS_BARRIER();
      if (tid == 0 && t > 1) st32((unsigned)(t - 1), rs, (CW_UXF + 16 * h) * 4);
      bf16x8 va[2][4];
#pragma unroll
      for (int m = 0; m < 2; ++m)
#pragma unroll
        for (int ks = 0; ks < 4; ++ks) va[m][ks] = *(const bf16x8*)(vb + (16 * m + r16) * KS + 32 * ks + 8 * g);
#pragma unroll
      for (int t2 = 0; t2 < 10; ++t2) {
        f32x4 c0 = Z4, c1 = Z4;
#pragma unroll
        for (int ks = 0; ks < 4; ++ks) { c0 = mf16(va[0][ks], Wo[t2][ks], c0); c1 = mf16(va[1][ks], Wo[t2][ks], c1); }
        int col = 160 * w + 16 * t2 + r16;
        u32x4 o = {pk2(c0[0], c0[1]), pk2(c0[2], c0[3]), pk2(c1[0], c1[1]), pk2(c1[2], c1[3])};
        st128(o, rs, (int)OFF_UX + ((t * 80 + (col >> 3)) * 32 + h) * 512 + (col & 7) * 64 + 16 * g);
      }
      if (t == 0 || t == NSEG - 1) {
        WAIT_VM0();
        LDS_BARRIER();
        if (tid == 0) st32((unsigned)t, rs, (CW_UXF + 16 * h) * 4);
      }
    }
  } else if (b < 232) {
    const int f = b - 200;
    wait_until(__builtin_amdgcn_s_memtime() + START_DLY);
    u8* slab0 = smem;
    const unsigned ldsb = __builtin_amdgcn_readfirstlane((unsigned)(size_t)(LAS u8*)smem);
    u8* wdb = smem + 65536;
    u16* hb = (u16*)(smem + 65536 + 81920);
    bf16x8 A[20];
#pragma unroll
    for (int ks = 0; ks < 20; ++ks)
      A[ks] = *(const bf16x8*)(p.x + (size_t)(64 * f + 16 * w + r16) * 640 + 32 * ks + 8 * g);
#pragma unroll
    for (int ks = 0; ks < 16; ++ks) PIN_A(A[ks]);
    f32x4 acc2[4][10];
#pragma unroll
    for (int m = 0; m < 4; ++m)
#pragma unroll
      for (int n = 0; n < 10; ++n) acc2[m][n] = Z4;
    const int i16 = lane & 15;
    unsigned so[2], tb[4], wo2[2], tw;
#pragma unroll
    for (int q = 0; q < 2; ++q) {
      int row = 8 * (2 * w + q) + (lane >> 3);
      so[q] = (unsigned)(row * (DF * 2) + (((lane & 7) ^ swz<0>(row)) << 4));
      wo2[q] = (unsigned)((lane >> 4) * 1280 + (((lane & 15) ^ (2 * ((lane >> 4) | (q << 2)))) << 4));
    }
#pragma unroll
    for (int q = 0; q < 4; ++q) {
      int s0 = swz<0>(8 * g + (i16 >> 2));
      tb[q] = ldsb + (unsigned)((8 * g + (i16 >> 2)) * 128 + (((2 * q + ((i16 & 3) >> 1)) ^ s0) << 4) + (i16 & 1) * 8);
    }
    const int s1 = 2 * ((i16 >> 2) | ((g & 1) << 2));
    tw = ldsb + 65536 + (unsigned)((8 * g + (i16 >> 2)) * 256 + ((i16 & 3) >> 1) * 16 + (i16 & 1) * 8);
#define FFN_SLAB(c_, si_, slot_) do { \
      const u8* sg_ = (const u8*)(Wg_ + (size_t)(64 * (si_)) * DF + 64 * (c_)); \
      const u8* su_ = (const u8*)(Wu_ + (size_t)(64 * (si_)) * DF + 64 * (c_)); \
      unsigned bb_ = ldsb + (slot_) * 16384 + 2 * wq * 1024; \
      glds16_s(sg_, so[0], bb_); glds16_s(sg_, so[1], bb_ + 1024); \
      glds16_s(su_, so[0], bb_ + 8192); glds16_s(su_, so[1], bb_ + 8192 + 1024); } while (0)
#define FFN_WD(si_) ((si_) < 0 ? 0 : (si_) < 6 ? 3 : (si_) == 6 ? 2 : 0)
    WAIT_VM0();
    const u16* Wg_ = p.Wg; const u16* Wu_ = p.Wu; int wq = w;
    const int c0 = f % 27;
    FFN_SLAB(c0, 0, 0); FFN_SLAB(c0, 1, 1); FFN_SLAB(c0, 2, 2);
#pragma unroll 1
    for (int ci = 0; ci < 27; ++ci) {
      const int c = c0 + ci < 27 ? c0 + ci : c0 + ci - 27;
      const int cn = c + 1 < 27 ? c + 1 : 0;
      const int slotb = (ci * 10) & 3;
      const u8* sd = (const u8*)(p.Wd + (size_t)(64 * c) * 640);
      f32x4 ag[4], au[4];
#pragma unroll
      for (int si = 0; si < 10; ++si) {
        asm volatile("s_waitcnt vmcnt(%0)" ::"n"(FFN_WD(si - 3) + 4 + FFN_WD(si - 2) + 4 + FFN_WD(si - 1)) : "memory");
        LDS_BARRIER();
        asm volatile("" : "+s"(Wg_), "+s"(Wu_), "+s"(wq));
        if (si + 3 < 10) FFN_SLAB(c, si + 3, (slotb + si + 3) & 3);
        else FFN_SLAB(cn, si + 3 - 10, (slotb + si + 3) & 3);
#pragma unroll
        for (int q = 0; q < FFN_WD(si); ++q) {
          const int P = 20 * wq + 3 * si + q;
          glds16_s(sd + (P & 15) * 4 * 1280 + (P >> 4) * 256, (P & 2) ? wo2[1] : wo2[0], ldsb + 65536 + P * 1024);
        }
        const unsigned sof = ((slotb + si) & 3) * 16384;
#pragma unroll
        for (int k2 = 0; k2 < 2; ++k2) {
          const int ks = si * 2 + k2;
          bf16x8 bg[4], bu[4];
#pragma unroll
          for (int ft = 0; ft < 4; ++ft) {
            const unsigned t0 = tb[ft] + sof + k2 * 4096;
            bg[ft] = trfrag(t0, t0 + 512);
            bu[ft] = trfrag(t0 + 8192, t0 + 8192 + 512);
          }
#pragma unroll
          for (int ft = 0; ft < 4; ++ft) {
            ag[ft] = mf16(A[ks], bg[ft], ks == 0 ? Z4 : ag[ft]);
            au[ft] = mf16(A[ks], bu[ft], ks == 0 ? Z4 : au[ft]);
          }
        }
      }
#pragma unroll
      for (int ft = 0; ft < 4; ++ft)
#pragma unroll
        for (int j = 0; j < 4; ++j) {
          float gv = rbf(ag[ft][j]), uv = rbf(au[ft][j]);
          hb[(16 * w + 4 * g + j) * 72 + 16 * ft + r16] = f2bf(rbf(gelu_tanh(gv)) * uv);
        }
      asm volatile("s_waitcnt vmcnt(12)" ::: "memory");
      LDS_BARRIER();
#pragma unroll
      for (int k2 = 0; k2 < 2; ++k2) {
        bf16x8 ha[4];
#pragma unroll
        for (int m = 0; m < 4; ++m) ha[m] = *(const bf16x8*)(hb + (16 * m + r16) * 72 + 32 * k2 + 8 * g);
#pragma unroll
        for (int n = 0; n < 10; ++n) {
          const int nc = 160 * wq + 16 * n;
          const unsigned ta = tw + (nc >> 7) * 16384 + ((((nc & 127) >> 3) ^ s1) << 4) + k2 * 8192;
          bf16x8 bd = trfrag(ta, ta + 1024);
#pragma unroll
          for (int m = 0; m < 4; ++m) acc2[m][n] = mf16(ha[m], bd, acc2[m][n]);
        }
      }
      LDS_BARRIER();
    }
    WAIT_VM0();
    LDS_BARRIER();
    {
      u16* ob = (u16*)smem;
#pragma unroll
      for (int m = 0; m < 4; ++m)
#pragma unroll
        for (int n = 0; n < 10; ++n)
#pragma unroll
          for (int j = 0; j < 4; ++j) ob[(16 * m + 4 * g + j) * XS + 160 * w + 16 * n + r16] = f2bf(acc2[m][n][j]);
      LDS_BARRIER();
      for (int pc = tid; pc < 64 * 80; pc += 256) {
        int row = pc / 80, cc = pc % 80;
        u32x4 o = *(const u32x4*)(ob + row * XS + cc * 8);
        st128(o, rs, (int)OFF_BO + (64 * f + row) * 1280 + cc * 16);
      }
      WAIT_VM0();
      LDS_BARRIER();
      unsigned* smx = (unsigned*)(smem + 90112);
      if (tid == 0) {
        st32(0u, rs, (CW_FF + 16 * f) * 4);
        *smx = atomicAnd(ctl + CW_MIX + 16 * f, 0x7FFFFFFFu);
      }
      LDS_BARRIER();
      const unsigned done = ~(*smx) & 0xFFFFFu;
      if (done) {
        const int row = tid >> 3, c8 = tid & 7;
        float omg[2];
#pragma unroll
        for (int hs = 0; hs < 2; ++hs) omg[hs] = rbf(1.0f - gate8(p.x + (size_t)(64 * f + 32 * hs + row) * 640, p.gw, c8));
        for (int k = 0; k < 20; ++k) {
          if (!((done >> k) & 1)) continue;
          const int hs = k / NCH, cj = k % NCH;
          const int tr = 32 * hs + row, cc = cj * 64 + c8 * 8;
          u32x4 ga = ld128(rs, (int)OFF_GA + ((64 * f + tr) * 640 + cc) * 2);
          u32x4 bv = *(const u32x4*)(ob + tr * XS + cc);
          const float om = hs ? omg[1] : omg[0];
          u32x4 o;
#pragma unroll
          for (int e = 0; e < 4; ++e)
            o[e] = pk2(blo(ga[e]) + rbf(om * blo(bv[e])), bhi(ga[e]) + rbf(om * bhi(bv[e])));
          *(u32x4*)(p.out + (size_t)(64 * f + tr) * 640 + cc) = o;
        }
      }
    }
  }

  if (b >= 104 && !(b >= 136 && b < 168)) {
    int tid = threadIdx.x;
    asm volatile("" : "+v"(tid));
    const int lane = tid & 63, r16 = lane & 15, g = lane >> 4;
    float* sg = (float*)smem;
    int* stask = (int*)(smem + 256);
    for (;;) {
      LDS_BARRIER();
      if (tid == 0) *stask = (int)atomicAdd(ctl + CW_QUEUE, 1u) + 1;
      LDS_BARRIER();
      const int task = *stask;
      if (task >= NTASK) break;
      const int t = task / NCH, cj = task % NCH;
      STAMP(0);
      {
        float gv = gate8(p.x + (size_t)(t * 32 + (tid >> 3)) * 640, p.gw, tid & 7);
        if ((tid & 7) == 0) sg[tid >> 3] = gv;
      }
      if (w == 0) wait_flag<OUT_SLP>(rs, CW_UXF + 16 * (lane & 31), (unsigned)t, ctl, lane);
      LDS_BARRIER();
      STAMP(1);
      const int col = cj * 64 + w * 16 + r16;
      const int uxo = (int)OFF_UX + (t * 80 + (col >> 3)) * 32 * 512 + (col & 7) * 64 + 16 * g;
      const int umo = (int)OFF_UM + t * STEPB + (col >> 3) * 4096 + (col & 7) * 16;
      u32x4 bx[32];
#pragma unroll
      for (int h = 0; h < 32; ++h) bx[h] = ld128(rs, uxo + h * 512);
      if (w == 0) wait_flag<OUT_SLP>(rs, CW_PXF + 16 * (lane & 31), (unsigned)t, ctl, lane);
      LDS_BARRIER();
      u8* pxl = smem + 1024;
      {
        const u8* pxg = ws + OFF_PX + (size_t)t * 32 * 2560;
#pragma unroll
        for (int q = 0; q < 20; ++q)
          __builtin_amdgcn_global_load_lds((const unsigned*)(pxg + (20 * w + q) * 1024 + lane * 16), (unsigned*)(pxl + (20 * w + q) * 1024), 16, 0, AUX_SC1);
      }
      u32x4 um[8];
      {
        unsigned spins = 0;
        for (;;) {
          asm volatile("" ::: "memory");
          unsigned mxv = 0;
#pragma unroll
          for (int hq = 0; hq < 8; ++hq) um[hq] = ld128(rs, umo + (4 * hq + g) * 128);
#pragma unroll
          for (int hq = 0; hq < 8; ++hq) mxv = umax3(mxv, um[hq].x, um[hq].z);
          if (__all(mxv != 0xFFFFFFFFu) || spins > SPIN_CAP) break;
          __builtin_amdgcn_s_sleep(4);
          ++spins;
        }
      }
      WAIT_VM0();
      LDS_BARRIER();
      f32x4 acc0 = Z4, acc1 = Z4;
#pragma unroll
      for (int h = 0; h < 32; ++h) {
        bf16x8 a0 = *(const bf16x8*)(pxl + h * 2560 + 512 + g * 512 + r16 * 16);
        bf16x8 a1 = *(const bf16x8*)(pxl + h * 2560 + 512 + g * 512 + 256 + r16 * 16);
        acc0 = mf16(a0, __builtin_bit_cast(bf16x8, bx[h]), acc0);
        acc1 = mf16(a1, __builtin_bit_cast(bf16x8, bx[h]), acc1);
      }
#pragma unroll
      for (int hq = 0; hq < 8; ++hq) {
        bf16x8 a0 = *(const bf16x8*)(pxl + (4 * hq + g) * 2560 + r16 * 16);
        bf16x8 a1 = *(const bf16x8*)(pxl + (4 * hq + g) * 2560 + 256 + r16 * 16);
        acc0 = mf16(a0, __builtin_bit_cast(bf16x8, um[hq]), acc0);
        acc1 = mf16(a1, __builtin_bit_cast(bf16x8, um[hq]), acc1);
      }
      STAMP(2);
      u16* rt = (u16*)(smem + 512 + 83968);
#pragma unroll
      for (int i = 0; i < 2; ++i)
#pragma unroll
        for (int q = 0; q < 4; ++q) {
          int tok = 16 * i + 4 * g + q;
          rt[tok * 72 + w * 16 + r16] = f2bf(sg[tok] * rbf(i == 0 ? acc0[q] : acc1[q]));
        }
      if (tid == 0) stask[1] = (int)ld32(rs, (CW_FF + 16 * (t >> 1)) * 4);
      LDS_BARRIER();
      {
        const int row = tid >> 3, c8 = tid & 7;
        const int go = ((t * 32 + row) * 640 + cj * 64 + c8 * 8) * 2;
        u32x4 ga = *(const u32x4*)(rt + row * 72 + c8 * 8);
        bool mix = stask[1] >= 0;
        if (!mix) {
          st128(ga, rs, (int)OFF_GA + go);
          WAIT_VM0();
          LDS_BARRIER();
          if (tid == 0) stask[2] = (int)atomicAnd(ctl + CW_MIX + 16 * (t >> 1), ~(1u << ((t & 1) * NCH + cj)));
          LDS_BARRIER();
          mix = stask[2] >= 0;
        }
        if (mix) {
          u32x4 bv = ld128(rs, (int)OFF_BO + go);
          const float om = rbf(1.0f - sg[row]);
          u32x4 o;
#pragma unroll
          for (int e = 0; e < 4; ++e)
            o[e] = pk2(blo(ga[e]) + rbf(om * blo(bv[e])), bhi(ga[e]) + rbf(om * bhi(bv[e])));
          *(u32x4*)((u8*)p.out + go) = o;
        }
      }
    }
  }
}

extern "C" void kernel_launch(void* const* d_in, const int* in_sizes, int n_in, void* d_out, int out_size, void* d_ws,
                              size_t ws_size, hipStream_t stream) {
  u8* ws = (u8*)d_ws;
  Params p;
  p.x = (const u16*)d_in[0]; p.mem0 = (const u16*)d_in[1]; p.Wq = (const u16*)d_in[2]; p.Wk = (const u16*)d_in[3];
  p.Wv = (const u16*)d_in[4]; p.Wo = (const u16*)d_in[5];
  p.Wg = (const u16*)d_in[8]; p.Wu = (const u16*)d_in[9]; p.Wd = (const u16*)d_in[10]; p.gw = (const u16*)d_in[11];
  p.ws = ws; p.out = (u16*)d_out;

  (void)hipFuncSetAttribute((const void*)k_main, hipFuncAttributeMaxDynamicSharedMemorySize, 163840);
  (void)hipMemsetAsync(ws, 0xFF, INIT_BYTES, stream);
  k_main<<<256, 256, 163840, stream>>>(p);
}
```

```cpp
#include <hip/hip_runtime.h>
#include <hip/hip_bf16.h>
#include <stdint.h>

typedef __bf16 bf16x8 __attribute__((ext_vector_type(8)));
typedef __bf16 bf16x16 __attribute__((ext_vector_type(16)));
typedef __bf16 bf16x2 __attribute__((ext_vector_type(2)));
typedef float f32x2 __attribute__((ext_vector_type(2)));
typedef float f32x4 __attribute__((ext_vector_type(4)));
typedef unsigned u32x4 __attribute__((ext_vector_type(4)));
typedef unsigned u32x2 __attribute__((ext_vector_type(2)));
typedef unsigned u32x8 __attribute__((ext_vector_type(8)));
typedef short s16x4 __attribute__((ext_vector_type(4)));
typedef unsigned short u16;
typedef unsigned char u8;

constexpr int SEQ = 2048, DM = 640, NSEG = 64, DF = 1728;

constexpr size_t OFF_CTL = 0;
constexpr size_t CTL_BYTES = 16384;
constexpr int STEPB = 10240 + 20480 + 65536 + 327680;
constexpr size_t OFF_MEM = CTL_BYTES;
constexpr size_t OFF_P = OFF_MEM + 10240;
constexpr size_t OFF_KM = OFF_P + 20480;
constexpr size_t OFF_UM = OFF_KM + 65536;
constexpr size_t OFF_SENT_END = CTL_BYTES + (size_t)66 * STEPB;
constexpr size_t INIT_BYTES = CTL_BYTES + (size_t)3 * STEPB;
constexpr size_t OFF_KX = OFF_SENT_END;
constexpr size_t OFF_UX = OFF_KX + (size_t)SEQ * 4096 * 2;
constexpr size_t OFF_PX = OFF_UX + (size_t)64 * 80 * 32 * 8 * 32 * 2;
constexpr size_t OFF_BO = OFF_PX + (size_t)64 * 32 * 2560;
constexpr size_t OFF_GA = OFF_BO + (size_t)SEQ * DM * 2;
constexpr size_t OFF_END = OFF_GA + (size_t)SEQ * DM * 2;
constexpr int CW_TMO = 0, CW_QUEUE = 32, CW_KXF = 64, CW_UXF = 1024, CW_PXF = 2048, CW_FF = 3072, CW_MIX = 3584;

#define AUX_SC1 16
#define RSRC_W3 0x00020000
#define OOB 0x7FFF0000

__device__ __forceinline__ float bf2f(u16 v) { return __uint_as_float(((unsigned)v) << 16); }
__device__ __forceinline__ u16 f2bf(float f) { __bf16 b = (__bf16)f; return __builtin_bit_cast(u16, b); }
__device__ __forceinline__ float rbf(float f) { return (float)((__bf16)f); }
__device__ __forceinline__ unsigned pk2(float a, float b) {
  f32x2 v = {a, b};
  return __builtin_bit_cast(unsigned, __builtin_convertvector(v, bf16x2));
}
__device__ __forceinline__ unsigned nosent(unsigned x) { return x < 0xFFFFFFFEu ? x : 0xFFFFFFFEu; }
__device__ __forceinline__ f32x4 smf(bf16x8 a, bf16x16 b, f32x4 c, int idx) {
  return __builtin_amdgcn_smfmac_f32_16x16x64_bf16(a, b, c, idx, 0, 0);
}
__device__ __forceinline__ f32x4 mf16(bf16x8 a, bf16x8 b, f32x4 c) {
  return __builtin_amdgcn_mfma_f32_16x16x32_bf16(a, b, c, 0, 0, 0);
}
__device__ __forceinline__ float comb(float a, float b) {
  auto r = __builtin_amdgcn_permlane32_swap(__float_as_uint(a), __float_as_uint(b), false, false);
  return __uint_as_float(r[0]) + __uint_as_float(r[1]);
}
__device__ __forceinline__ u32x4 ld128(__amdgpu_buffer_rsrc_t rs, int off) {
  return __builtin_amdgcn_raw_buffer_load_b128(rs, off, 0, AUX_SC1);
}
__device__ __forceinline__ unsigned ld32(__amdgpu_buffer_rsrc_t rs, int off) {
  return __builtin_amdgcn_raw_buffer_load_b32(rs, off, 0, AUX_SC1);
}
__device__ __forceinline__ void st128(u32x4 v, __amdgpu_buffer_rsrc_t rs, int off) {
  __builtin_amdgcn_raw_buffer_store_b128(v, rs, off, 0, AUX_SC1);
}
__device__ __forceinline__ void st64(u32x2 v, __amdgpu_buffer_rsrc_t rs, int off) {
  __builtin_amdgcn_raw_buffer_store_b64(v, rs, off, 0, AUX_SC1);
}
__device__ __forceinline__ void st32(unsigned v, __amdgpu_buffer_rsrc_t rs, int off) {
  __builtin_amdgcn_raw_buffer_store_b32(v, rs, off, 0, AUX_SC1);
}

constexpr unsigned SPIN_CAP = 1u << 16;
#define STAMP(slot) do { } while (0)
#define STAMP_FLUSH() do { } while (0)
#define LDS_BARRIER() do { asm volatile("s_waitcnt lgkmcnt(0)" ::: "memory"); __builtin_amdgcn_s_barrier(); asm volatile("" ::: "memory"); } while (0)
#define WAIT_VM0() asm volatile("s_waitcnt vmcnt(0)" ::: "memory")
#define WAIT_LGKM0() asm volatile("s_waitcnt lgkmcnt(0)" ::: "memory")
#define PIN_A(x) asm volatile("" : "+a"(x))
#define PIN_V(x) asm volatile("" : "+v"(x))
#define LAS __attribute__((address_space(3)))

__device__ __forceinline__ float rmax3(float a, float b, float c) {
  float r; asm("v_max3_f32 %0, %1, %2, %3\n\ts_nop 1" : "=v"(r) : "v"(a), "v"(b), "v"(c)); return r;
}
__device__ __forceinline__ float rmax2(float a, float b) {
  float r; asm("v_max_f32 %0, %1, %2\n\ts_nop 1" : "=v"(r) : "v"(a), "v"(b)); return r;
}
__device__ __forceinline__ float xmax8(float v) {
  int o = __builtin_amdgcn_update_dpp(0, __float_as_int(v), 0x128, 0xf, 0xf, false);
  return rmax2(v, __int_as_float(o));
}
__device__ __forceinline__ float xadd8(float v) {
  int o = __builtin_amdgcn_update_dpp(0, __float_as_int(v), 0x128, 0xf, 0xf, false);
  return v + __int_as_float(o);
}
__device__ __forceinline__ float xmax16(float v) {
  auto r = __builtin_amdgcn_permlane16_swap(__float_as_uint(v), __float_as_uint(v), false, false);
  return rmax2(__uint_as_float(r[0]), __uint_as_float(r[1]));
}
__device__ __forceinline__ float xadd16(float v) {
  auto r = __builtin_amdgcn_permlane16_swap(__float_as_uint(v), __float_as_uint(v), false, false);
  return __uint_as_float(r[0]) + __uint_as_float(r[1]);
}
__device__ __forceinline__ float xmax32(float v) {
  auto r = __builtin_amdgcn_permlane32_swap(__float_as_uint(v), __float_as_uint(v), false, false);
  return rmax2(__uint_as_float(r[0]), __uint_as_float(r[1]));
}
__device__ __forceinline__ float xadd32(float v) {
  auto r = __builtin_amdgcn_permlane32_swap(__float_as_uint(v), __float_as_uint(v), false, false);
  return __uint_as_float(r[0]) + __uint_as_float(r[1]);
}
__device__ __forceinline__ unsigned umax3(unsigned a, unsigned b, unsigned c) { return max(max(a, b), c); }
__device__ __forceinline__ float blo(unsigned p) { return __uint_as_float(p << 16); }
__device__ __forceinline__ float bhi(unsigned p) { return __uint_as_float(p & 0xFFFF0000u); }

__device__ __forceinline__ void glds16_asm(const void* gsrc, unsigned lds_dst) {
  unsigned keep;
  asm volatile("s_mov_b32 %0, m0\n\ts_mov_b32 m0, %2\n\ts_nop 0\n\tglobal_load_lds_dwordx4 %1, off\n\ts_mov_b32 m0, %0"
               : "=&s"(keep) : "v"(gsrc), "s"(lds_dst) : "memory");
}
template <int R, int CB>
__device__ __forceinline__ void dma_tile(const u8* src, int ldb, unsigned lds, int lane) {
  constexpr int CPR = CB / 16;
  constexpr int N = R * CPR / 64;
#pragma unroll
  for (int q = 0; q < N; ++q) {
    int idx = q * 64 + lane;
    int row = idx / CPR, ch = idx % CPR;
    glds16_asm(src + (size_t)row * ldb + ch * 16, lds + q * 1024);
  }
}
#define WAIT_VM(n) asm volatile("s_waitcnt vmcnt(%0)" ::"n"(n) : "memory")
__device__ __forceinline__ u32x2 tr4(const u8* tile, int rsb, int k0, int n0, int lane) {
  int i = lane & 15;
  const u8* p = tile + (k0 + (i >> 2)) * rsb + (n0 + 4 * (i & 3)) * 2;
  s16x4 v = __builtin_amdgcn_ds_read_tr16_b64_v4i16((LAS s16x4*)p);
  return __builtin_bit_cast(u32x2, v);
}
__device__ __forceinline__ bf16x16 frag_sparse(const u8* blk, int rsb, int n0, int lane) {
  int gb = lane >> 4;
  int kb = 16 * (gb >> 1) + 4 * (gb & 1);
  u32x2 t00 = tr4(blk, rsb, kb, n0, lane), t01 = tr4(blk, rsb, kb + 8, n0, lane);
  u32x2 t10 = tr4(blk, rsb, kb + 32, n0, lane), t11 = tr4(blk, rsb, kb + 40, n0, lane);
  u32x8 d = {t00.x, t01.x, t00.y, t01.y, t10.x, t11.x, t10.y, t11.y};
  return __builtin_bit_cast(bf16x16, d);
}
__device__ __forceinline__ bf16x8 frag_dense(const u8* blk, int rsb, int k0, int n0, int lane) {
  int g = lane >> 4;
  u32x2 a = tr4(blk, rsb, k0 + 8 * g, n0, lane), b = tr4(blk, rsb, k0 + 8 * g + 4, n0, lane);
  u32x4 d = {a.x, a.y, b.x, b.y};
  return __builtin_bit_cast(bf16x8, d);
}

template <int MODE> __device__ __forceinline__ int swz(int row) {
  return MODE == 0 ? 2 * (((row >> 1) & 1) | (((row >> 3) & 1) << 1)) : 2 * ((row & 3) | (((row >> 3) & 1) << 2));
}
__device__ __forceinline__ void glds16_s(const void* sbase, unsigned voff, unsigned lds_dst) {
  unsigned keep;
  asm volatile("s_mov_b32 %0, m0\n\ts_mov_b32 m0, %3\n\ts_nop 0\n\tglobal_load_lds_dwordx4 %1, %2\n\ts_mov_b32 m0, %0"
               : "=&s"(keep) : "v"(voff), "s"(sbase), "s"(lds_dst) : "memory");
}
#define GLDS_I(IMM, sbase, voff, lds_base) \
  asm volatile("s_add_u32 m0, %2, %3\n\ts_nop 0\n\tglobal_load_lds_dwordx4 %0, %1" \
               :: "v"(voff), "s"(sbase), "s"(lds_base), "n"(IMM) : "memory", "scc")
__device__ __forceinline__ u32x2 trr(unsigned addr) {
  s16x4 v = __builtin_amdgcn_ds_read_tr16_b64_v4i16((LAS s16x4*)(size_t)addr);
  return __builtin_bit_cast(u32x2, v);
}
__device__ __forceinline__ bf16x8 trfrag(unsigned a0, unsigned a1) {
  u32x2 a = trr(a0), b = trr(a1);
  u32x4 d = {a.x, a.y, b.x, b.y};
  return __builtin_bit_cast(bf16x8, d);
}
__device__ __forceinline__ float gelu_tanh(float x) {
  float u = 0.7978845608028654f * (x + 0.044715f * x * x * x);
  return x * __builtin_amdgcn_rcpf(1.0f + __builtin_amdgcn_exp2f(-2.8853900817779268f * u));
}

__device__ __forceinline__ float gate8(const u16* xrow, const u16* gw, int part) {
  float sacc = 0.f;
#pragma unroll
  for (int q = 0; q < 10; ++q) {
    u32x4 xv = *(const u32x4*)(xrow + part * 8 + q * 64), gv = *(const u32x4*)(gw + part * 8 + q * 64);
#pragma unroll
    for (int e = 0; e < 4; ++e) {
      sacc += rbf(blo(xv[e]) * blo(gv[e]));
      sacc += rbf(bhi(xv[e]) * bhi(gv[e]));
    }
  }
  sacc += __shfl_xor(sacc, 1); sacc += __shfl_xor(sacc, 2); sacc += __shfl_xor(sacc, 4);
  float z = rbf(sacc);
  return rbf(1.0f / (1.0f + __expf(-z)));
}

template <int SLP = 2>
__device__ __forceinline__ void wait_flag(__amdgpu_buffer_rsrc_t rs, int cw, unsigned v, unsigned* ctl, int lane) {
  for (unsigned spins = 0;; ++spins) {
    asm volatile("" ::: "memory");
    int f = (int)ld32(rs, cw * 4);
    if (__all(f >= (int)v)) break;
    if (spins > SPIN_CAP) { if (lane == 0) atomicAdd(ctl, 1u); break; }
    __builtin_amdgcn_s_sleep(SLP);
  }
  asm volatile("" ::: "memory");
}

#define ADAPT 7
#define AD_DEC 16
#define AD_INC 64
#define D1 3800
#define DV 4300
#define D2 4800
__device__ __forceinline__ void wait_until(unsigned long long t) {
  while (__builtin_amdgcn_s_memtime() < t) __builtin_amdgcn_s_sleep(1);
}
#define START_DLY 8000
#define OUT_SLP 16
#define SCHED() __builtin_amdgcn_sched_barrier(0)
#define PROJ_GEMM(ACC, WF) do { \
    bf16x8 A0_[4], A1_[4]; \
    _Pragma("unroll") for (int ks = 0; ks < 3; ++ks) { \
      A0_[ks] = *(const bf16x8*)(xs + r16 * XS + 32 * ks + 8 * g); A1_[ks] = *(const bf16x8*)(xs + (16 + r16) * XS + 32 * ks + 8 * g); } \
    _Pragma("unroll") for (int ks = 0; ks < 20; ++ks) { \
      if (ks + 3 < 20) { A0_[(ks + 3) & 3] = *(const bf16x8*)(xs + r16 * XS + 32 * (ks + 3) + 8 * g); \
                         A1_[(ks + 3) & 3] = *(const bf16x8*)(xs + (16 + r16) * XS + 32 * (ks + 3) + 8 * g); } \
      SCHED(); \
      _Pragma("unroll") for (int tn = 0; tn < 2; ++tn) { \
        ACC[0][tn] = mf16(A0_[ks & 3], WF[tn][ks], ks == 0 ? Z4 : ACC[0][tn]); \
        ACC[1][tn] = mf16(A1_[ks & 3], WF[tn][ks], ks == 0 ? Z4 : ACC[1][tn]); } \
      SCHED(); \
    } } while (0)

#define LDX(t_) do { _Pragma("unroll") for (int q = 0; q < 10; ++q) { int pc = q * 256 + tid; int row = pc / 80, cc = pc % 80; \
      xp[q] = *(const u32x4*)(p.x + (size_t)((t_) * 32 + row) * 640 + cc * 8); } } while (0)
#define STX() do { _Pragma("unroll") for (int q = 0; q < 10; ++q) { int pc = q * 256 + tid; int row = pc / 80, cc = pc % 80; \
      *(u32x4*)(xs + row * XS + cc * 8) = xp[q]; } } while (0)
constexpr int XS = 648;
constexpr int KS = 136;

struct Params {
  const u16* x; const u16* mem0; const u16* Wq; const u16* Wk; const u16* Wv; const u16* Wo;
  const u16* Wg; const u16* Wu; const u16* Wd; const u16* gw; u8* ws; u16* out;
};
constexpr int NCH = 10;
constexpr int NTASK = NSEG * NCH;

__global__ void __launch_bounds__(256, 1) k_main(Params p) {
  extern __shared__ __attribute__((aligned(16))) u8 smem[];
  u8* ws = p.ws;
  const int tid = threadIdx.x, w = __builtin_amdgcn_readfirstlane(tid >> 6), lane = tid & 63;
  const int r16 = lane & 15, g = lane >> 4, par = r16 >> 3, r = r16 & 7;
  const int idx = par ? 0xEEEE : 0x4444;
#define XMAP 2
  int b;
  {
    const int bx = blockIdx.x, vi = (bx >> 3) * 4 + (bx & 3);
    if (XMAP == 0) b = bx;
    else if (XMAP == 1) {
      if ((bx & 4) == 0) b = vi < 104 ? vi : 232 + (vi - 104);
      else b = 104 + vi;
    } else if (XMAP == 2) {
      if ((bx & 4) == 0) b = vi < 84 ? vi : vi < 108 ? 232 + (vi - 84) : 104 + (vi - 108);
      else b = vi < 20 ? 84 + vi : 124 + (vi - 20);
    } else {
      if ((bx & 4) == 0) b = vi < 32 ? vi : vi < 72 ? 64 + (vi - 32) : vi < 96 ? 232 + (vi - 72) : 104 + (vi - 96);
      else b = vi < 32 ? 32 + vi : 136 + (vi - 32);
    }
  }
  const __amdgpu_buffer_rsrc_t rs = __builtin_amdgcn_make_buffer_rsrc((void*)ws, 0, (int)OFF_END, RSRC_W3);
  unsigned* ctl = (unsigned*)(ws + OFF_CTL);
  const f32x4 Z4 = {0.f, 0.f, 0.f, 0.f};
  unsigned long long tanchor = 0;
  int dly = b < 32 ? D1 : b < 64 ? DV : D2;
#define AD_UPDATE() do { if (s > 0) { if (spins == 0) dly -= AD_DEC; else dly += AD_INC; if (dly < 0) dly = 0; } } while (0)
  const u32x4 PZ = {0xFFFFFFFFu, 0xFFFFFFFFu, 0xFFFFFFFFu, 0xFFFFFFFFu};
  u8* wl = smem + w * 40960;
  const unsigned wla = __builtin_amdgcn_readfirstlane((unsigned)(size_t)(LAS u8*)smem) + w * 40960;

  if (b < 32) {
    const int h = b;
    u16* sq = (u16*)smem;
    unsigned* spl = (unsigned*)(smem + 4608);
    u32x4* smem_a = (u32x4*)(smem + 8192);
    bf16x16 W[4][10];
    {
      const u8* src = (const u8*)((w < 2 ? p.Wq : p.Wk) + h * 128 + 64 * (w & 1));
#pragma unroll
      for (int i = 0; i < 5; ++i) dma_tile<64, 128>(src + (size_t)i * 64 * 8192, 8192, wla + i * 8192, lane);
#pragma unroll
      for (int i = 0; i < 10; ++i) {
        WAIT_VM(8 * (i < 5 ? 4 : 9 - i));
#pragma unroll
        for (int tn = 0; tn < 4; ++tn) W[tn][i] = frag_sparse(wl + (i % 5) * 8192, 128, 16 * tn, lane);
        if (i < 5) {
          WAIT_LGKM0();
          dma_tile<64, 128>(src + (size_t)(i + 5) * 64 * 8192, 8192, wla + i * 8192, lane);
        }
      }
      WAIT_LGKM0();
    }
#pragma unroll
    for (int tn = 0; tn < 4; ++tn)
#pragma unroll
      for (int i = 0; i < (tn < 2 ? 8 : 7); ++i) PIN_A(W[tn][i]);
    LDS_BARRIER();
    const int moff = (int)OFF_MEM + (2 * g + par) * 128 + r * 16;
    for (int s = 0; s < NSEG; ++s) {
      u32x4 kxf[2][4];
      if (w == 0 && s < NSEG - 1) {
        wait_flag(rs, CW_KXF + 16 * h, (unsigned)s, ctl, lane);
        const int kxo = (int)OFF_KX + (s * 32 + h) * 8192 + r16 * 256 + 16 * g;
#pragma unroll
        for (int t2 = 0; t2 < 2; ++t2)
#pragma unroll
          for (int kk = 0; kk < 4; ++kk) kxf[t2][kk] = ld128(rs, kxo + t2 * 4096 + 64 * kk);
      }
      f32x4 acc[4];
      unsigned spins = 0;
      if ((ADAPT & 1) && s > 0) wait_until(tanchor + dly);
      STAMP(0);
      for (;;) {
        asm volatile("" ::: "memory");
        u32x4 a0 = ld128(rs, moff + s * STEPB + w * 1024);
        u32x4 a1 = ld128(rs, moff + s * STEPB + (w + 4) * 1024);
        u32x4 a2 = ld128(rs, w < 2 ? moff + s * STEPB + (w + 8) * 1024 : OOB);
        asm volatile("" ::: "memory");
        smem_a[w * 64 + lane] = a0; smem_a[(w + 4) * 64 + lane] = a1; smem_a[(w + 8) * 64 + lane] = a2;
        unsigned mxv = umax3(umax3(a0.x, a0.z, a1.x), a1.z, umax3(a2.x, a2.z, 0u));
        if (__all(mxv != 0xFFFFFFFFu) || ++spins > SPIN_CAP) break;
      }
      LDS_BARRIER();
#pragma unroll
      for (int tn = 0; tn < 4; ++tn) { acc[tn] = Z4; PIN_V(acc[tn]); }
      {
        u32x4 af[10];
#pragma unroll
        for (int i = 0; i < 10; ++i) af[i] = smem_a[i * 64 + lane];
        SCHED();
#pragma unroll
        for (int i = 0; i < 10; ++i)
#pragma unroll
          for (int tn = 0; tn < 4; ++tn) acc[tn] = smf(__builtin_bit_cast(bf16x8, af[i]), W[tn][i], acc[tn], idx);
      }
      STAMP(1);
      u16* dst = sq + (w >> 1) * (8 * KS) + (4 * (g & 1)) * KS + 64 * (w & 1) + 16 * (lane >> 5) + r16;
#pragma unroll
      for (int pp = 0; pp < 2; ++pp) {
        float v0 = comb(acc[2 * pp][0], acc[2 * pp + 1][0]), v1 = comb(acc[2 * pp][1], acc[2 * pp + 1][1]);
        float v2 = comb(acc[2 * pp][2], acc[2 * pp + 1][2]), v3 = comb(acc[2 * pp][3], acc[2 * pp + 1][3]);
        unsigned p01 = pk2(v0, v1), p23 = pk2(v2, v3);
        dst[32 * pp] = (u16)p01; dst[32 * pp + KS] = (u16)(p01 >> 16);
        dst[32 * pp + 2 * KS] = (u16)p23; dst[32 * pp + 3 * KS] = (u16)(p23 >> 16);
      }
      LDS_BARRIER();
      STAMP(2);
      if (w == 0 && s < NSEG - 1) {
        f32x4 sc[3] = {Z4, Z4, Z4};
        bf16x8 qf[4], kf[4];
#pragma unroll
        for (int kk = 0; kk < 4; ++kk) qf[kk] = *(const bf16x8*)(sq + r * KS + 32 * kk + 8 * g);
#pragma unroll
        for (int kk = 0; kk < 4; ++kk) kf[kk] = *(const bf16x8*)(sq + 8 * KS + r * KS + 32 * kk + 8 * g);
        SCHED();
#pragma unroll
        for (int kk = 0; kk < 4; ++kk) {
          sc[1] = mf16(__builtin_bit_cast(bf16x8, kxf[0][kk]), qf[kk], sc[1]);
          sc[2] = mf16(__builtin_bit_cast(bf16x8, kxf[1][kk]), qf[kk], sc[2]);
          sc[0] = mf16(kf[kk], qf[kk], sc[0]);
        }
        const float NEG = -3.0e38f;
        float v[6];
        v[0] = par ? sc[1][2] : (g < 2 ? sc[0][0] : NEG);
        v[1] = par ? sc[1][3] : (g < 2 ? sc[0][1] : NEG);
        v[2] = par ? sc[2][0] : (g < 2 ? sc[0][2] : NEG);
        v[3] = par ? sc[2][1] : (g < 2 ? sc[0][3] : NEG);
        v[4] = par ? sc[2][2] : sc[1][0];
        v[5] = par ? sc[2][3] : sc[1][1];
        float mx = rmax3(rmax3(v[0], v[1], v[2]), rmax3(v[3], v[4], v[5]), v[0]);
        mx = xmax32(xmax16(xmax8(mx)));
        const float scale = 0.08838834764831845f;
        const float L2E = 1.4426950408889634f;
        float mr = rbf(rbf(mx) * scale) * L2E;
        float e[6];
#pragma unroll
        for (int q = 0; q < 3; ++q) {
          unsigned p1 = pk2(v[2 * q], v[2 * q + 1]);
          unsigned p2 = pk2(blo(p1) * scale, bhi(p1) * scale);
          e[2 * q] = __builtin_amdgcn_exp2f(fmaf(blo(p2), L2E, -mr));
          e[2 * q + 1] = __builtin_amdgcn_exp2f(fmaf(bhi(p2), L2E, -mr));
        }
        float sum = ((e[0] + e[1]) + (e[2] + e[3])) + (e[4] + e[5]);
        sum = xadd32(xadd16(xadd8(sum)));
        float inv = __builtin_amdgcn_rcpf(sum);
        unsigned o0 = pk2(e[0] * inv, e[1] * inv), o1 = pk2(e[2] * inv, e[3] * inv), o2 = pk2(e[4] * inv, e[5] * inv);
        unsigned* pl = spl + r * 4;
        int dA = 32 + (g >> 1) * 32 + 2 * (g & 1), dB = dA + 64;
        int d0 = par ? dA + 1 : 2 * g, d1 = par ? dB : 2 * g + 1, d2 = par ? dB + 1 : dA;
        if (par || g < 2) { pl[d0] = o0; pl[d1] = o1; }
        pl[d2] = o2;
        WAIT_LGKM0();
        if (lane < 40) {
          u32x4 o = *(const u32x4*)(spl + lane * 4);
          o.x = nosent(o.x); o.y = nosent(o.y); o.z = nosent(o.z); o.w = nosent(o.w);
          st128(o, rs, (int)OFF_P + s * STEPB + h * 640 + lane * 16);
          st128(PZ, rs, (int)OFF_P + (s + 2) * STEPB + h * 640 + lane * 16);
        }
      }
      if (w == 1) {
#pragma unroll
        for (int q = 0; q < 2; ++q) {
          int pc = q * 64 + lane, row = pc >> 4, ch = pc & 15;
          u32x4 o = *(const u32x4*)(sq + 8 * KS + row * KS + ch * 8);
          o.x = nosent(o.x); o.y = nosent(o.y); o.z = nosent(o.z); o.w = nosent(o.w);
          st128(o, rs, (int)OFF_KM + s * STEPB + h * 2048 + pc * 16);
          st128(PZ, rs, (int)OFF_KM + (s + 2) * STEPB + h * 2048 + pc * 16);
        }
      }
      STAMP(3);
      LDS_BARRIER();
      AD_UPDATE();
      tanchor = __builtin_amdgcn_s_memtime();
      STAMP_FLUSH();
    }
  } else if (b < 64) {
    const int h = b - 32;
    u16* sq = (u16*)smem;
    u32x4* smem_a = (u32x4*)(smem + 8192);
    bf16x16 Wv[2][10], Wo[10][2];
    {
      const u8* src = (const u8*)(p.Wv + h * 128 + 32 * w);
      const u8* so = (const u8*)(p.Wo + (size_t)h * 128 * 640 + 160 * w);
#pragma unroll
      for (int i = 0; i < 10; ++i) dma_tile<64, 64>(src + (size_t)i * 64 * 8192, 8192, wla + i * 4096, lane);
      WAIT_VM(20);
#pragma unroll
      for (int i = 0; i < 5; ++i)
#pragma unroll
        for (int tn = 0; tn < 2; ++tn) Wv[tn][i] = frag_sparse(wl + i * 4096, 64, 16 * tn, lane);
      WAIT_LGKM0();
      dma_tile<64, 320>(so, 1280, wla, lane);
      WAIT_VM(20);
#pragma unroll
      for (int i = 5; i < 10; ++i)
#pragma unroll
        for (int tn = 0; tn < 2; ++tn) Wv[tn][i] = frag_sparse(wl + i * 4096, 64, 16 * tn, lane);
      WAIT_LGKM0();
      dma_tile<64, 320>(so + (size_t)64 * 1280, 1280, wla + 20480, lane);
      WAIT_VM(20);
#pragma unroll
      for (int t2 = 0; t2 < 10; ++t2) Wo[t2][0] = frag_sparse(wl, 320, 16 * t2, lane);
      WAIT_VM(0);
#pragma unroll
      for (int t2 = 0; t2 < 10; ++t2) Wo[t2][1] = frag_sparse(wl + 20480, 320, 16 * t2, lane);
      WAIT_LGKM0();
    }
#pragma unroll
    for (int tn = 0; tn < 2; ++tn)
#pragma unroll
      for (int i = 0; i < 6; ++i) PIN_A(Wv[tn][i]);
#pragma unroll
    for (int t2 = 0; t2 < 10; ++t2)
#pragma unroll
      for (int i = 0; i < 2; ++i) PIN_A(Wo[t2][i]);
    LDS_BARRIER();
    const int moff = (int)OFF_MEM + (2 * g + par) * 128 + r * 16;
    const int ucol = 160 * w + 16 * (lane >> 5) + r16;
    const int uoff = (int)OFF_UM + h * 128 + (ucol >> 3) * 4096 + (ucol & 7) * 16 + 8 * (g & 1);
    for (int s = 0; s < NSEG; ++s) {
      f32x4 acc[2];
      unsigned spins = 0;
      if ((ADAPT & 2) && s > 0) wait_until(tanchor + dly);
      STAMP(0);
      for (;;) {
        asm volatile("" ::: "memory");
        u32x4 a0 = ld128(rs, moff + s * STEPB + w * 1024);
        u32x4 a1 = ld128(rs, moff + s * STEPB + (w + 4) * 1024);
        u32x4 a2 = ld128(rs, w < 2 ? moff + s * STEPB + (w + 8) * 1024 : OOB);
        asm volatile("" ::: "memory");
        smem_a[w * 64 + lane] = a0; smem_a[(w + 4) * 64 + lane] = a1; smem_a[(w + 8) * 64 + lane] = a2;
        unsigned mxv = umax3(umax3(a0.x, a0.z, a1.x), a1.z, umax3(a2.x, a2.z, 0u));
        if (__all(mxv != 0xFFFFFFFFu) || ++spins > SPIN_CAP) break;
      }
      LDS_BARRIER();
      acc[0] = Z4; acc[1] = Z4; PIN_V(acc[0]); PIN_V(acc[1]);
      {
        u32x4 af[10];
#pragma unroll
        for (int i = 0; i < 10; ++i) af[i] = smem_a[i * 64 + lane];
        SCHED();
#pragma unroll
        for (int i = 0; i < 10; ++i) {
          acc[0] = smf(__builtin_bit_cast(bf16x8, af[i]), Wv[0][i], acc[0], idx);
          acc[1] = smf(__builtin_bit_cast(bf16x8, af[i]), Wv[1][i], acc[1], idx);
        }
      }
      STAMP(1);
      u16* vs = sq + (s & 1) * (8 * KS);
      {
        u16* dst = vs + (4 * (g & 1)) * KS + 32 * w + 16 * (lane >> 5) + r16;
        float v0 = comb(acc[0][0], acc[1][0]), v1 = comb(acc[0][1], acc[1][1]);
        float v2 = comb(acc[0][2], acc[1][2]), v3 = comb(acc[0][3], acc[1][3]);
        unsigned p01 = pk2(v0, v1), p23 = pk2(v2, v3);
        dst[0] = (u16)p01; dst[KS] = (u16)(p01 >> 16); dst[2 * KS] = (u16)p23; dst[3 * KS] = (u16)(p23 >> 16);
      }
      f32x4 c[10];
#pragma unroll
      for (int t2 = 0; t2 < 10; ++t2) { c[t2] = Z4; PIN_V(c[t2]); }
      LDS_BARRIER();
      STAMP(2);
      bf16x8 a0 = *(const bf16x8*)(vs + r * KS + 8 * (2 * g + par));
      bf16x8 a1 = *(const bf16x8*)(vs + r * KS + 64 + 8 * (2 * g + par));
#pragma unroll
      for (int t2 = 0; t2 < 10; ++t2) c[t2] = smf(a0, Wo[t2][0], c[t2], idx);
#pragma unroll
      for (int t2 = 0; t2 < 10; ++t2) c[t2] = smf(a1, Wo[t2][1], c[t2], idx);
#pragma unroll
      for (int pp = 0; pp < 5; ++pp) {
        float v0 = comb(c[2 * pp][0], c[2 * pp + 1][0]), v1 = comb(c[2 * pp][1], c[2 * pp + 1][1]);
        float v2 = comb(c[2 * pp][2], c[2 * pp + 1][2]), v3 = comb(c[2 * pp][3], c[2 * pp + 1][3]);
        u32x2 o = {nosent(pk2(v0, v1)), nosent(pk2(v2, v3))};
        st64(o, rs, uoff + s * STEPB + pp * 4 * 4096);
      }
#pragma unroll
      for (int pp = 0; pp < 5; ++pp) st64(u32x2{0xFFFFFFFFu, 0xFFFFFFFFu}, rs, uoff + (s + 2) * STEPB + pp * 4 * 4096);
      STAMP(3);
      AD_UPDATE();
      tanchor = __builtin_amdgcn_s_memtime();
      STAMP_FLUSH();
    }
  } else if (b < 104) {
    const int c = b - 64;
    float* sred = (float*)smem;
    if (tid < 32) {
      int sh = tid >> 4, row = (tid >> 1) & 7, cp = tid & 1;
      u32x2 v = *(const u32x2*)(p.mem0 + row * 640 + c * 16 + sh * 8 + cp * 4);
      v.x = nosent(v.x); v.y = nosent(v.y);
      st64(v, rs, (int)OFF_MEM + c * 256 + tid * 8);
    }
    const int sl = 2 * c + par;
    for (int s = 0; s < NSEG - 1; ++s) {
      wait_flag(rs, CW_UXF + 16 * (8 * w + (lane & 7)), (unsigned)s, ctl, lane);
      const int uxo = (int)OFF_UX + ((s * 80 + sl) * 32 + 8 * w) * 512 + r * 64 + 16 * g;
      u32x4 bx[8];
#pragma unroll
      for (int i = 0; i < 8; ++i) bx[i] = ld128(rs, uxo + i * 512);
      const int pb = (r16 < 8) ? (int)OFF_P + s * STEPB + (8 * w) * 640 + r * 16 : OOB;
      const int ub = (int)OFF_UM + s * STEPB + sl * 4096 + (8 * w) * 128 + r * 16;
      f32x4 acc0, acc1;
      unsigned spins = 0;
      if ((ADAPT & 4) && s > 0) wait_until(tanchor + dly);
      STAMP(0);
      for (;;) {
        asm volatile("" ::: "memory");
        u32x4 pa[10], um[2];
#pragma unroll
        for (int i = 0; i < 2; ++i) um[i] = ld128(rs, ub + (4 * i + g) * 128);
#pragma unroll
        for (int i = 0; i < 2; ++i) pa[8 + i] = ld128(rs, pb + (4 * i + g) * 640);
#pragma unroll
        for (int i = 0; i < 8; ++i) pa[i] = ld128(rs, pb + i * 640 + 128 + 128 * g);
        asm volatile("" ::: "memory");
        unsigned mxv = umax3(um[0].x, um[0].z, 0u);
        mxv = umax3(mxv, um[1].x, um[1].z);
#pragma unroll
        for (int i = 0; i < 10; ++i) mxv = umax3(mxv, pa[i].x, pa[i].z);
        if (__all(mxv != 0xFFFFFFFFu) || spins > SPIN_CAP) {
          acc0 = mf16(__builtin_bit_cast(bf16x8, pa[8]), __builtin_bit_cast(bf16x8, um[0]), Z4);
          acc1 = mf16(__builtin_bit_cast(bf16x8, pa[9]), __builtin_bit_cast(bf16x8, um[1]), Z4);
#pragma unroll
          for (int i = 0; i < 8; i += 2) {
            acc0 = mf16(__builtin_bit_cast(bf16x8, pa[i]), __builtin_bit_cast(bf16x8, bx[i]), acc0);
            acc1 = mf16(__builtin_bit_cast(bf16x8, pa[i + 1]), __builtin_bit_cast(bf16x8, bx[i + 1]), acc1);
          }
          break;
        }
        ++spins;
      }
      STAMP(1);
      if (g < 2) {
#pragma unroll
        for (int j = 0; j < 4; ++j) sred[w * 128 + par * 64 + (4 * g + j) * 8 + r] = acc0[j] + acc1[j];
      }
      LDS_BARRIER();
      STAMP(2);
      if (w == 0) {
        const f32x4* sr = (const f32x4*)sred;
        if (lane < 32) {
          f32x4 a = sr[lane], b2 = sr[32 + lane], c2 = sr[64 + lane], d2 = sr[96 + lane];
          f32x4 t = (a + b2) + (c2 + d2);
          u32x2 o = {nosent(pk2(t[0], t[1])), nosent(pk2(t[2], t[3]))};
          st64(o, rs, (int)OFF_MEM + (s + 1) * STEPB + c * 256 + lane * 8);
          st64(u32x2{0xFFFFFFFFu, 0xFFFFFFFFu}, rs, (int)OFF_MEM + (s + 3) * STEPB + c * 256 + lane * 8);
        }
      }
      STAMP(3);
      LDS_BARRIER();
      AD_UPDATE();
      tanchor = __builtin_amdgcn_s_memtime();
      STAMP_FLUSH();
    }
  } else if (b < 136) {
    const int h = b - 104;
    bf16x8 Wk[2][20];
    u32x4 xp[10];
    LDX(0);
    asm volatile("" ::: "memory");
    {
      const u8* sk = (const u8*)(p.Wk + h * 128 + 32 * w);
#pragma unroll
      for (int i = 0; i < 10; ++i) dma_tile<64, 64>(sk + (size_t)i * 64 * 8192, 8192, wla + i * 4096, lane);
      WAIT_VM0();
#pragma unroll
      for (int ks = 0; ks < 20; ++ks)
#pragma unroll
        for (int tn = 0; tn < 2; ++tn) Wk[tn][ks] = frag_dense(wl, 64, 32 * ks, 16 * tn, lane);
      WAIT_LGKM0();
    }
#pragma unroll
    for (int tn = 0; tn < 2; ++tn)
#pragma unroll
      for (int ks = 0; ks < 20; ++ks) PIN_A(Wk[tn][ks]);
    LDS_BARRIER();
    u16* xs = (u16*)smem;
    u16* kb = (u16*)(smem + 41472);
    for (int t = 0; t < NSEG; ++t) {
      STAMP(0);
      STX();
      LDS_BARRIER();
      STAMP(1);
      LDX(t + 1 < NSEG ? t + 1 : 0);
      f32x4 acc[2][2];
      PROJ_GEMM(acc, Wk);
#pragma unroll
      for (int m = 0; m < 2; ++m)
#pragma unroll
        for (int tn = 0; tn < 2; ++tn) {
          u16* dst = kb + (8 * g + 4 * m) * KS + 32 * w + 16 * tn + r16;
          unsigned p01 = pk2(acc[m][tn][0], acc[m][tn][1]), p23 = pk2(acc[m][tn][2], acc[m][tn][3]);
          dst[0] = (u16)p01; dst[KS] = (u16)(p01 >> 16); dst[2 * KS] = (u16)p23; dst[3 * KS] = (u16)(p23 >> 16);
        }
      WAIT_VM(10);
      LDS_BARRIER();
      if (tid == 0 && t > 1) st32((unsigned)(t - 1), rs, (CW_KXF + 16 * h) * 4);
      STAMP(2);
#pragma unroll
      for (int q = 0; q < 2; ++q) {
        int pc = q * 256 + tid, row = pc >> 4, ch = pc & 15;
        u32x4 o = *(const u32x4*)(kb + row * KS + ch * 8);
        st128(o, rs, (int)OFF_KX + (t * 32 + h) * 8192 + pc * 16);
      }
      if (t == 0 || t == NSEG - 1) {
        WAIT_VM0();
        LDS_BARRIER();
        if (tid == 0) st32((unsigned)t, rs, (CW_KXF + 16 * h) * 4);
      }
      STAMP(3);
    }
  } else if (b < 168) {
    const int h = b - 136;
    wait_until(__builtin_amdgcn_s_memtime() + START_DLY);
    bf16x8 Wq[2][20];
    {
      const u8* sqp = (const u8*)(p.Wq + h * 128 + 32 * w);
#pragma unroll
      for (int i = 0; i < 10; ++i) dma_tile<64, 64>(sqp + (size_t)i * 64 * 8192, 8192, wla + i * 4096, lane);
      WAIT_VM0();
#pragma unroll
      for (int ks = 0; ks < 20; ++ks)
#pragma unroll
        for (int tn = 0; tn < 2; ++tn) Wq[tn][ks] = frag_dense(wl, 64, 32 * ks, 16 * tn, lane);
      WAIT_LGKM0();
    }
#pragma unroll
    for (int tn = 0; tn < 2; ++tn)
#pragma unroll
      for (int ks = 0; ks < 20; ++ks) PIN_A(Wq[tn][ks]);
    LDS_BARRIER();
    u16* xs = (u16*)smem;
    u16* kb = (u16*)(smem + 41472);
    u16* kmb = (u16*)(smem + 41472 + 8704);
    u8* pxb = smem + 41472 + 8704 + 2176;
    u32x4 xp[10];
    LDX(0);
    const float scale = 0.08838834764831845f;
    const float L2E = 1.4426950408889634f;
    int kflag = -1;
    u16* kxb = (u16*)(smem + 57344);
    {
      wait_flag(rs, CW_KXF + 16 * h, 0u, ctl, lane);
      u32x4 a = ld128(rs, (int)OFF_KX + h * 8192 + tid * 16), c2 = ld128(rs, (int)OFF_KX + h * 8192 + 4096 + tid * 16);
      *(u32x4*)(kxb + (tid >> 4) * KS + (tid & 15) * 8) = a;
      *(u32x4*)(kxb + (16 + (tid >> 4)) * KS + (tid & 15) * 8) = c2;
    }
    for (int u = 0; u < NSEG; ++u) {
      STAMP(0);
      STX();
      LDS_BARRIER();
      STAMP(1);
      const int un = u + 1 < NSEG ? u + 1 : u;
      if (kflag < un) {
        wait_flag(rs, CW_KXF + 16 * h, (unsigned)un, ctl, lane);
        kflag = __builtin_amdgcn_readfirstlane((int)ld32(rs, (CW_KXF + 16 * h) * 4));
      }
      u32x4 kn0 = ld128(rs, (int)OFF_KX + (un * 32 + h) * 8192 + tid * 16);
      u32x4 kn1 = ld128(rs, (int)OFF_KX + (un * 32 + h) * 8192 + 4096 + tid * 16);
      u32x4 k0 = ld128(rs, w == 3 ? (int)OFF_KM + u * STEPB + h * 2048 + lane * 16 : OOB);
      u32x4 k1 = ld128(rs, w == 3 ? (int)OFF_KM + u * STEPB + h * 2048 + 1024 + lane * 16 : OOB);
      LDX(u + 1 < NSEG ? u + 1 : 0);
      f32x4 acc[2][2];
      PROJ_GEMM(acc, Wq);
#pragma unroll
      for (int m = 0; m < 2; ++m)
#pragma unroll
        for (int tn = 0; tn < 2; ++tn) {
          u16* dst = kb + (16 * m + 4 * g) * KS + 32 * w + 16 * tn + r16;
          unsigned p01 = pk2(acc[m][tn][0], acc[m][tn][1]), p23 = pk2(acc[m][tn][2], acc[m][tn][3]);
          dst[0] = (u16)p01; dst[KS] = (u16)(p01 >> 16); dst[2 * KS] = (u16)p23; dst[3 * KS] = (u16)(p23 >> 16);
        }
      WAIT_VM(14);
      LDS_BARRIER();
      if (tid == 0 && u > 0 && u < NSEG - 1) st32((unsigned)(u - 1), rs, (CW_PXF + 16 * h) * 4);
      if (w == 3) {
        unsigned spins = 0;
        for (;;) {
          unsigned mxv = umax3(umax3(k0.x, k0.z, k1.x), k1.z, 0u);
          if (__all(mxv != 0xFFFFFFFFu) || spins > SPIN_CAP) {
            *(u32x4*)(kmb + (lane >> 4) * KS + (lane & 15) * 8) = k0;
            *(u32x4*)(kmb + (4 + (lane >> 4)) * KS + (lane & 15) * 8) = k1;
            break;
          }
          if (spins) __builtin_amdgcn_s_sleep(6);
          ++spins;
          asm volatile("" ::: "memory");
          k0 = ld128(rs, (int)OFF_KM + u * STEPB + h * 2048 + lane * 16);
          k1 = ld128(rs, (int)OFF_KM + u * STEPB + h * 2048 + 1024 + lane * 16);
          asm volatile("" ::: "memory");
        }
      }
      LDS_BARRIER();
      STAMP(2);
      {
        f32x4 sc[3];
        bf16x8 qf[4], kf[4];
#pragma unroll
        for (int kk = 0; kk < 4; ++kk) qf[kk] = *(const bf16x8*)(kb + (8 * w + r) * KS + 32 * kk + 8 * g);
#pragma unroll
        for (int kk = 0; kk < 4; ++kk) kf[kk] = *(const bf16x8*)(kmb + r * KS + 32 * kk + 8 * g);
        bf16x8 kxf[2][4];
        const u16* kxc = kxb + (u & 1) * (32 * KS);
#pragma unroll
        for (int t2 = 0; t2 < 2; ++t2)
#pragma unroll
          for (int kk = 0; kk < 4; ++kk) kxf[t2][kk] = *(const bf16x8*)(kxc + (16 * t2 + r16) * KS + 32 * kk + 8 * g);
        SCHED();
#pragma unroll
        for (int kk = 0; kk < 4; ++kk) {
          sc[1] = mf16(kxf[0][kk], qf[kk], kk == 0 ? Z4 : sc[1]);
          sc[2] = mf16(kxf[1][kk], qf[kk], kk == 0 ? Z4 : sc[2]);
          sc[0] = mf16(kf[kk], qf[kk], kk == 0 ? Z4 : sc[0]);
        }
        const int tok = 8 * w + r;
        const int tkA = 16 * (g & 1) + 4 * (g >> 1), tkB = tkA + 8;
        float v[6];
        int tk[6];
        v[0] = par ? sc[1][2] : sc[0][0]; tk[0] = par ? tkA + 2 : -1;
        v[1] = par ? sc[1][3] : sc[0][1]; tk[1] = par ? tkA + 3 : -1;
        v[2] = par ? sc[2][0] : sc[0][2]; tk[2] = par ? tkB : -1;
        v[3] = par ? sc[2][1] : sc[0][3]; tk[3] = par ? tkB + 1 : -1;
        v[4] = par ? sc[2][2] : sc[1][0]; tk[4] = par ? tkB + 2 : tkA;
        v[5] = par ? sc[2][3] : sc[1][1]; tk[5] = par ? tkB + 3 : tkA + 1;
        const bool nomem = !par && g >= 2;
        float sv[6];
#pragma unroll
        for (int q = 0; q < 3; ++q) {
          unsigned p1 = pk2(v[2 * q], v[2 * q + 1]);
          unsigned p2 = pk2(blo(p1) * scale, bhi(p1) * scale);
          sv[2 * q] = blo(p2); sv[2 * q + 1] = bhi(p2);
        }
        const float MASKV = rbf(-1.0e9f);
#pragma unroll
        for (int q = 0; q < 6; ++q) {
          if (tk[q] > tok) sv[q] = MASKV;
          if (q < 4 && nomem) sv[q] = -3.0e38f;
        }
        float mx = fmaxf(fmaxf(fmaxf(sv[0], sv[1]), fmaxf(sv[2], sv[3])), fmaxf(sv[4], sv[5]));
        mx = xmax32(xmax16(xmax8(mx)));
        float e[6];
#pragma unroll
        for (int q = 0; q < 6; ++q) e[q] = __builtin_amdgcn_exp2f((sv[q] - mx) * L2E);
        float sum = ((e[0] + e[1]) + (e[2] + e[3])) + (e[4] + e[5]);
        sum = xadd32(xadd16(xadd8(sum)));
        float inv = __builtin_amdgcn_rcpf(sum);
        unsigned o0 = pk2(e[0] * inv, e[1] * inv), o1 = pk2(e[2] * inv, e[3] * inv), o2 = pk2(e[4] * inv, e[5] * inv);
        unsigned* pl = (unsigned*)pxb + tok * 4;
        int dA = 128 + (g >> 1) * 128 + 2 * (g & 1), dB = dA + 256;
        int d0 = par ? dA + 1 : 2 * g, d1 = par ? dB : 2 * g + 1, d2 = par ? dB + 1 : dA;
        if (!nomem) { pl[d0] = o0; pl[d1] = o1; }
        pl[d2] = o2;
      }
      {
        u16* kxn = kxb + ((u + 1) & 1) * (32 * KS);
        *(u32x4*)(kxn + (tid >> 4) * KS + (tid & 15) * 8) = kn0;
        *(u32x4*)(kxn + (16 + (tid >> 4)) * KS + (tid & 15) * 8) = kn1;
      }
      LDS_BARRIER();
      if (tid < 160) {
        u32x4 o = *(const u32x4*)(pxb + tid * 16);
        st128(o, rs, (int)OFF_PX + (u * 32 + h) * 2560 + tid * 16);
      }
      if (u >= NSEG - 2) {
        WAIT_VM0();
        LDS_BARRIER();
        if (tid == 0) st32((unsigned)u, rs, (CW_PXF + 16 * h) * 4);
      }
    }
  } else if (b < 200) {
    const int h = b - 168;
    bf16x8 Wv[2][20], Wo[10][4];
    u32x4 xp[10];
    LDX(0);
    asm volatile("" ::: "memory");
    {
      const u8* sv = (const u8*)(p.Wv + h * 128 + 32 * w);
      const u8* so = (const u8*)(p.Wo + (size_t)h * 128 * 640 + 160 * w);
#pragma unroll
      for (int i = 0; i < 10; ++i) dma_tile<64, 64>(sv + (size_t)i * 64 * 8192, 8192, wla + i * 4096, lane);
      WAIT_VM(20);
#pragma unroll
      for (int ks = 0; ks < 10; ++ks)
#pragma unroll
        for (int tn = 0; tn < 2; ++tn) Wv[tn][ks] = frag_dense(wl, 64, 32 * ks, 16 * tn, lane);
      WAIT_LGKM0();
      dma_tile<64, 320>(so, 1280, wla, lane);
      WAIT_VM(20);
#pragma unroll
      for (int ks = 10; ks < 20; ++ks)
#pragma unroll
        for (int tn = 0; tn < 2; ++tn) Wv[tn][ks] = frag_dense(wl, 64, 32 * ks, 16 * tn, lane);
      WAIT_LGKM0();
      dma_tile<64, 320>(so + (size_t)64 * 1280, 1280, wla + 20480, lane);
      WAIT_VM(20);
#pragma unroll
      for (int ks = 0; ks < 2; ++ks)
#pragma unroll
        for (int t2 = 0; t2 < 10; ++t2) Wo[t2][ks] = frag_dense(wl, 320, 32 * ks, 16 * t2, lane);
      WAIT_VM(0);
#pragma unroll
      for (int ks = 2; ks < 4; ++ks)
#pragma unroll
        for (int t2 = 0; t2 < 10; ++t2) Wo[t2][ks] = frag_dense(wl, 320, 32 * ks, 16 * t2, lane);
      WAIT_LGKM0();
    }
#pragma unroll
    for (int t2 = 0; t2 < 10; ++t2)
#pragma unroll
      for (int ks = 0; ks < 4; ++ks) PIN_A(Wo[t2][ks]);
#pragma unroll
    for (int tn = 0; tn < 2; ++tn)
#pragma unroll
      for (int ks = 0; ks < 8; ++ks) PIN_A(Wv[tn][ks]);
    LDS_BARRIER();
    u16* xs = (u16*)smem;
    u16* vb = (u16*)(smem + 41472);
    for (int t = 0; t < NSEG; ++t) {
      STX();
      LDS_BARRIER();
      LDX(t + 1 < NSEG ? t + 1 : 0);
      f32x4 acc[2][2];
      PROJ_GEMM(acc, Wv);
#pragma unroll
      for (int m = 0; m < 2; ++m)
#pragma unroll
        for (int tn = 0; tn < 2; ++tn) {
          u16* dst = vb + (16 * m + 4 * g) * KS + 32 * w + 16 * tn + r16;
          unsigned p01 = pk2(acc[m][tn][0], acc[m][tn][1]), p23 = pk2(acc[m][tn][2], acc[m][tn][3]);
          dst[0] = (u16)p01; dst[KS] = (u16)(p01 >> 16); dst[2 * KS] = (u16)p23; dst[3 * KS] = (u16)(p23 >> 16);
        }
      WAIT_VM(10);
      LDS_BARRIER();
      if (tid == 0 && t > 1) st32((unsigned)(t - 1), rs, (CW_UXF + 16 * h) * 4);
      bf16x8 va[2][4];
#pragma unroll
      for (int m = 0; m < 2; ++m)
#pragma unroll
        for (int ks = 0; ks < 4; ++ks) va[m][ks] = *(const bf16x8*)(vb + (16 * m + r16) * KS + 32 * ks + 8 * g);
#pragma unroll
      for (int t2 = 0; t2 < 10; ++t2) {
        f32x4 c0 = Z4, c1 = Z4;
#pragma unroll
        for (int ks = 0; ks < 4; ++ks) { c0 = mf16(va[0][ks], Wo[t2][ks], c0); c1 = mf16(va[1][ks], Wo[t2][ks], c1); }
        int col = 160 * w + 16 * t2 + r16;
        u32x4 o = {pk2(c0[0], c0[1]), pk2(c0[2], c0[3]), pk2(c1[0], c1[1]), pk2(c1[2], c1[3])};
        st128(o, rs, (int)OFF_UX + ((t * 80 + (col >> 3)) * 32 + h) * 512 + (col & 7) * 64 + 16 * g);
      }
      if (t == 0 || t == NSEG - 1) {
        WAIT_VM0();
        LDS_BARRIER();
        if (tid == 0) st32((unsigned)t, rs, (CW_UXF + 16 * h) * 4);
      }
    }
  } else if (b < 232) {
    const int f = b - 200;
    wait_until(__builtin_amdgcn_s_memtime() + START_DLY);
    u8* slab0 = smem;
    const unsigned ldsb = __builtin_amdgcn_readfirstlane((unsigned)(size_t)(LAS u8*)smem);
    u8* wdb = smem + 65536;
    u16* hb = (u16*)(smem + 65536 + 81920);
    bf16x8 A[20];
#pragma unroll
    for (int ks = 0; ks < 20; ++ks)
      A[ks] = *(const bf16x8*)(p.x + (size_t)(64 * f + 16 * w + r16) * 640 + 32 * ks + 8 * g);
#pragma unroll
    for (int ks = 0; ks < 16; ++ks) PIN_A(A[ks]);
    f32x4 acc2[4][10];
#pragma unroll
    for (int m = 0; m < 4; ++m)
#pragma unroll
      for (int n = 0; n < 10; ++n) acc2[m][n] = Z4;
    const int i16 = lane & 15;
    unsigned so[2], tb[4], wo2[2], tw;
#pragma unroll
    for (int q = 0; q < 2; ++q) {
      int row = 8 * (2 * w + q) + (lane >> 3);
      so[q] = (unsigned)(row * (DF * 2) + (((lane & 7) ^ swz<0>(row)) << 4));
      wo2[q] = (unsigned)((lane >> 4) * 1280 + (((lane & 15) ^ (2 * ((lane >> 4) | (q << 2)))) << 4));
    }
#pragma unroll
    for (int q = 0; q < 4; ++q) {
      int s0 = swz<0>(8 * g + (i16 >> 2));
      tb[q] = ldsb + (unsigned)((8 * g + (i16 >> 2)) * 128 + (((2 * q + ((i16 & 3) >> 1)) ^ s0) << 4) + (i16 & 1) * 8);
    }
    const int s1 = 2 * ((i16 >> 2) | ((g & 1) << 2));
    tw = ldsb + 65536 + (unsigned)((8 * g + (i16 >> 2)) * 256 + ((i16 & 3) >> 1) * 16 + (i16 & 1) * 8);
#define FFN_SLAB(c_, si_, slot_) do { \
      const unsigned uo_ = (unsigned)((64 * (si_)) * (DF * 2) + 128 * (c_)); \
      const unsigned v0_ = so[0] + uo_, v1_ = so[1] + uo_; \
      const unsigned bb_ = lw2 + (slot_) * 16384; \
      GLDS_I(0, p.Wg, v0_, bb_); GLDS_I(1024, p.Wg, v1_, bb_); \
      GLDS_I(8192, p.Wu, v0_, bb_); GLDS_I(9216, p.Wu, v1_, bb_); } while (0)
#define FFN_WD(si_) ((si_) < 0 ? 0 : (si_) < 6 ? 3 : (si_) == 6 ? 2 : 0)
    WAIT_VM0();
    int wq = w;
    const unsigned lw2 = ldsb + 2 * w * 1024;
    const unsigned lwd = ldsb + 65536 + w * 4096;
    const unsigned wdw = (unsigned)(w * 4 * 5120);
    const int c0 = f % 27;
    unsigned m0keep;
    asm volatile("s_mov_b32 %0, m0" : "=s"(m0keep) :: "memory");
    FFN_SLAB(c0, 0, 0); FFN_SLAB(c0, 1, 1); FFN_SLAB(c0, 2, 2);
#pragma unroll 1
    for (int ci = 0; ci < 27; ++ci) {
      const int c = c0 + ci < 27 ? c0 + ci : c0 + ci - 27;
      const int cn = c + 1 < 27 ? c + 1 : 0;
      const int slotb = (ci * 10) & 3;
      const unsigned wd0 = wo2[0] + wdw + (unsigned)(c * 64 * 1280), wd1 = wo2[1] + wdw + (unsigned)(c * 64 * 1280);
      f32x4 ag[4], au[4];
#pragma unroll
      for (int si = 0; si < 10; ++si) {
        asm volatile("s_waitcnt vmcnt(%0)" ::"n"(FFN_WD(si - 3) + 4 + FFN_WD(si - 2) + 4 + FFN_WD(si - 1)) : "memory");
        LDS_BARRIER();
        if (si + 3 < 10) FFN_SLAB(c, si + 3, (slotb + si + 3) & 3);
        else FFN_SLAB(cn, si + 3 - 10, (slotb + si + 3) & 3);
#define FFN_WDP(j_) do { if ((j_) < 20 && (j_) < 3 * si + FFN_WD(si)) { \
          const unsigned vo_ = ((((j_) >> 1) & 1) ? wd1 : wd0) + (unsigned)(((j_) & 3) * 5120 + ((j_) >> 2) * 256); \
          GLDS_I((((j_) >> 2) * 16384 + ((j_) & 3) * 1024), p.Wd, vo_, lwd); } } while (0)
        FFN_WDP(3 * si); FFN_WDP(3 * si + 1); FFN_WDP(3 * si + 2);
        const unsigned sof = ((slotb + si) & 3) * 16384;
#pragma unroll
        for (int k2 = 0; k2 < 2; ++k2) {
          const int ks = si * 2 + k2;
          bf16x8 bg[4], bu[4];
#pragma unroll
          for (int ft = 0; ft < 4; ++ft) {
            const unsigned t0 = tb[ft] + sof + k2 * 4096;
            bg[ft] = trfrag(t0, t0 + 512);
            bu[ft] = trfrag(t0 + 8192, t0 + 8192 + 512);
          }
#pragma unroll
          for (int ft = 0; ft < 4; ++ft) {
            ag[ft] = mf16(A[ks], bg[ft], ks == 0 ? Z4 : ag[ft]);
            au[ft] = mf16(A[ks], bu[ft], ks == 0 ? Z4 : au[ft]);
          }
        }
      }
#pragma unroll
      for (int ft = 0; ft < 4; ++ft)
#pragma unroll
        for (int j = 0; j < 4; ++j) {
          float gv = rbf(ag[ft][j]), uv = rbf(au[ft][j]);
          hb[(16 * w + 4 * g + j) * 72 + 16 * ft + r16] = f2bf(rbf(gelu_tanh(gv)) * uv);
        }
      asm volatile("s_waitcnt vmcnt(12)" ::: "memory");
      LDS_BARRIER();
#pragma unroll
      for (int k2 = 0; k2 < 2; ++k2) {
        bf16x8 ha[4];
#pragma unroll
        for (int m = 0; m < 4; ++m) ha[m] = *(const bf16x8*)(hb + (16 * m + r16) * 72 + 32 * k2 + 8 * g);
#pragma unroll
        for (int n = 0; n < 10; ++n) {
          const int nc = 160 * wq + 16 * n;
          const unsigned ta = tw + (nc >> 7) * 16384 + ((((nc & 127) >> 3) ^ s1) << 4) + k2 * 8192;
          bf16x8 bd = trfrag(ta, ta + 1024);
#pragma unroll
          for (int m = 0; m < 4; ++m) acc2[m][n] = mf16(ha[m], bd, acc2[m][n]);
        }
      }
      LDS_BARRIER();
    }
    asm volatile("s_mov_b32 m0, %0" :: "s"(m0keep) : "memory");
    WAIT_VM0();
    LDS_BARRIER();
    {
      u16* ob = (u16*)smem;
#pragma unroll
      for (int m = 0; m < 4; ++m)
#pragma unroll
        for (int n = 0; n < 10; ++n)
#pragma unroll
          for (int j = 0; j < 4; ++j) ob[(16 * m + 4 * g + j) * XS + 160 * w + 16 * n + r16] = f2bf(acc2[m][n][j]);
      LDS_BARRIER();
      for (int pc = tid; pc < 64 * 80; pc += 256) {
        int row = pc / 80, cc = pc % 80;
        u32x4 o = *(const u32x4*)(ob + row * XS + cc * 8);
        st128(o, rs, (int)OFF_BO + (64 * f + row) * 1280 + cc * 16);
      }
      WAIT_VM0();
      LDS_BARRIER();
      unsigned* smx = (unsigned*)(smem + 90112);
      if (tid == 0) {
        st32(0u, rs, (CW_FF + 16 * f) * 4);
        *smx = atomicAnd(ctl + CW_MIX + 16 * f, 0x7FFFFFFFu);
      }
      LDS_BARRIER();
      const unsigned done = ~(*smx) & 0xFFFFFu;
      if (done) {
        const int row = tid >> 3, c8 = tid & 7;
        float omg[2];
#pragma unroll
        for (int hs = 0; hs < 2; ++hs) omg[hs] = rbf(1.0f - gate8(p.x + (size_t)(64 * f + 32 * hs + row) * 640, p.gw, c8));
        for (int k = 0; k < 20; ++k) {
          if (!((done >> k) & 1)) continue;
          const int hs = k / NCH, cj = k % NCH;
          const int tr = 32 * hs + row, cc = cj * 64 + c8 * 8;
          u32x4 ga = ld128(rs, (int)OFF_GA + ((64 * f + tr) * 640 + cc) * 2);
          u32x4 bv = *(const u32x4*)(ob + tr * XS + cc);
          const float om = hs ? omg[1] : omg[0];
          u32x4 o;
#pragma unroll
          for (int e = 0; e < 4; ++e)
            o[e] = pk2(blo(ga[e]) + rbf(om * blo(bv[e])), bhi(ga[e]) + rbf(om * bhi(bv[e])));
          *(u32x4*)(p.out + (size_t)(64 * f + tr) * 640 + cc) = o;
        }
      }
    }
  }

  if (b >= 104 && !(b >= 136 && b < 168)) {
    int tid = threadIdx.x;
    asm volatile("" : "+v"(tid));
    const int lane = tid & 63, r16 = lane & 15, g = lane >> 4;
    float* sg = (float*)smem;
    int* stask = (int*)(smem + 256);
    for (;;) {
      LDS_BARRIER();
      if (tid == 0) *stask = (int)atomicAdd(ctl + CW_QUEUE, 1u) + 1;
      LDS_BARRIER();
      const int task = *stask;
      if (task >= NTASK) break;
      const int t = task / NCH, cj = task % NCH;
      STAMP(0);
      {
        float gv = gate8(p.x + (size_t)(t * 32 + (tid >> 3)) * 640, p.gw, tid & 7);
        if ((tid & 7) == 0) sg[tid >> 3] = gv;
      }
      if (w == 0) wait_flag<OUT_SLP>(rs, CW_UXF + 16 * (lane & 31), (unsigned)t, ctl, lane);
      LDS_BARRIER();
      STAMP(1);
      const int col = cj * 64 + w * 16 + r16;
      const int uxo = (int)OFF_UX + (t * 80 + (col >> 3)) * 32 * 512 + (col & 7) * 64 + 16 * g;
      const int umo = (int)OFF_UM + t * STEPB + (col >> 3) * 4096 + (col & 7) * 16;
      u32x4 bx[32];
#pragma unroll
      for (int h = 0; h < 32; ++h) bx[h] = ld128(rs, uxo + h * 512);
      if (w == 0) wait_flag<OUT_SLP>(rs, CW_PXF + 16 * (lane & 31), (unsigned)t, ctl, lane);
      LDS_BARRIER();
      u8* pxl = smem + 1024;
      {
        const u8* pxg = ws + OFF_PX + (size_t)t * 32 * 2560;
#pragma unroll
        for (int q = 0; q < 20; ++q)
          __builtin_amdgcn_global_load_lds((const unsigned*)(pxg + (20 * w + q) * 1024 + lane * 16), (unsigned*)(pxl + (20 * w + q) * 1024), 16, 0, AUX_SC1);
      }
      u32x4 um[8];
      {
        unsigned spins = 0;
        for (;;) {
          asm volatile("" ::: "memory");
          unsigned mxv = 0;
#pragma unroll
          for (int hq = 0; hq < 8; ++hq) um[hq] = ld128(rs, umo + (4 * hq + g) * 128);
#pragma unroll
          for (int hq = 0; hq < 8; ++hq) mxv = umax3(mxv, um[hq].x, um[hq].z);
          if (__all(mxv != 0xFFFFFFFFu) || spins > SPIN_CAP) break;
          __builtin_amdgcn_s_sleep(4);
          ++spins;
        }
      }
      WAIT_VM0();
      LDS_BARRIER();
      f32x4 acc0 = Z4, acc1 = Z4;
#pragma unroll
      for (int h = 0; h < 32; ++h) {
        bf16x8 a0 = *(const bf16x8*)(pxl + h * 2560 + 512 + g * 512 + r16 * 16);
        bf16x8 a1 = *(const bf16x8*)(pxl + h * 2560 + 512 + g * 512 + 256 + r16 * 16);
        acc0 = mf16(a0, __builtin_bit_cast(bf16x8, bx[h]), acc0);
        acc1 = mf16(a1, __builtin_bit_cast(bf16x8, bx[h]), acc1);
      }
#pragma unroll
      for (int hq = 0; hq < 8; ++hq) {
        bf16x8 a0 = *(const bf16x8*)(pxl + (4 * hq + g) * 2560 + r16 * 16);
        bf16x8 a1 = *(const bf16x8*)(pxl + (4 * hq + g) * 2560 + 256 + r16 * 16);
        acc0 = mf16(a0, __builtin_bit_cast(bf16x8, um[hq]), acc0);
        acc1 = mf16(a1, __builtin_bit_cast(bf16x8, um[hq]), acc1);
      }
      STAMP(2);
      u16* rt = (u16*)(smem + 512 + 83968);
#pragma unroll
      for (int i = 0; i < 2; ++i)
#pragma unroll
        for (int q = 0; q < 4; ++q) {
          int tok = 16 * i + 4 * g + q;
          rt[tok * 72 + w * 16 + r16] = f2bf(sg[tok] * rbf(i == 0 ? acc0[q] : acc1[q]));
        }
      if (tid == 0) stask[1] = (int)ld32(rs, (CW_FF + 16 * (t >> 1)) * 4);
      LDS_BARRIER();
      {
        const int row = tid >> 3, c8 = tid & 7;
        const int go = ((t * 32 + row) * 640 + cj * 64 + c8 * 8) * 2;
        u32x4 ga = *(const u32x4*)(rt + row * 72 + c8 * 8);
        bool mix = stask[1] >= 0;
        if (!mix) {
          st128(ga, rs, (int)OFF_GA + go);
          WAIT_VM0();
          LDS_BARRIER();
          if (tid == 0) stask[2] = (int)atomicAnd(ctl + CW_MIX + 16 * (t >> 1), ~(1u << ((t & 1) * NCH + cj)));
          LDS_BARRIER();
          mix = stask[2] >= 0;
        }
        if (mix) {
          u32x4 bv = ld128(rs, (int)OFF_BO + go);
          const float om = rbf(1.0f - sg[row]);
          u32x4 o;
#pragma unroll
          for (int e = 0; e < 4; ++e)
            o[e] = pk2(blo(ga[e]) + rbf(om * blo(bv[e])), bhi(ga[e]) + rbf(om * bhi(bv[e])));
          *(u32x4*)((u8*)p.out + go) = o;
        }
      }
    }
  }
}

extern "C" void kernel_launch(void* const* d_in, const int* in_sizes, int n_in, void* d_out, int out_size, void* d_ws,
                              size_t ws_size, hipStream_t stream) {
  u8* ws = (u8*)d_ws;
  Params p;
  p.x = (const u16*)d_in[0]; p.mem0 = (const u16*)d_in[1]; p.Wq = (const u16*)d_in[2]; p.Wk = (const u16*)d_in[3];
  p.Wv = (const u16*)d_in[4]; p.Wo = (const u16*)d_in[5];
  p.Wg = (const u16*)d_in[8]; p.Wu = (const u16*)d_in[9]; p.Wd = (const u16*)d_in[10]; p.gw = (const u16*)d_in[11];
  p.ws = ws; p.out = (u16*)d_out;

  (void)hipFuncSetAttribute((const void*)k_main, hipFuncAttributeMaxDynamicSharedMemorySize, 163840);
  (void)hipMemsetAsync(ws, 0xFF, INIT_BYTES, stream);
  k_main<<<256, 256, 163840, stream>>>(p);
}
```

```cpp
#include <hip/hip_runtime.h>
#include <hip/hip_bf16.h>
#include <stdint.h>

typedef __bf16 bf16x8 __attribute__((ext_vector_type(8)));
typedef __bf16 bf16x16 __attribute__((ext_vector_type(16)));
typedef __bf16 bf16x2 __attribute__((ext_vector_type(2)));
typedef float f32x2 __attribute__((ext_vector_type(2)));
typedef float f32x4 __attribute__((ext_vector_type(4)));
typedef unsigned u32x4 __attribute__((ext_vector_type(4)));
typedef unsigned u32x2 __attribute__((ext_vector_type(2)));
typedef unsigned u32x8 __attribute__((ext_vector_type(8)));
typedef short s16x4 __attribute__((ext_vector_type(4)));
typedef unsigned short u16;
typedef unsigned char u8;

constexpr int SEQ = 2048, DM = 640, NSEG = 64, DF = 1728;

constexpr size_t OFF_CTL = 0;
constexpr size_t CTL_BYTES = 16384;
constexpr int STEPB = 10240 + 20480 + 65536 + 327680;
constexpr size_t OFF_MEM = CTL_BYTES;
constexpr size_t OFF_P = OFF_MEM + 10240;
constexpr size_t OFF_KM = OFF_P + 20480;
constexpr size_t OFF_UM = OFF_KM + 65536;
constexpr size_t OFF_SENT_END = CTL_BYTES + (size_t)66 * STEPB;
constexpr size_t INIT_BYTES = CTL_BYTES + (size_t)3 * STEPB;
constexpr size_t OFF_KX = OFF_SENT_END;
constexpr size_t OFF_UX = OFF_KX + (size_t)SEQ * 4096 * 2;
constexpr size_t OFF_PX = OFF_UX + (size_t)64 * 80 * 32 * 8 * 32 * 2;
constexpr size_t OFF_BO = OFF_PX + (size_t)64 * 32 * 2560;
constexpr size_t OFF_GA = OFF_BO + (size_t)SEQ * DM * 2;
constexpr size_t OFF_END = OFF_GA + (size_t)SEQ * DM * 2;
constexpr int CW_TMO = 0, CW_QUEUE = 32, CW_KXF = 64, CW_UXF = 1024, CW_PXF = 2048, CW_FF = 3072, CW_MIX = 3584;

#define AUX_SC1 16
#define RSRC_W3 0x00020000
#define OOB 0x7FFF0000

__device__ __forceinline__ float bf2f(u16 v) { return __uint_as_float(((unsigned)v) << 16); }
__device__ __forceinline__ u16 f2bf(float f) { __bf16 b = (__bf16)f; return __builtin_bit_cast(u16, b); }
__device__ __forceinline__ float rbf(float f) { return (float)((__bf16)f); }
__device__ __forceinline__ unsigned pk2(float a, float b) {
  f32x2 v = {a, b};
  return __builtin_bit_cast(unsigned, __builtin_convertvector(v, bf16x2));
}
__device__ __forceinline__ unsigned nosent(unsigned x) { return x < 0xFFFFFFFEu ? x : 0xFFFFFFFEu; }
__device__ __forceinline__ f32x4 smf(bf16x8 a, bf16x16 b, f32x4 c, int idx) {
  return __builtin_amdgcn_smfmac_f32_16x16x64_bf16(a, b, c, idx, 0, 0);
}
__device__ __forceinline__ f32x4 mf16(bf16x8 a, bf16x8 b, f32x4 c) {
  return __builtin_amdgcn_mfma_f32_16x16x32_bf16(a, b, c, 0, 0, 0);
}
__device__ __forceinline__ float comb(float a, float b) {
  auto r = __builtin_amdgcn_permlane32_swap(__float_as_uint(a), __float_as_uint(b), false, false);
  return __uint_as_float(r[0]) + __uint_as_float(r[1]);
}
__device__ __forceinline__ u32x4 ld128(__amdgpu_buffer_rsrc_t rs, int off) {
  return __builtin_amdgcn_raw_buffer_load_b128(rs, off, 0, AUX_SC1);
}
__device__ __forceinline__ unsigned ld32(__amdgpu_buffer_rsrc_t rs, int off) {
  return __builtin_amdgcn_raw_buffer_load_b32(rs, off, 0, AUX_SC1);
}
__device__ __forceinline__ void st128(u32x4 v, __amdgpu_buffer_rsrc_t rs, int off) {
  __builtin_amdgcn_raw_buffer_store_b128(v, rs, off, 0, AUX_SC1);
}
__device__ __forceinline__ void st64(u32x2 v, __amdgpu_buffer_rsrc_t rs, int off) {
  __builtin_amdgcn_raw_buffer_store_b64(v, rs, off, 0, AUX_SC1);
}
__device__ __forceinline__ void st32(unsigned v, __amdgpu_buffer_rsrc_t rs, int off) {
  __builtin_amdgcn_raw_buffer_store_b32(v, rs, off, 0, AUX_SC1);
}

constexpr unsigned SPIN_CAP = 1u << 16;
#define STAMP(slot) do { } while (0)
#define STAMP_FLUSH() do { } while (0)
#define LDS_BARRIER() do { asm volatile("s_waitcnt lgkmcnt(0)" ::: "memory"); __builtin_amdgcn_s_barrier(); asm volatile("" ::: "memory"); } while (0)
#define WAIT_VM0() asm volatile("s_waitcnt vmcnt(0)" ::: "memory")
#define WAIT_LGKM0() asm volatile("s_waitcnt lgkmcnt(0)" ::: "memory")
#define PIN_A(x) asm volatile("" : "+a"(x))
#define PIN_V(x) asm volatile("" : "+v"(x))
#define LAS __attribute__((address_space(3)))

__device__ __forceinline__ float rmax3(float a, float b, float c) {
  float r; asm("v_max3_f32 %0, %1, %2, %3\n\ts_nop 1" : "=v"(r) : "v"(a), "v"(b), "v"(c)); return r;
}
__device__ __forceinline__ float rmax2(float a, float b) {
  float r; asm("v_max_f32 %0, %1, %2\n\ts_nop 1" : "=v"(r) : "v"(a), "v"(b)); return r;
}
__device__ __forceinline__ float xmax8(float v) {
  int o = __builtin_amdgcn_update_dpp(0, __float_as_int(v), 0x128, 0xf, 0xf, false);
  return rmax2(v, __int_as_float(o));
}
__device__ __forceinline__ float xadd8(float v) {
  int o = __builtin_amdgcn_update_dpp(0, __float_as_int(v), 0x128, 0xf, 0xf, false);
  return v + __int_as_float(o);
}
__device__ __forceinline__ float xmax16(float v) {
  auto r = __builtin_amdgcn_permlane16_swap(__float_as_uint(v), __float_as_uint(v), false, false);
  return rmax2(__uint_as_float(r[0]), __uint_as_float(r[1]));
}
__device__ __forceinline__ float xadd16(float v) {
  auto r = __builtin_amdgcn_permlane16_swap(__float_as_uint(v), __float_as_uint(v), false, false);
  return __uint_as_float(r[0]) + __uint_as_float(r[1]);
}
__device__ __forceinline__ float xmax32(float v) {
  auto r = __builtin_amdgcn_permlane32_swap(__float_as_uint(v), __float_as_uint(v), false, false);
  return rmax2(__uint_as_float(r[0]), __uint_as_float(r[1]));
}
__device__ __forceinline__ float xadd32(float v) {
  auto r = __builtin_amdgcn_permlane32_swap(__float_as_uint(v), __float_as_uint(v), false, false);
  return __uint_as_float(r[0]) + __uint_as_float(r[1]);
}
__device__ __forceinline__ unsigned umax3(unsigned a, unsigned b, unsigned c) { return max(max(a, b), c); }
__device__ __forceinline__ float blo(unsigned p) { return __uint_as_float(p << 16); }
__device__ __forceinline__ float bhi(unsigned p) { return __uint_as_float(p & 0xFFFF0000u); }

__device__ __forceinline__ void glds16_asm(const void* gsrc, unsigned lds_dst) {
  unsigned keep;
  asm volatile("s_mov_b32 %0, m0\n\ts_mov_b32 m0, %2\n\ts_nop 0\n\tglobal_load_lds_dwordx4 %1, off\n\ts_mov_b32 m0, %0"
               : "=&s"(keep) : "v"(gsrc), "s"(lds_dst) : "memory");
}
template <int R, int CB>
__device__ __forceinline__ void dma_tile(const u8* src, int ldb, unsigned lds, int lane) {
  constexpr int CPR = CB / 16;
  constexpr int N = R * CPR / 64;
#pragma unroll
  for (int q = 0; q < N; ++q) {
    int idx = q * 64 + lane;
    int row = idx / CPR, ch = idx % CPR;
    glds16_asm(src + (size_t)row * ldb + ch * 16, lds + q * 1024);
  }
}
#define WAIT_VM(n) asm volatile("s_waitcnt vmcnt(%0)" ::"n"(n) : "memory")
__device__ __forceinline__ u32x2 tr4(const u8* tile, int rsb, int k0, int n0, int lane) {
  int i = lane & 15;
  const u8* p = tile + (k0 + (i >> 2)) * rsb + (n0 + 4 * (i & 3)) * 2;
  s16x4 v = __builtin_amdgcn_ds_read_tr16_b64_v4i16((LAS s16x4*)p);
  return __builtin_bit_cast(u32x2, v);
}
__device__ __forceinline__ bf16x16 frag_sparse(const u8* blk, int rsb, int n0, int lane) {
  int gb = lane >> 4;
  int kb = 16 * (gb >> 1) + 4 * (gb & 1);
  u32x2 t00 = tr4(blk, rsb, kb, n0, lane), t01 = tr4(blk, rsb, kb + 8, n0, lane);
  u32x2 t10 = tr4(blk, rsb, kb + 32, n0, lane), t11 = tr4(blk, rsb, kb + 40, n0, lane);
  u32x8 d = {t00.x, t01.x, t00.y, t01.y, t10.x, t11.x, t10.y, t11.y};
  return __builtin_bit_cast(bf16x16, d);
}
__device__ __forceinline__ bf16x8 frag_dense(const u8* blk, int rsb, int k0, int n0, int lane) {
  int g = lane >> 4;
  u32x2 a = tr4(blk, rsb, k0 + 8 * g, n0, lane), b = tr4(blk, rsb, k0 + 8 * g + 4, n0, lane);
  u32x4 d = {a.x, a.y, b.x, b.y};
  return __builtin_bit_cast(bf16x8, d);
}

template <int MODE> __device__ __forceinline__ int swz(int row) {
  return MODE == 0 ? 2 * (((row >> 1) & 1) | (((row >> 3) & 1) << 1)) : 2 * ((row & 3) | (((row >> 3) & 1) << 2));
}
__device__ __forceinline__ void glds16_s(const void* sbase, unsigned voff, unsigned lds_dst) {
  unsigned keep;
  asm volatile("s_mov_b32 %0, m0\n\ts_mov_b32 m0, %3\n\ts_nop 0\n\tglobal_load_lds_dwordx4 %1, %2\n\ts_mov_b32 m0, %0"
               : "=&s"(keep) : "v"(voff), "s"(sbase), "s"(lds_dst) : "memory");
}
#define GLDS_I(IMM, sbase, voff, lds_base) \
  asm volatile("s_add_u32 m0, %2, %3\n\ts_nop 0\n\tglobal_load_lds_dwordx4 %0, %1" \
               :: "v"(voff), "s"(sbase), "s"(lds_base), "n"(IMM) : "memory", "scc")
__device__ __forceinline__ u32x2 trr(unsigned addr) {
  s16x4 v = __builtin_amdgcn_ds_read_tr16_b64_v4i16((LAS s16x4*)(size_t)addr);
  return __builtin_bit_cast(u32x2, v);
}
__device__ __forceinline__ bf16x8 trfrag(unsigned a0, unsigned a1) {
  u32x2 a = trr(a0), b = trr(a1);
  u32x4 d = {a.x, a.y, b.x, b.y};
  return __builtin_bit_cast(bf16x8, d);
}
__device__ __forceinline__ float gelu_tanh(float x) {
  float u = 0.7978845608028654f * (x + 0.044715f * x * x * x);
  return x * __builtin_amdgcn_rcpf(1.0f + __builtin_amdgcn_exp2f(-2.8853900817779268f * u));
}

__device__ __forceinline__ float gate8(const u16* xrow, const u16* gw, int part) {
  float sacc = 0.f;
#pragma unroll
  for (int q = 0; q < 10; ++q) {
    u32x4 xv = *(const u32x4*)(xrow + part * 8 + q * 64), gv = *(const u32x4*)(gw + part * 8 + q * 64);
#pragma unroll
    for (int e = 0; e < 4; ++e) {
      sacc += rbf(blo(xv[e]) * blo(gv[e]));
      sacc += rbf(bhi(xv[e]) * bhi(gv[e]));
    }
  }
  sacc += __shfl_xor(sacc, 1); sacc += __shfl_xor(sacc, 2); sacc += __shfl_xor(sacc, 4);
  float z = rbf(sacc);
  return rbf(1.0f / (1.0f + __expf(-z)));
}

template <int SLP = 2>
__device__ __forceinline__ void wait_flag(__amdgpu_buffer_rsrc_t rs, int cw, unsigned v, unsigned* ctl, int lane) {
  for (unsigned spins = 0;; ++spins) {
    asm volatile("" ::: "memory");
    int f = (int)ld32(rs, cw * 4);
    if (__all(f >= (int)v)) break;
    if (spins > SPIN_CAP) { if (lane == 0) atomicAdd(ctl, 1u); break; }
    __builtin_amdgcn_s_sleep(SLP);
  }
  asm volatile("" ::: "memory");
}

#define ADAPT 7
#define AD_DEC 32
#define AD_INC 96
#define D1 3500
#define DV 4000
#define D2 4500
__device__ __forceinline__ void wait_until(unsigned long long t) {
  while (__builtin_amdgcn_s_memtime() < t) __builtin_amdgcn_s_sleep(1);
}
#define START_DLY 8000
#define OUT_SLP 16
#define SCHED() __builtin_amdgcn_sched_barrier(0)
#define PROJ_GEMM(ACC, WF) do { \
    bf16x8 A0_[4], A1_[4]; \
    _Pragma("unroll") for (int ks = 0; ks < 3; ++ks) { \
      A0_[ks] = *(const bf16x8*)(xs + r16 * XS + 32 * ks + 8 * g); A1_[ks] = *(const bf16x8*)(xs + (16 + r16) * XS + 32 * ks + 8 * g); } \
    _Pragma("unroll") for (int ks = 0; ks < 20; ++ks) { \
      if (ks + 3 < 20) { A0_[(ks + 3) & 3] = *(const bf16x8*)(xs + r16 * XS + 32 * (ks + 3) + 8 * g); \
                         A1_[(ks + 3) & 3] = *(const bf16x8*)(xs + (16 + r16) * XS + 32 * (ks + 3) + 8 * g); } \
      SCHED(); \
      _Pragma("unroll") for (int tn = 0; tn < 2; ++tn) { \
        ACC[0][tn] = mf16(A0_[ks & 3], WF[tn][ks], ks == 0 ? Z4 : ACC[0][tn]); \
        ACC[1][tn] = mf16(A1_[ks & 3], WF[tn][ks], ks == 0 ? Z4 : ACC[1][tn]); } \
      SCHED(); \
    } } while (0)

#define LDX(t_) do { _Pragma("unroll") for (int q = 0; q < 10; ++q) { int pc = q * 256 + tid; int row = pc / 80, cc = pc % 80; \
      xp[q] = *(const u32x4*)(p.x + (size_t)((t_) * 32 + row) * 640 + cc * 8); } } while (0)
#define STX() do { _Pragma("unroll") for (int q = 0; q < 10; ++q) { int pc = q * 256 + tid; int row = pc / 80, cc = pc % 80; \
      *(u32x4*)(xs + row * XS + cc * 8) = xp[q]; } } while (0)
constexpr int XS = 648;
constexpr int KS = 136;

struct Params {
  const u16* x; const u16* mem0; const u16* Wq; const u16* Wk; const u16* Wv; const u16* Wo;
  const u16* Wg; const u16* Wu; const u16* Wd; const u16* gw; u8* ws; u16* out;
};
constexpr int NCH = 10;
constexpr int NTASK = NSEG * NCH;

__global__ void __launch_bounds__(256, 1) k_main(Params p) {
  extern __shared__ __attribute__((aligned(16))) u8 smem[];
  u8* ws = p.ws;
  const int tid = threadIdx.x, w = __builtin_amdgcn_readfirstlane(tid >> 6), lane = tid & 63;
  const int r16 = lane & 15, g = lane >> 4, par = r16 >> 3, r = r16 & 7;
  const int idx = par ? 0xEEEE : 0x4444;
#define XMAP 2
  int b;
  {
    const int bx = blockIdx.x, vi = (bx >> 3) * 4 + (bx & 3);
    if (XMAP == 0) b = bx;
    else if (XMAP == 1) {
      if ((bx & 4) == 0) b = vi < 104 ? vi : 232 + (vi - 104);
      else b = 104 + vi;
    } else if (XMAP == 2) {
      if ((bx & 4) == 0) b = vi < 84 ? vi : vi < 108 ? 232 + (vi - 84) : 104 + (vi - 108);
      else b = vi < 20 ? 84 + vi : 124 + (vi - 20);
    } else {
      if ((bx & 4) == 0) b = vi < 32 ? vi : vi < 72 ? 64 + (vi - 32) : vi < 96 ? 232 + (vi - 72) : 104 + (vi - 96);
      else b = vi < 32 ? 32 + vi : 136 + (vi - 32);
    }
  }
  const __amdgpu_buffer_rsrc_t rs = __builtin_amdgcn_make_buffer_rsrc((void*)ws, 0, (int)OFF_END, RSRC_W3);
  unsigned* ctl = (unsigned*)(ws + OFF_CTL);
  const f32x4 Z4 = {0.f, 0.f, 0.f, 0.f};
  unsigned long long tanchor = 0;
  int dly = b < 32 ? D1 : b < 64 ? DV : D2;
#define AD_UPDATE() do { if (s > 0) { if (spins == 0) dly -= AD_DEC; else dly += AD_INC; if (dly < 0) dly = 0; } } while (0)
  const u32x4 PZ = {0xFFFFFFFFu, 0xFFFFFFFFu, 0xFFFFFFFFu, 0xFFFFFFFFu};
  u8* wl = smem + w * 40960;
  const unsigned wla = __builtin_amdgcn_readfirstlane((unsigned)(size_t)(LAS u8*)smem) + w * 40960;

  if (b < 32) {
    const int h = b;
    u16* sq = (u16*)smem;
    unsigned* spl = (unsigned*)(smem + 4608);
    u32x4* smem_a = (u32x4*)(smem + 8192);
    bf16x16 W[4][10];
    {
      const u8* src = (const u8*)((w < 2 ? p.Wq : p.Wk) + h * 128 + 64 * (w & 1));
#pragma unroll
      for (int i = 0; i < 5; ++i) dma_tile<64, 128>(src + (size_t)i * 64 * 8192, 8192, wla + i * 8192, lane);
#pragma unroll
      for (int i = 0; i < 10; ++i) {
        WAIT_VM(8 * (i < 5 ? 4 : 9 - i));
#pragma unroll
        for (int tn = 0; tn < 4; ++tn) W[tn][i] = frag_sparse(wl + (i % 5) * 8192, 128, 16 * tn, lane);
        if (i < 5) {
          WAIT_LGKM0();
          dma_tile<64, 128>(src + (size_t)(i + 5) * 64 * 8192, 8192, wla + i * 8192, lane);
        }
      }
      WAIT_LGKM0();
    }
#pragma unroll
    for (int tn = 0; tn < 4; ++tn)
#pragma unroll
      for (int i = 0; i < (tn < 2 ? 8 : 7); ++i) PIN_A(W[tn][i]);
    LDS_BARRIER();
    const int moff = (int)OFF_MEM + (2 * g + par) * 128 + r * 16;
    for (int s = 0; s < NSEG; ++s) {
      u32x4 kxf[2][4];
      if (w == 0 && s < NSEG - 1) {
        wait_flag(rs, CW_KXF + 16 * h, (unsigned)s, ctl, lane);
        const int kxo = (int)OFF_KX + (s * 32 + h) * 8192 + r16 * 256 + 16 * g;
#pragma unroll
        for (int t2 = 0; t2 < 2; ++t2)
#pragma unroll
          for (int kk = 0; kk < 4; ++kk) kxf[t2][kk] = ld128(rs, kxo + t2 * 4096 + 64 * kk);
      }
      f32x4 acc[4];
      unsigned spins = 0;
      if ((ADAPT & 1) && s > 0) wait_until(tanchor + dly);
      STAMP(0);
      for (;;) {
        asm volatile("" ::: "memory");
        u32x4 a0 = ld128(rs, moff + s * STEPB + w * 1024);
        u32x4 a1 = ld128(rs, moff + s * STEPB + (w + 4) * 1024);
        u32x4 a2 = ld128(rs, w < 2 ? moff + s * STEPB + (w + 8) * 1024 : OOB);
        asm volatile("" ::: "memory");
        smem_a[w * 64 + lane] = a0; smem_a[(w + 4) * 64 + lane] = a1; smem_a[(w + 8) * 64 + lane] = a2;
        unsigned mxv = umax3(umax3(a0.x, a0.z, a1.x), a1.z, umax3(a2.x, a2.z, 0u));
        if (__all(mxv != 0xFFFFFFFFu) || ++spins > SPIN_CAP) break;
      }
      LDS_BARRIER();
#pragma unroll
      for (int tn = 0; tn < 4; ++tn) { acc[tn] = Z4; PIN_V(acc[tn]); }
      {
        u32x4 af[10];
#pragma unroll
        for (int i = 0; i < 10; ++i) af[i] = smem_a[i * 64 + lane];
        SCHED();
#pragma unroll
        for (int i = 0; i < 10; ++i)
#pragma unroll
          for (int tn = 0; tn < 4; ++tn) acc[tn] = smf(__builtin_bit_cast(bf16x8, af[i]), W[tn][i], acc[tn], idx);
      }
      STAMP(1);
      u16* dst = sq + (w >> 1) * (8 * KS) + (4 * (g & 1)) * KS + 64 * (w & 1) + 16 * (lane >> 5) + r16;
#pragma unroll
      for (int pp = 0; pp < 2; ++pp) {
        float v0 = comb(acc[2 * pp][0], acc[2 * pp + 1][0]), v1 = comb(acc[2 * pp][1], acc[2 * pp + 1][1]);
        float v2 = comb(acc[2 * pp][2], acc[2 * pp + 1][2]), v3 = comb(acc[2 * pp][3], acc[2 * pp + 1][3]);
        unsigned p01 = pk2(v0, v1), p23 = pk2(v2, v3);
        dst[32 * pp] = (u16)p01; dst[32 * pp + KS] = (u16)(p01 >> 16);
        dst[32 * pp + 2 * KS] = (u16)p23; dst[32 * pp + 3 * KS] = (u16)(p23 >> 16);
      }
      LDS_BARRIER();
      STAMP(2);
      if (w == 0 && s < NSEG - 1) {
        f32x4 sc[3] = {Z4, Z4, Z4};
        bf16x8 qf[4], kf[4];
#pragma unroll
        for (int kk = 0; kk < 4; ++kk) qf[kk] = *(const bf16x8*)(sq + r * KS + 32 * kk + 8 * g);
#pragma unroll
        for (int kk = 0; kk < 4; ++kk) kf[kk] = *(const bf16x8*)(sq + 8 * KS + r * KS + 32 * kk + 8 * g);
        SCHED();
#pragma unroll
        for (int kk = 0; kk < 4; ++kk) {
          sc[1] = mf16(__builtin_bit_cast(bf16x8, kxf[0][kk]), qf[kk], sc[1]);
          sc[2] = mf16(__builtin_bit_cast(bf16x8, kxf[1][kk]), qf[kk], sc[2]);
          sc[0] = mf16(kf[kk], qf[kk], sc[0]);
        }
        const float NEG = -3.0e38f;
        float v[6];
        v[0] = par ? sc[1][2] : (g < 2 ? sc[0][0] : NEG);
        v[1] = par ? sc[1][3] : (g < 2 ? sc[0][1] : NEG);
        v[2] = par ? sc[2][0] : (g < 2 ? sc[0][2] : NEG);
        v[3] = par ? sc[2][1] : (g < 2 ? sc[0][3] : NEG);
        v[4] = par ? sc[2][2] : sc[1][0];
        v[5] = par ? sc[2][3] : sc[1][1];
        float mx = rmax3(rmax3(v[0], v[1], v[2]), rmax3(v[3], v[4], v[5]), v[0]);
        mx = xmax32(xmax16(xmax8(mx)));
        const float scale = 0.08838834764831845f;
        const float L2E = 1.4426950408889634f;
        float mr = rbf(rbf(mx) * scale) * L2E;
        float e[6];
#pragma unroll
        for (int q = 0; q < 3; ++q) {
          unsigned p1 = pk2(v[2 * q], v[2 * q + 1]);
          unsigned p2 = pk2(blo(p1) * scale, bhi(p1) * scale);
          e[2 * q] = __builtin_amdgcn_exp2f(fmaf(blo(p2), L2E, -mr));
          e[2 * q + 1] = __builtin_amdgcn_exp2f(fmaf(bhi(p2), L2E, -mr));
        }
        float sum = ((e[0] + e[1]) + (e[2] + e[3])) + (e[4] + e[5]);
        sum = xadd32(xadd16(xadd8(sum)));
        float inv = __builtin_amdgcn_rcpf(sum);
        unsigned o0 = pk2(e[0] * inv, e[1] * inv), o1 = pk2(e[2] * inv, e[3] * inv), o2 = pk2(e[4] * inv, e[5] * inv);
        unsigned* pl = spl + r * 4;
        int dA = 32 + (g >> 1) * 32 + 2 * (g & 1), dB = dA + 64;
        int d0 = par ? dA + 1 : 2 * g, d1 = par ? dB : 2 * g + 1, d2 = par ? dB + 1 : dA;
        if (par || g < 2) { pl[d0] = o0; pl[d1] = o1; }
        pl[d2] = o2;
        WAIT_LGKM0();
        if (lane < 40) {
          u32x4 o = *(const u32x4*)(spl + lane * 4);
          o.x = nosent(o.x); o.y = nosent(o.y); o.z = nosent(o.z); o.w = nosent(o.w);
          st128(o, rs, (int)OFF_P + s * STEPB + h * 640 + lane * 16);
          st128(PZ, rs, (int)OFF_P + (s + 2) * STEPB + h * 640 + lane * 16);
        }
      }
      if (w == 1) {
#pragma unroll
        for (int q = 0; q < 2; ++q) {
          int pc = q * 64 + lane, row = pc >> 4, ch = pc & 15;
          u32x4 o = *(const u32x4*)(sq + 8 * KS + row * KS + ch * 8);
          o.x = nosent(o.x); o.y = nosent(o.y); o.z = nosent(o.z); o.w = nosent(o.w);
          st128(o, rs, (int)OFF_KM + s * STEPB + h * 2048 + pc * 16);
          st128(PZ, rs, (int)OFF_KM + (s + 2) * STEPB + h * 2048 + pc * 16);
        }
      }
      STAMP(3);
      LDS_BARRIER();
      AD_UPDATE();
      tanchor = __builtin_amdgcn_s_memtime();
      STAMP_FLUSH();
    }
  } else if (b < 64) {
    const int h = b - 32;
    u16* sq = (u16*)smem;
    u32x4* smem_a = (u32x4*)(smem + 8192);
    bf16x16 Wv[2][10], Wo[10][2];
    {
      const u8* src = (const u8*)(p.Wv + h * 128 + 32 * w);
      const u8* so = (const u8*)(p.Wo + (size_t)h * 128 * 640 + 160 * w);
#pragma unroll
      for (int i = 0; i < 10; ++i) dma_tile<64, 64>(src + (size_t)i * 64 * 8192, 8192, wla + i * 4096, lane);
      WAIT_VM(20);
#pragma unroll
      for (int i = 0; i < 5; ++i)
#pragma unroll
        for (int tn = 0; tn < 2; ++tn) Wv[tn][i] = frag_sparse(wl + i * 4096, 64, 16 * tn, lane);
      WAIT_LGKM0();
      dma_tile<64, 320>(so, 1280, wla, lane);
      WAIT_VM(20);
#pragma unroll
      for (int i = 5; i < 10; ++i)
#pragma unroll
        for (int tn = 0; tn < 2; ++tn) Wv[tn][i] = frag_sparse(wl + i * 4096, 64, 16 * tn, lane);
      WAIT_LGKM0();
      dma_tile<64, 320>(so + (size_t)64 * 1280, 1280, wla + 20480, lane);
      WAIT_VM(20);
#pragma unroll
      for (int t2 = 0; t2 < 10; ++t2) Wo[t2][0] = frag_sparse(wl, 320, 16 * t2, lane);
      WAIT_VM(0);
#pragma unroll
      for (int t2 = 0; t2 < 10; ++t2) Wo[t2][1] = frag_sparse(wl + 20480, 320, 16 * t2, lane);
      WAIT_LGKM0();
    }
#pragma unroll
    for (int tn = 0; tn < 2; ++tn)
#pragma unroll
      for (int i = 0; i < 6; ++i) PIN_A(Wv[tn][i]);
#pragma unroll
    for (int t2 = 0; t2 < 10; ++t2)
#pragma unroll
      for (int i = 0; i < 2; ++i) PIN_A(Wo[t2][i]);
    LDS_BARRIER();
    const int moff = (int)OFF_MEM + (2 * g + par) * 128 + r * 16;
    const int ucol = 160 * w + 16 * (lane >> 5) + r16;
    const int uoff = (int)OFF_UM + h * 128 + (ucol >> 3) * 4096 + (ucol & 7) * 16 + 8 * (g & 1);
    for (int s = 0; s < NSEG; ++s) {
      f32x4 acc[2];
      unsigned spins = 0;
      if ((ADAPT & 2) && s > 0) wait_until(tanchor + dly);
      STAMP(0);
      for (;;) {
        asm volatile("" ::: "memory");
        u32x4 a0 = ld128(rs, moff + s * STEPB + w * 1024);
        u32x4 a1 = ld128(rs, moff + s * STEPB + (w + 4) * 1024);
        u32x4 a2 = ld128(rs, w < 2 ? moff + s * STEPB + (w + 8) * 1024 : OOB);
        asm volatile("" ::: "memory");
        smem_a[w * 64 + lane] = a0; smem_a[(w + 4) * 64 + lane] = a1; smem_a[(w + 8) * 64 + lane] = a2;
        unsigned mxv = umax3(umax3(a0.x, a0.z, a1.x), a1.z, umax3(a2.x, a2.z, 0u));
        if (__all(mxv != 0xFFFFFFFFu) || ++spins > SPIN_CAP) break;
      }
      LDS_BARRIER();
      acc[0] = Z4; acc[1] = Z4; PIN_V(acc[0]); PIN_V(acc[1]);
      {
        u32x4 af[10];
#pragma unroll
        for (int i = 0; i < 10; ++i) af[i] = smem_a[i * 64 + lane];
        SCHED();
#pragma unroll
        for (int i = 0; i < 10; ++i) {
          acc[0] = smf(__builtin_bit_cast(bf16x8, af[i]), Wv[0][i], acc[0], idx);
          acc[1] = smf(__builtin_bit_cast(bf16x8, af[i]), Wv[1][i], acc[1], idx);
        }
      }
      STAMP(1);
      u16* vs = sq + (s & 1) * (8 * KS);
      {
        u16* dst = vs + (4 * (g & 1)) * KS + 32 * w + 16 * (lane >> 5) + r16;
        float v0 = comb(acc[0][0], acc[1][0]), v1 = comb(acc[0][1], acc[1][1]);
        float v2 = comb(acc[0][2], acc[1][2]), v3 = comb(acc[0][3], acc[1][3]);
        unsigned p01 = pk2(v0, v1), p23 = pk2(v2, v3);
        dst[0] = (u16)p01; dst[KS] = (u16)(p01 >> 16); dst[2 * KS] = (u16)p23; dst[3 * KS] = (u16)(p23 >> 16);
      }
      f32x4 c[10];
#pragma unroll
      for (int t2 = 0; t2 < 10; ++t2) { c[t2] = Z4; PIN_V(c[t2]); }
      LDS_BARRIER();
      STAMP(2);
      bf16x8 a0 = *(const bf16x8*)(vs + r * KS + 8 * (2 * g + par));
      bf16x8 a1 = *(const bf16x8*)(vs + r * KS + 64 + 8 * (2 * g + par));
#pragma unroll
      for (int t2 = 0; t2 < 10; ++t2) c[t2] = smf(a0, Wo[t2][0], c[t2], idx);
#pragma unroll
      for (int t2 = 0; t2 < 10; ++t2) c[t2] = smf(a1, Wo[t2][1], c[t2], idx);
#pragma unroll
      for (int pp = 0; pp < 5; ++pp) {
        float v0 = comb(c[2 * pp][0], c[2 * pp + 1][0]), v1 = comb(c[2 * pp][1], c[2 * pp + 1][1]);
        float v2 = comb(c[2 * pp][2], c[2 * pp + 1][2]), v3 = comb(c[2 * pp][3], c[2 * pp + 1][3]);
        u32x2 o = {nosent(pk2(v0, v1)), nosent(pk2(v2, v3))};
        st64(o, rs, uoff + s * STEPB + pp * 4 * 4096);
      }
#pragma unroll
      for (int pp = 0; pp < 5; ++pp) st64(u32x2{0xFFFFFFFFu, 0xFFFFFFFFu}, rs, uoff + (s + 2) * STEPB + pp * 4 * 4096);
      STAMP(3);
      AD_UPDATE();
      tanchor = __builtin_amdgcn_s_memtime();
      STAMP_FLUSH();
    }
  } else if (b < 104) {
    const int c = b - 64;
    float* sred = (float*)smem;
    if (tid < 32) {
      int sh = tid >> 4, row = (tid >> 1) & 7, cp = tid & 1;
      u32x2 v = *(const u32x2*)(p.mem0 + row * 640 + c * 16 + sh * 8 + cp * 4);
      v.x = nosent(v.x); v.y = nosent(v.y);
      st64(v, rs, (int)OFF_MEM + c * 256 + tid * 8);
    }
    const int sl = 2 * c + par;
    for (int s = 0; s < NSEG - 1; ++s) {
      wait_flag(rs, CW_UXF + 16 * (8 * w + (lane & 7)), (unsigned)s, ctl, lane);
      const int uxo = (int)OFF_UX + ((s * 80 + sl) * 32 + 8 * w) * 512 + r * 64 + 16 * g;
      u32x4 bx[8];
#pragma unroll
      for (int i = 0; i < 8; ++i) bx[i] = ld128(rs, uxo + i * 512);
      const int pb = (r16 < 8) ? (int)OFF_P + s * STEPB + (8 * w) * 640 + r * 16 : OOB;
      const int ub = (int)OFF_UM + s * STEPB + sl * 4096 + (8 * w) * 128 + r * 16;
      f32x4 acc0, acc1;
      unsigned spins = 0;
      if ((ADAPT & 4) && s > 0) wait_until(tanchor + dly);
      STAMP(0);
      for (;;) {
        asm volatile("" ::: "memory");
        u32x4 pa[10], um[2];
#pragma unroll
        for (int i = 0; i < 2; ++i) um[i] = ld128(rs, ub + (4 * i + g) * 128);
#pragma unroll
        for (int i = 0; i < 2; ++i) pa[8 + i] = ld128(rs, pb + (4 * i + g) * 640);
#pragma unroll
        for (int i = 0; i < 8; ++i) pa[i] = ld128(rs, pb + i * 640 + 128 + 128 * g);
        asm volatile("" ::: "memory");
        unsigned mxv = umax3(um[0].x, um[0].z, 0u);
        mxv = umax3(mxv, um[1].x, um[1].z);
#pragma unroll
        for (int i = 0; i < 10; ++i) mxv = umax3(mxv, pa[i].x, pa[i].z);
        if (__all(mxv != 0xFFFFFFFFu) || spins > SPIN_CAP) {
          acc0 = mf16(__builtin_bit_cast(bf16x8, pa[8]), __builtin_bit_cast(bf16x8, um[0]), Z4);
          acc1 = mf16(__builtin_bit_cast(bf16x8, pa[9]), __builtin_bit_cast(bf16x8, um[1]), Z4);
#pragma unroll
          for (int i = 0; i < 8; i += 2) {
            acc0 = mf16(__builtin_bit_cast(bf16x8, pa[i]), __builtin_bit_cast(bf16x8, bx[i]), acc0);
            acc1 = mf16(__builtin_bit_cast(bf16x8, pa[i + 1]), __builtin_bit_cast(bf16x8, bx[i + 1]), acc1);
          }
          break;
        }
        ++spins;
      }
      STAMP(1);
      if (g < 2) {
#pragma unroll
        for (int j = 0; j < 4; ++j) sred[w * 128 + par * 64 + (4 * g + j) * 8 + r] = acc0[j] + acc1[j];
      }
      LDS_BARRIER();
      STAMP(2);
      if (w == 0) {
        const f32x4* sr = (const f32x4*)sred;
        if (lane < 32) {
          f32x4 a = sr[lane], b2 = sr[32 + lane], c2 = sr[64 + lane], d2 = sr[96 + lane];
          f32x4 t = (a + b2) + (c2 + d2);
          u32x2 o = {nosent(pk2(t[0], t[1])), nosent(pk2(t[2], t[3]))};
          st64(o, rs, (int)OFF_MEM + (s + 1) * STEPB + c * 256 + lane * 8);
          st64(u32x2{0xFFFFFFFFu, 0xFFFFFFFFu}, rs, (int)OFF_MEM + (s + 3) * STEPB + c * 256 + lane * 8);
        }
      }
      STAMP(3);
      LDS_BARRIER();
      AD_UPDATE();
      tanchor = __builtin_amdgcn_s_memtime();
      STAMP_FLUSH();
    }
  } else if (b < 136) {
    const int h = b - 104;
    bf16x8 Wk[2][20];
    u32x4 xp[10];
    LDX(0);
    asm volatile("" ::: "memory");
    {
      const u8* sk = (const u8*)(p.Wk + h * 128 + 32 * w);
#pragma unroll
      for (int i = 0; i < 10; ++i) dma_tile<64, 64>(sk + (size_t)i * 64 * 8192, 8192, wla + i * 4096, lane);
      WAIT_VM0();
#pragma unroll
      for (int ks = 0; ks < 20; ++ks)
#pragma unroll
        for (int tn = 0; tn < 2; ++tn) Wk[tn][ks] = frag_dense(wl, 64, 32 * ks, 16 * tn, lane);
      WAIT_LGKM0();
    }
#pragma unroll
    for (int tn = 0; tn < 2; ++tn)
#pragma unroll
      for (int ks = 0; ks < 20; ++ks) PIN_A(Wk[tn][ks]);
    LDS_BARRIER();
    u16* xs = (u16*)smem;
    u16* kb = (u16*)(smem + 41472);
    for (int t = 0; t < NSEG; ++t) {
      STAMP(0);
      STX();
      LDS_BARRIER();
      STAMP(1);
      LDX(t + 1 < NSEG ? t + 1 : 0);
      f32x4 acc[2][2];
      PROJ_GEMM(acc, Wk);
#pragma unroll
      for (int m = 0; m < 2; ++m)
#pragma unroll
        for (int tn = 0; tn < 2; ++tn) {
          u16* dst = kb + (8 * g + 4 * m) * KS + 32 * w + 16 * tn + r16;
          unsigned p01 = pk2(acc[m][tn][0], acc[m][tn][1]), p23 = pk2(acc[m][tn][2], acc[m][tn][3]);
          dst[0] = (u16)p01; dst[KS] = (u16)(p01 >> 16); dst[2 * KS] = (u16)p23; dst[3 * KS] = (u16)(p23 >> 16);
        }
      WAIT_VM(10);
      LDS_BARRIER();
      if (tid == 0 && t > 1) st32((unsigned)(t - 1), rs, (CW_KXF + 16 * h) * 4);
      STAMP(2);
#pragma unroll
      for (int q = 0; q < 2; ++q) {
        int pc = q * 256 + tid, row = pc >> 4, ch = pc & 15;
        u32x4 o = *(const u32x4*)(kb + row * KS + ch * 8);
        st128(o, rs, (int)OFF_KX + (t * 32 + h) * 8192 + pc * 16);
      }
      if (t == 0 || t == NSEG - 1) {
        WAIT_VM0();
        LDS_BARRIER();
        if (tid == 0) st32((unsigned)t, rs, (CW_KXF + 16 * h) * 4);
      }
      STAMP(3);
    }
  } else if (b < 168) {
    const int h = b - 136;
    wait_until(__builtin_amdgcn_s_memtime() + START_DLY);
    bf16x8 Wq[2][20];
    {
      const u8* sqp = (const u8*)(p.Wq + h * 128 + 32 * w);
#pragma unroll
      for (int i = 0; i < 10; ++i) dma_tile<64, 64>(sqp + (size_t)i * 64 * 8192, 8192, wla + i * 4096, lane);
      WAIT_VM0();
#pragma unroll
      for (int ks = 0; ks < 20; ++ks)
#pragma unroll
        for (int tn = 0; tn < 2; ++tn) Wq[tn][ks] = frag_dense(wl, 64, 32 * ks, 16 * tn, lane);
      WAIT_LGKM0();
    }
#pragma unroll
    for (int tn = 0; tn < 2; ++tn)
#pragma unroll
      for (int ks = 0; ks < 20; ++ks) PIN_A(Wq[tn][ks]);
    LDS_BARRIER();
    u16* xs = (u16*)smem;
    u16* kb = (u16*)(smem + 41472);
    u16* kmb = (u16*)(smem + 41472 + 8704);
    u8* pxb = smem + 41472 + 8704 + 2176;
    u32x4 xp[10];
    LDX(0);
    const float scale = 0.08838834764831845f;
    const float L2E = 1.4426950408889634f;
    int kflag = -1;
    u16* kxb = (u16*)(smem + 57344);
    {
      wait_flag(rs, CW_KXF + 16 * h, 0u, ctl, lane);
      u32x4 a = ld128(rs, (int)OFF_KX + h * 8192 + tid * 16), c2 = ld128(rs, (int)OFF_KX + h * 8192 + 4096 + tid * 16);
      *(u32x4*)(kxb + (tid >> 4) * KS + (tid & 15) * 8) = a;
      *(u32x4*)(kxb + (16 + (tid >> 4)) * KS + (tid & 15) * 8) = c2;
    }
    for (int u = 0; u < NSEG; ++u) {
      STAMP(0);
      STX();
      LDS_BARRIER();
      STAMP(1);
      const int un = u + 1 < NSEG ? u + 1 : u;
      if (kflag < un) {
        wait_flag(rs, CW_KXF + 16 * h, (unsigned)un, ctl, lane);
        kflag = __builtin_amdgcn_readfirstlane((int)ld32(rs, (CW_KXF + 16 * h) * 4));
      }
      u32x4 kn0 = ld128(rs, (int)OFF_KX + (un * 32 + h) * 8192 + tid * 16);
      u32x4 kn1 = ld128(rs, (int)OFF_KX + (un * 32 + h) * 8192 + 4096 + tid * 16);
      u32x4 k0 = ld128(rs, w == 3 ? (int)OFF_KM + u * STEPB + h * 2048 + lane * 16 : OOB);
      u32x4 k1 = ld128(rs, w == 3 ? (int)OFF_KM + u * STEPB + h * 2048 + 1024 + lane * 16 : OOB);
      LDX(u + 1 < NSEG ? u + 1 : 0);
      f32x4 acc[2][2];
      PROJ_GEMM(acc, Wq);
#pragma unroll
      for (int m = 0; m < 2; ++m)
#pragma unroll
        for (int tn = 0; tn < 2; ++tn) {
          u16* dst = kb + (16 * m + 4 * g) * KS + 32 * w + 16 * tn + r16;
          unsigned p01 = pk2(acc[m][tn][0], acc[m][tn][1]), p23 = pk2(acc[m][tn][2], acc[m][tn][3]);
          dst[0] = (u16)p01; dst[KS] = (u16)(p01 >> 16); dst[2 * KS] = (u16)p23; dst[3 * KS] = (u16)(p23 >> 16);
        }
      WAIT_VM(14);
      LDS_BARRIER();
      if (tid == 0 && u > 0 && u < NSEG - 1) st32((unsigned)(u - 1), rs, (CW_PXF + 16 * h) * 4);
      if (w == 3) {
        unsigned spins = 0;
        for (;;) {
          unsigned mxv = umax3(umax3(k0.x, k0.z, k1.x), k1.z, 0u);
          if (__all(mxv != 0xFFFFFFFFu) || spins > SPIN_CAP) {
            *(u32x4*)(kmb + (lane >> 4) * KS + (lane & 15) * 8) = k0;
            *(u32x4*)(kmb + (4 + (lane >> 4)) * KS + (lane & 15) * 8) = k1;
            break;
          }
          if (spins) __builtin_amdgcn_s_sleep(6);
          ++spins;
          asm volatile("" ::: "memory");
          k0 = ld128(rs, (int)OFF_KM + u * STEPB + h * 2048 + lane * 16);
          k1 = ld128(rs, (int)OFF_KM + u * STEPB + h * 2048 + 1024 + lane * 16);
          asm volatile("" ::: "memory");
        }
      }
      LDS_BARRIER();
      STAMP(2);
      {
        f32x4 sc[3];
        bf16x8 qf[4], kf[4];
#pragma unroll
        for (int kk = 0; kk < 4; ++kk) qf[kk] = *(const bf16x8*)(kb + (8 * w + r) * KS + 32 * kk + 8 * g);
#pragma unroll
        for (int kk = 0; kk < 4; ++kk) kf[kk] = *(const bf16x8*)(kmb + r * KS + 32 * kk + 8 * g);
        bf16x8 kxf[2][4];
        const u16* kxc = kxb + (u & 1) * (32 * KS);
#pragma unroll
        for (int t2 = 0; t2 < 2; ++t2)
#pragma unroll
          for (int kk = 0; kk < 4; ++kk) kxf[t2][kk] = *(const bf16x8*)(kxc + (16 * t2 + r16) * KS + 32 * kk + 8 * g);
        SCHED();
#pragma unroll
        for (int kk = 0; kk < 4; ++kk) {
          sc[1] = mf16(kxf[0][kk], qf[kk], kk == 0 ? Z4 : sc[1]);
          sc[2] = mf16(kxf[1][kk], qf[kk], kk == 0 ? Z4 : sc[2]);
          sc[0] = mf16(kf[kk], qf[kk], kk == 0 ? Z4 : sc[0]);
        }
        const int tok = 8 * w + r;
        const int tkA = 16 * (g & 1) + 4 * (g >> 1), tkB = tkA + 8;
        float v[6];
        int tk[6];
        v[0] = par ? sc[1][2] : sc[0][0]; tk[0] = par ? tkA + 2 : -1;
        v[1] = par ? sc[1][3] : sc[0][1]; tk[1] = par ? tkA + 3 : -1;
        v[2] = par ? sc[2][0] : sc[0][2]; tk[2] = par ? tkB : -1;
        v[3] = par ? sc[2][1] : sc[0][3]; tk[3] = par ? tkB + 1 : -1;
        v[4] = par ? sc[2][2] : sc[1][0]; tk[4] = par ? tkB + 2 : tkA;
        v[5] = par ? sc[2][3] : sc[1][1]; tk[5] = par ? tkB + 3 : tkA + 1;
        const bool nomem = !par && g >= 2;
        float sv[6];
#pragma unroll
        for (int q = 0; q < 3; ++q) {
          unsigned p1 = pk2(v[2 * q], v[2 * q + 1]);
          unsigned p2 = pk2(blo(p1) * scale, bhi(p1) * scale);
          sv[2 * q] = blo(p2); sv[2 * q + 1] = bhi(p2);
        }
        const float MASKV = rbf(-1.0e9f);
#pragma unroll
        for (int q = 0; q < 6; ++q) {
          if (tk[q] > tok) sv[q] = MASKV;
          if (q < 4 && nomem) sv[q] = -3.0e38f;
        }
        float mx = fmaxf(fmaxf(fmaxf(sv[0], sv[1]), fmaxf(sv[2], sv[3])), fmaxf(sv[4], sv[5]));
        mx = xmax32(xmax16(xmax8(mx)));
        float e[6];
#pragma unroll
        for (int q = 0; q < 6; ++q) e[q] = __builtin_amdgcn_exp2f((sv[q] - mx) * L2E);
        float sum = ((e[0] + e[1]) + (e[2] + e[3])) + (e[4] + e[5]);
        sum = xadd32(xadd16(xadd8(sum)));
        float inv = __builtin_amdgcn_rcpf(sum);
        unsigned o0 = pk2(e[0] * inv, e[1] * inv), o1 = pk2(e[2] * inv, e[3] * inv), o2 = pk2(e[4] * inv, e[5] * inv);
        unsigned* pl = (unsigned*)pxb + tok * 4;
        int dA = 128 + (g >> 1) * 128 + 2 * (g & 1), dB = dA + 256;
        int d0 = par ? dA + 1 : 2 * g, d1 = par ? dB : 2 * g + 1, d2 = par ? dB + 1 : dA;
        if (!nomem) { pl[d0] = o0; pl[d1] = o1; }
        pl[d2] = o2;
      }
      {
        u16* kxn = kxb + ((u + 1) & 1) * (32 * KS);
        *(u32x4*)(kxn + (tid >> 4) * KS + (tid & 15) * 8) = kn0;
        *(u32x4*)(kxn + (16 + (tid >> 4)) * KS + (tid & 15) * 8) = kn1;
      }
      LDS_BARRIER();
      if (tid < 160) {
        u32x4 o = *(const u32x4*)(pxb + tid * 16);
        st128(o, rs, (int)OFF_PX + (u * 32 + h) * 2560 + tid * 16);
      }
      if (u >= NSEG - 2) {
        WAIT_VM0();
        LDS_BARRIER();
        if (tid == 0) st32((unsigned)u, rs, (CW_PXF + 16 * h) * 4);
      }
    }
  } else if (b < 200) {
    const int h = b - 168;
    bf16x8 Wv[2][20], Wo[10][4];
    u32x4 xp[10];
    LDX(0);
    asm volatile("" ::: "memory");
    {
      const u8* sv = (const u8*)(p.Wv + h * 128 + 32 * w);
      const u8* so = (const u8*)(p.Wo + (size_t)h * 128 * 640 + 160 * w);
#pragma unroll
      for (int i = 0; i < 10; ++i) dma_tile<64, 64>(sv + (size_t)i * 64 * 8192, 8192, wla + i * 4096, lane);
      WAIT_VM(20);
#pragma unroll
      for (int ks = 0; ks < 10; ++ks)
#pragma unroll
        for (int tn = 0; tn < 2; ++tn) Wv[tn][ks] = frag_dense(wl, 64, 32 * ks, 16 * tn, lane);
      WAIT_LGKM0();
      dma_tile<64, 320>(so, 1280, wla, lane);
      WAIT_VM(20);
#pragma unroll
      for (int ks = 10; ks < 20; ++ks)
#pragma unroll
        for (int tn = 0; tn < 2; ++tn) Wv[tn][ks] = frag_dense(wl, 64, 32 * ks, 16 * tn, lane);
      WAIT_LGKM0();
      dma_tile<64, 320>(so + (size_t)64 * 1280, 1280, wla + 20480, lane);
      WAIT_VM(20);
#pragma unroll
      for (int ks = 0; ks < 2; ++ks)
#pragma unroll
        for (int t2 = 0; t2 < 10; ++t2) Wo[t2][ks] = frag_dense(wl, 320, 32 * ks, 16 * t2, lane);
      WAIT_VM(0);
#pragma unroll
      for (int ks = 2; ks < 4; ++ks)
#pragma unroll
        for (int t2 = 0; t2 < 10; ++t2) Wo[t2][ks] = frag_dense(wl, 320, 32 * ks, 16 * t2, lane);
      WAIT_LGKM0();
    }
#pragma unroll
    for (int t2 = 0; t2 < 10; ++t2)
#pragma unroll
      for (int ks = 0; ks < 4; ++ks) PIN_A(Wo[t2][ks]);
#pragma unroll
    for (int tn = 0; tn < 2; ++tn)
#pragma unroll
      for (int ks = 0; ks < 8; ++ks) PIN_A(Wv[tn][ks]);
    LDS_BARRIER();
    u16* xs = (u16*)smem;
    u16* vb = (u16*)(smem + 41472);
    for (int t = 0; t < NSEG; ++t) {
      STX();
      LDS_BARRIER();
      LDX(t + 1 < NSEG ? t + 1 : 0);
      f32x4 acc[2][2];
      PROJ_GEMM(acc, Wv);
#pragma unroll
      for (int m = 0; m < 2; ++m)
#pragma unroll
        for (int tn = 0; tn < 2; ++tn) {
          u16* dst = vb + (16 * m + 4 * g) * KS + 32 * w + 16 * tn + r16;
          unsigned p01 = pk2(acc[m][tn][0], acc[m][tn][1]), p23 = pk2(acc[m][tn][2], acc[m][tn][3]);
          dst[0] = (u16)p01; dst[KS] = (u16)(p01 >> 16); dst[2 * KS] = (u16)p23; dst[3 * KS] = (u16)(p23 >> 16);
        }
      WAIT_VM(10);
      LDS_BARRIER();
      if (tid == 0 && t > 1) st32((unsigned)(t - 1), rs, (CW_UXF + 16 * h) * 4);
      bf16x8 va[2][4];
#pragma unroll
      for (int m = 0; m < 2; ++m)
#pragma unroll
        for (int ks = 0; ks < 4; ++ks) va[m][ks] = *(const bf16x8*)(vb + (16 * m + r16) * KS + 32 * ks + 8 * g);
#pragma unroll
      for (int t2 = 0; t2 < 10; ++t2) {
        f32x4 c0 = Z4, c1 = Z4;
#pragma unroll
        for (int ks = 0; ks < 4; ++ks) { c0 = mf16(va[0][ks], Wo[t2][ks], c0); c1 = mf16(va[1][ks], Wo[t2][ks], c1); }
        int col = 160 * w + 16 * t2 + r16;
        u32x4 o = {pk2(c0[0], c0[1]), pk2(c0[2], c0[3]), pk2(c1[0], c1[1]), pk2(c1[2], c1[3])};
        st128(o, rs, (int)OFF_UX + ((t * 80 + (col >> 3)) * 32 + h) * 512 + (col & 7) * 64 + 16 * g);
      }
      if (t == 0 || t == NSEG - 1) {
        WAIT_VM0();
        LDS_BARRIER();
        if (tid == 0) st32((unsigned)t, rs, (CW_UXF + 16 * h) * 4);
      }
    }
  } else if (b < 232) {
    const int f = b - 200;
    wait_until(__builtin_amdgcn_s_memtime() + START_DLY);
    u8* slab0 = smem;
    const unsigned ldsb = __builtin_amdgcn_readfirstlane((unsigned)(size_t)(LAS u8*)smem);
    u8* wdb = smem + 65536;
    u16* hb = (u16*)(smem + 65536 + 81920);
    bf16x8 A[20];
#pragma unroll
    for (int ks = 0; ks < 20; ++ks)
      A[ks] = *(const bf16x8*)(p.x + (size_t)(64 * f + 16 * w + r16) * 640 + 32 * ks + 8 * g);
#pragma unroll
    for (int ks = 0; ks < 16; ++ks) PIN_A(A[ks]);
    f32x4 acc2[4][10];
#pragma unroll
    for (int m = 0; m < 4; ++m)
#pragma unroll
      for (int n = 0; n < 10; ++n) acc2[m][n] = Z4;
    const int i16 = lane & 15;
    unsigned so[2], tb[4], wo2[2], tw;
#pragma unroll
    for (int q = 0; q < 2; ++q) {
      int row = 8 * (2 * w + q) + (lane >> 3);
      so[q] = (unsigned)(row * (DF * 2) + (((lane & 7) ^ swz<0>(row)) << 4));
      wo2[q] = (unsigned)((lane >> 4) * 1280 + (((lane & 15) ^ (2 * ((lane >> 4) | (q << 2)))) << 4));
    }
#pragma unroll
    for (int q = 0; q < 4; ++q) {
      int s0 = swz<0>(8 * g + (i16 >> 2));
      tb[q] = ldsb + (unsigned)((8 * g + (i16 >> 2)) * 128 + (((2 * q + ((i16 & 3) >> 1)) ^ s0) << 4) + (i16 & 1) * 8);
    }
    const int s1 = 2 * ((i16 >> 2) | ((g & 1) << 2));
    tw = ldsb + 65536 + (unsigned)((8 * g + (i16 >> 2)) * 256 + ((i16 & 3) >> 1) * 16 + (i16 & 1) * 8);
#define FFN_SLAB(c_, si_, slot_) do { \
      const unsigned uo_ = (unsigned)((64 * (si_)) * (DF * 2) + 128 * (c_)); \
      const unsigned v0_ = so[0] + uo_, v1_ = so[1] + uo_; \
      const unsigned bb_ = lw2 + (slot_) * 16384; \
      GLDS_I(0, p.Wg, v0_, bb_); GLDS_I(1024, p.Wg, v1_, bb_); \
      GLDS_I(8192, p.Wu, v0_, bb_); GLDS_I(9216, p.Wu, v1_, bb_); } while (0)
#define FFN_WD(si_) ((si_) < 0 ? 0 : (si_) < 6 ? 3 : (si_) == 6 ? 2 : 0)
    WAIT_VM0();
    int wq = w;
    const unsigned lw2 = ldsb + 2 * w * 1024;
    const unsigned lwd = ldsb + 65536 + w * 4096;
    const unsigned wdw = (unsigned)(w * 4 * 5120);
    const int c0 = f % 27;
    unsigned m0keep;
    asm volatile("s_mov_b32 %0, m0" : "=s"(m0keep) :: "memory");
    FFN_SLAB(c0, 0, 0); FFN_SLAB(c0, 1, 1); FFN_SLAB(c0, 2, 2);
#pragma unroll 1
    for (int ci = 0; ci < 27; ++ci) {
      const int c = c0 + ci < 27 ? c0 + ci : c0 + ci - 27;
      const int cn = c + 1 < 27 ? c + 1 : 0;
      const int slotb = (ci * 10) & 3;
      const unsigned wd0 = wo2[0] + wdw + (unsigned)(c * 64 * 1280), wd1 = wo2[1] + wdw + (unsigned)(c * 64 * 1280);
      f32x4 ag[4], au[4];
#pragma unroll
      for (int si = 0; si < 10; ++si) {
        asm volatile("s_waitcnt vmcnt(%0)" ::"n"(FFN_WD(si - 3) + 4 + FFN_WD(si - 2) + 4 + FFN_WD(si - 1)) : "memory");
        LDS_BARRIER();
        if (si + 3 < 10) FFN_SLAB(c, si + 3, (slotb + si + 3) & 3);
        else FFN_SLAB(cn, si + 3 - 10, (slotb + si + 3) & 3);
#define FFN_WDP(j_) do { if ((j_) < 20 && (j_) < 3 * si + FFN_WD(si)) { \
          const unsigned vo_ = ((((j_) >> 1) & 1) ? wd1 : wd0) + (unsigned)(((j_) & 3) * 5120 + ((j_) >> 2) * 256); \
          GLDS_I((((j_) >> 2) * 16384 + ((j_) & 3) * 1024), p.Wd, vo_, lwd); } } while (0)
        FFN_WDP(3 * si); FFN_WDP(3 * si + 1); FFN_WDP(3 * si + 2);
        const unsigned sof = ((slotb + si) & 3) * 16384;
#pragma unroll
        for (int k2 = 0; k2 < 2; ++k2) {
          const int ks = si * 2 + k2;
          bf16x8 bg[4], bu[4];
#pragma unroll
          for (int ft = 0; ft < 4; ++ft) {
            const unsigned t0 = tb[ft] + sof + k2 * 4096;
            bg[ft] = trfrag(t0, t0 + 512);
            bu[ft] = trfrag(t0 + 8192, t0 + 8192 + 512);
          }
#pragma unroll
          for (int ft = 0; ft < 4; ++ft) {
            ag[ft] = mf16(A[ks], bg[ft], ks == 0 ? Z4 : ag[ft]);
            au[ft] = mf16(A[ks], bu[ft], ks == 0 ? Z4 : au[ft]);
          }
        }
      }
#pragma unroll
      for (int ft = 0; ft < 4; ++ft)
#pragma unroll
        for (int j = 0; j < 4; ++j) {
          float gv = rbf(ag[ft][j]), uv = rbf(au[ft][j]);
          hb[(16 * w + 4 * g + j) * 72 + 16 * ft + r16] = f2bf(rbf(gelu_tanh(gv)) * uv);
        }
      asm volatile("s_waitcnt vmcnt(12)" ::: "memory");
      LDS_BARRIER();
#pragma unroll
      for (int k2 = 0; k2 < 2; ++k2) {
        bf16x8 ha[4];
#pragma unroll
        for (int m = 0; m < 4; ++m) ha[m] = *(const bf16x8*)(hb + (16 * m + r16) * 72 + 32 * k2 + 8 * g);
#pragma unroll
        for (int n = 0; n < 10; ++n) {
          const int nc = 160 * wq + 16 * n;
          const unsigned ta = tw + (nc >> 7) * 16384 + ((((nc & 127) >> 3) ^ s1) << 4) + k2 * 8192;
          bf16x8 bd = trfrag(ta, ta + 1024);
#pragma unroll
          for (int m = 0; m < 4; ++m) acc2[m][n] = mf16(ha[m], bd, acc2[m][n]);
        }
      }
      LDS_BARRIER();
    }
    asm volatile("s_mov_b32 m0, %0" :: "s"(m0keep) : "memory");
    WAIT_VM0();
    LDS_BARRIER();
    {
      u16* ob = (u16*)smem;
#pragma unroll
      for (int m = 0; m < 4; ++m)
#pragma unroll
        for (int n = 0; n < 10; ++n)
#pragma unroll
          for (int j = 0; j < 4; ++j) ob[(16 * m + 4 * g + j) * XS + 160 * w + 16 * n + r16] = f2bf(acc2[m][n][j]);
      LDS_BARRIER();
      for (int pc = tid; pc < 64 * 80; pc += 256) {
        int row = pc / 80, cc = pc % 80;
        u32x4 o = *(const u32x4*)(ob + row * XS + cc * 8);
        st128(o, rs, (int)OFF_BO + (64 * f + row) * 1280 + cc * 16);
      }
      WAIT_VM0();
      LDS_BARRIER();
      unsigned* smx = (unsigned*)(smem + 90112);
      if (tid == 0) {
        st32(0u, rs, (CW_FF + 16 * f) * 4);
        *smx = atomicAnd(ctl + CW_MIX + 16 * f, 0x7FFFFFFFu);
      }
      LDS_BARRIER();
      const unsigned done = ~(*smx) & 0xFFFFFu;
      if (done) {
        const int row = tid >> 3, c8 = tid & 7;
        float omg[2];
#pragma unroll
        for (int hs = 0; hs < 2; ++hs) omg[hs] = rbf(1.0f - gate8(p.x + (size_t)(64 * f + 32 * hs + row) * 640, p.gw, c8));
        for (int k = 0; k < 20; ++k) {
          if (!((done >> k) & 1)) continue;
          const int hs = k / NCH, cj = k % NCH;
          const int tr = 32 * hs + row, cc = cj * 64 + c8 * 8;
          u32x4 ga = ld128(rs, (int)OFF_GA + ((64 * f + tr) * 640 + cc) * 2);
          u32x4 bv = *(const u32x4*)(ob + tr * XS + cc);
          const float om = hs ? omg[1] : omg[0];
          u32x4 o;
#pragma unroll
          for (int e = 0; e < 4; ++e)
            o[e] = pk2(blo(ga[e]) + rbf(om * blo(bv[e])), bhi(ga[e]) + rbf(om * bhi(bv[e])));
          *(u32x4*)(p.out + (size_t)(64 * f + tr) * 640 + cc) = o;
        }
      }
    }
  }

  if (b >= 104 && !(b >= 136 && b < 168)) {
    int tid = threadIdx.x;
    asm volatile("" : "+v"(tid));
    const int lane = tid & 63, r16 = lane & 15, g = lane >> 4;
    float* sg = (float*)smem;
    int* stask = (int*)(smem + 256);
    for (;;) {
      LDS_BARRIER();
      if (tid == 0) *stask = (int)atomicAdd(ctl + CW_QUEUE, 1u) + 1;
      LDS_BARRIER();
      const int task = *stask;
      if (task >= NTASK) break;
      const int t = task / NCH, cj = task % NCH;
      STAMP(0);
      {
        float gv = gate8(p.x + (size_t)(t * 32 + (tid >> 3)) * 640, p.gw, tid & 7);
        if ((tid & 7) == 0) sg[tid >> 3] = gv;
      }
      if (w == 0) wait_flag<OUT_SLP>(rs, CW_UXF + 16 * (lane & 31), (unsigned)t, ctl, lane);
      LDS_BARRIER();
      STAMP(1);
      const int col = cj * 64 + w * 16 + r16;
      const int uxo = (int)OFF_UX + (t * 80 + (col >> 3)) * 32 * 512 + (col & 7) * 64 + 16 * g;
      const int umo = (int)OFF_UM + t * STEPB + (col >> 3) * 4096 + (col & 7) * 16;
      u32x4 bx[32];
#pragma unroll
      for (int h = 0; h < 32; ++h) bx[h] = ld128(rs, uxo + h * 512);
      if (w == 0) wait_flag<OUT_SLP>(rs, CW_PXF + 16 * (lane & 31), (unsigned)t, ctl, lane);
      LDS_BARRIER();
      u8* pxl = smem + 1024;
      {
        const u8* pxg = ws + OFF_PX + (size_t)t * 32 * 2560;
#pragma unroll
        for (int q = 0; q < 20; ++q)
          __builtin_amdgcn_global_load_lds((const unsigned*)(pxg + (20 * w + q) * 1024 + lane * 16), (unsigned*)(pxl + (20 * w + q) * 1024), 16, 0, AUX_SC1);
      }
      u32x4 um[8];
      {
        unsigned spins = 0;
        for (;;) {
          asm volatile("" ::: "memory");
          unsigned mxv = 0;
#pragma unroll
          for (int hq = 0; hq < 8; ++hq) um[hq] = ld128(rs, umo + (4 * hq + g) * 128);
#pragma unroll
          for (int hq = 0; hq < 8; ++hq) mxv = umax3(mxv, um[hq].x, um[hq].z);
          if (__all(mxv != 0xFFFFFFFFu) || spins > SPIN_CAP) break;
          __builtin_amdgcn_s_sleep(4);
          ++spins;
        }
      }
      WAIT_VM0();
      LDS_BARRIER();
      f32x4 acc0 = Z4, acc1 = Z4;
#pragma unroll
      for (int h = 0; h < 32; ++h) {
        bf16x8 a0 = *(const bf16x8*)(pxl + h * 2560 + 512 + g * 512 + r16 * 16);
        bf16x8 a1 = *(const bf16x8*)(pxl + h * 2560 + 512 + g * 512 + 256 + r16 * 16);
        acc0 = mf16(a0, __builtin_bit_cast(bf16x8, bx[h]), acc0);
        acc1 = mf16(a1, __builtin_bit_cast(bf16x8, bx[h]), acc1);
      }
#pragma unroll
      for (int hq = 0; hq < 8; ++hq) {
        bf16x8 a0 = *(const bf16x8*)(pxl + (4 * hq + g) * 2560 + r16 * 16);
        bf16x8 a1 = *(const bf16x8*)(pxl + (4 * hq + g) * 2560 + 256 + r16 * 16);
        acc0 = mf16(a0, __builtin_bit_cast(bf16x8, um[hq]), acc0);
        acc1 = mf16(a1, __builtin_bit_cast(bf16x8, um[hq]), acc1);
      }
      STAMP(2);
      u16* rt = (u16*)(smem + 512 + 83968);
#pragma unroll
      for (int i = 0; i < 2; ++i)
#pragma unroll
        for (int q = 0; q < 4; ++q) {
          int tok = 16 * i + 4 * g + q;
          rt[tok * 72 + w * 16 + r16] = f2bf(sg[tok] * rbf(i == 0 ? acc0[q] : acc1[q]));
        }
      if (tid == 0) stask[1] = (int)ld32(rs, (CW_FF + 16 * (t >> 1)) * 4);
      LDS_BARRIER();
      {
        const int row = tid >> 3, c8 = tid & 7;
        const int go = ((t * 32 + row) * 640 + cj * 64 + c8 * 8) * 2;
        u32x4 ga = *(const u32x4*)(rt + row * 72 + c8 * 8);
        bool mix = stask[1] >= 0;
        if (!mix) {
          st128(ga, rs, (int)OFF_GA + go);
          WAIT_VM0();
          LDS_BARRIER();
          if (tid == 0) stask[2] = (int)atomicAnd(ctl + CW_MIX + 16 * (t >> 1), ~(1u << ((t & 1) * NCH + cj)));
          LDS_BARRIER();
          mix = stask[2] >= 0;
        }
        if (mix) {
          u32x4 bv = ld128(rs, (int)OFF_BO + go);
          const float om = rbf(1.0f - sg[row]);
          u32x4 o;
#pragma unroll
          for (int e = 0; e < 4; ++e)
            o[e] = pk2(blo(ga[e]) + rbf(om * blo(bv[e])), bhi(ga[e]) + rbf(om * bhi(bv[e])));
          *(u32x4*)((u8*)p.out + go) = o;
        }
      }
    }
  }
}

extern "C" void kernel_launch(void* const* d_in, const int* in_sizes, int n_in, void* d_out, int out_size, void* d_ws,
                              size_t ws_size, hipStream_t stream) {
  u8* ws = (u8*)d_ws;
  Params p;
  p.x = (const u16*)d_in[0]; p.mem0 = (const u16*)d_in[1]; p.Wq = (const u16*)d_in[2]; p.Wk = (const u16*)d_in[3];
  p.Wv = (const u16*)d_in[4]; p.Wo = (const u16*)d_in[5];
  p.Wg = (const u16*)d_in[8]; p.Wu = (const u16*)d_in[9]; p.Wd = (const u16*)d_in[10]; p.gw = (const u16*)d_in[11];
  p.ws = ws; p.out = (u16*)d_out;

  (void)hipFuncSetAttribute((const void*)k_main, hipFuncAttributeMaxDynamicSharedMemorySize, 163840);
  (void)hipMemsetAsync(ws, 0xFF, INIT_BYTES, stream);
  k_main<<<256, 256, 163840, stream>>>(p);
}
```

```cpp
#include <hip/hip_runtime.h>
#include <hip/hip_bf16.h>
#include <stdint.h>

typedef __bf16 bf16x8 __attribute__((ext_vector_type(8)));
typedef __bf16 bf16x16 __attribute__((ext_vector_type(16)));
typedef __bf16 bf16x2 __attribute__((ext_vector_type(2)));
typedef float f32x2 __attribute__((ext_vector_type(2)));
typedef float f32x4 __attribute__((ext_vector_type(4)));
typedef unsigned u32x4 __attribute__((ext_vector_type(4)));
typedef unsigned u32x2 __attribute__((ext_vector_type(2)));
typedef unsigned u32x8 __attribute__((ext_vector_type(8)));
typedef short s16x4 __attribute__((ext_vector_type(4)));
typedef unsigned short u16;
typedef unsigned char u8;

constexpr int SEQ = 2048, DM = 640, NSEG = 64, DF = 1728;

constexpr size_t OFF_CTL = 0;
constexpr size_t CTL_BYTES = 16384;
constexpr int STEPB = 10240 + 20480 + 65536 + 327680;
constexpr size_t OFF_MEM = CTL_BYTES;
constexpr size_t OFF_P = OFF_MEM + 10240;
constexpr size_t OFF_KM = OFF_P + 20480;
constexpr size_t OFF_UM = OFF_KM + 65536;
constexpr size_t OFF_SENT_END = CTL_BYTES + (size_t)66 * STEPB;
constexpr size_t INIT_BYTES = CTL_BYTES + (size_t)3 * STEPB;
constexpr size_t OFF_KX = OFF_SENT_END;
constexpr size_t OFF_UX = OFF_KX + (size_t)SEQ * 4096 * 2;
constexpr size_t OFF_PX = OFF_UX + (size_t)64 * 80 * 32 * 8 * 32 * 2;
constexpr size_t OFF_BO = OFF_PX + (size_t)64 * 32 * 2560;
constexpr size_t OFF_GA = OFF_BO + (size_t)SEQ * DM * 2;
constexpr size_t OFF_END = OFF_GA + (size_t)SEQ * DM * 2;
constexpr int CW_TMO = 0, CW_QUEUE = 32, CW_KXF = 64, CW_UXF = 1024, CW_PXF = 2048, CW_FF = 3072, CW_MIX = 3584;

#define AUX_SC1 16
#define RSRC_W3 0x00020000
#define OOB 0x7FFF0000

__device__ __forceinline__ float bf2f(u16 v) { return __uint_as_float(((unsigned)v) << 16); }
__device__ __forceinline__ u16 f2bf(float f) { __bf16 b = (__bf16)f; return __builtin_bit_cast(u16, b); }
__device__ __forceinline__ float rbf(float f) { return (float)((__bf16)f); }
__device__ __forceinline__ unsigned pk2(float a, float b) {
  f32x2 v = {a, b};
  return __builtin_bit_cast(unsigned, __builtin_convertvector(v, bf16x2));
}
__device__ __forceinline__ unsigned nosent(unsigned x) { return x < 0xFFFFFFFEu ? x : 0xFFFFFFFEu; }
__device__ __forceinline__ f32x4 smf(bf16x8 a, bf16x16 b, f32x4 c, int idx) {
  return __builtin_amdgcn_smfmac_f32_16x16x64_bf16(a, b, c, idx, 0, 0);
}
__device__ __forceinline__ f32x4 mf16(bf16x8 a, bf16x8 b, f32x4 c) {
  return __builtin_amdgcn_mfma_f32_16x16x32_bf16(a, b, c, 0, 0, 0);
}
__device__ __forceinline__ float comb(float a, float b) {
  auto r = __builtin_amdgcn_permlane32_swap(__float_as_uint(a), __float_as_uint(b), false, false);
  return __uint_as_float(r[0]) + __uint_as_float(r[1]);
}
__device__ __forceinline__ u32x4 ld128(__amdgpu_buffer_rsrc_t rs, int off) {
  return __builtin_amdgcn_raw_buffer_load_b128(rs, off, 0, AUX_SC1);
}
__device__ __forceinline__ unsigned ld32(__amdgpu_buffer_rsrc_t rs, int off) {
  return __builtin_amdgcn_raw_buffer_load_b32(rs, off, 0, AUX_SC1);
}
__device__ __forceinline__ void st128(u32x4 v, __amdgpu_buffer_rsrc_t rs, int off) {
  __builtin_amdgcn_raw_buffer_store_b128(v, rs, off, 0, AUX_SC1);
}
__device__ __forceinline__ void st64(u32x2 v, __amdgpu_buffer_rsrc_t rs, int off) {
  __builtin_amdgcn_raw_buffer_store_b64(v, rs, off, 0, AUX_SC1);
}
__device__ __forceinline__ void st32(unsigned v, __amdgpu_buffer_rsrc_t rs, int off) {
  __builtin_amdgcn_raw_buffer_store_b32(v, rs, off, 0, AUX_SC1);
}

constexpr unsigned SPIN_CAP = 1u << 16;
#define STAMP(slot) do { } while (0)
#define STAMP_FLUSH() do { } while (0)
#define LDS_BARRIER() do { asm volatile("s_waitcnt lgkmcnt(0)" ::: "memory"); __builtin_amdgcn_s_barrier(); asm volatile("" ::: "memory"); } while (0)
#define WAIT_VM0() asm volatile("s_waitcnt vmcnt(0)" ::: "memory")
#define WAIT_LGKM0() asm volatile("s_waitcnt lgkmcnt(0)" ::: "memory")
#define PIN_A(x) asm volatile("" : "+a"(x))
#define PIN_V(x) asm volatile("" : "+v"(x))
#define LAS __attribute__((address_space(3)))

__device__ __forceinline__ float rmax3(float a, float b, float c) {
  float r; asm("v_max3_f32 %0, %1, %2, %3\n\ts_nop 1" : "=v"(r) : "v"(a), "v"(b), "v"(c)); return r;
}
__device__ __forceinline__ float rmax2(float a, float b) {
  float r; asm("v_max_f32 %0, %1, %2\n\ts_nop 1" : "=v"(r) : "v"(a), "v"(b)); return r;
}
__device__ __forceinline__ float xmax8(float v) {
  int o = __builtin_amdgcn_update_dpp(0, __float_as_int(v), 0x128, 0xf, 0xf, false);
  return rmax2(v, __int_as_float(o));
}
__device__ __forceinline__ float xadd8(float v) {
  int o = __builtin_amdgcn_update_dpp(0, __float_as_int(v), 0x128, 0xf, 0xf, false);
  return v + __int_as_float(o);
}
__device__ __forceinline__ float xmax16(float v) {
  auto r = __builtin_amdgcn_permlane16_swap(__float_as_uint(v), __float_as_uint(v), false, false);
  return rmax2(__uint_as_float(r[0]), __uint_as_float(r[1]));
}
__device__ __forceinline__ float xadd16(float v) {
  auto r = __builtin_amdgcn_permlane16_swap(__float_as_uint(v), __float_as_uint(v), false, false);
  return __uint_as_float(r[0]) + __uint_as_float(r[1]);
}
__device__ __forceinline__ float xmax32(float v) {
  auto r = __builtin_amdgcn_permlane32_swap(__float_as_uint(v), __float_as_uint(v), false, false);
  return rmax2(__uint_as_float(r[0]), __uint_as_float(r[1]));
}
__device__ __forceinline__ float xadd32(float v) {
  auto r = __builtin_amdgcn_permlane32_swap(__float_as_uint(v), __float_as_uint(v), false, false);
  return __uint_as_float(r[0]) + __uint_as_float(r[1]);
}
__device__ __forceinline__ unsigned umax3(unsigned a, unsigned b, unsigned c) { return max(max(a, b), c); }
__device__ __forceinline__ float blo(unsigned p) { return __uint_as_float(p << 16); }
__device__ __forceinline__ float bhi(unsigned p) { return __uint_as_float(p & 0xFFFF0000u); }

__device__ __forceinline__ void glds16_s(const void* sbase, unsigned voff, unsigned lds_dst) {
  unsigned keep;
  asm volatile("s_mov_b32 %0, m0\n\ts_mov_b32 m0, %3\n\ts_nop 0\n\tglobal_load_lds_dwordx4 %1, %2\n\ts_mov_b32 m0, %0"
               : "=&s"(keep) : "v"(voff), "s"(sbase), "s"(lds_dst) : "memory");
}
__device__ __forceinline__ void dma_run(const void* sbase, unsigned voff, unsigned gstep, unsigned lds, int n) {
#pragma unroll 1
  for (int q = 0; q < n; ++q) { glds16_s(sbase, voff, lds); voff += gstep; lds += 1024; }
}
__device__ __forceinline__ void dma_wo(const void* sbase, unsigned boff, unsigned lds, int lane) {
  dma_run(sbase, boff + (unsigned)((lane >> 4) * 1280 + (lane & 15) * 16), 4 * 1280, lds, 16);
  dma_run(sbase, boff + (unsigned)((lane >> 2) * 1280 + 256 + (lane & 3) * 16), 16 * 1280, lds + 16384, 4);
}
#define WO_BLK(base_, t2_) ((t2_) < 8 ? (base_) : (base_) + 16384)
#define WO_RS(t2_) ((t2_) < 8 ? 256 : 64)
#define WO_N0(t2_) ((t2_) < 8 ? 16 * (t2_) : 16 * ((t2_) - 8))
#define WAIT_VM(n) asm volatile("s_waitcnt vmcnt(%0)" ::"n"(n) : "memory")
__device__ __forceinline__ u32x2 tr4(const u8* tile, int rsb, int k0, int n0, int lane) {
  int i = lane & 15;
  const u8* p = tile + (k0 + (i >> 2)) * rsb + (n0 + 4 * (i & 3)) * 2;
  s16x4 v = __builtin_amdgcn_ds_read_tr16_b64_v4i16((LAS s16x4*)p);
  return __builtin_bit_cast(u32x2, v);
}
__device__ __forceinline__ bf16x16 frag_sparse(const u8* blk, int rsb, int n0, int lane) {
  int gb = lane >> 4;
  int kb = 16 * (gb >> 1) + 4 * (gb & 1);
  u32x2 t00 = tr4(blk, rsb, kb, n0, lane), t01 = tr4(blk, rsb, kb + 8, n0, lane);
  u32x2 t10 = tr4(blk, rsb, kb + 32, n0, lane), t11 = tr4(blk, rsb, kb + 40, n0, lane);
  u32x8 d = {t00.x, t01.x, t00.y, t01.y, t10.x, t11.x, t10.y, t11.y};
  return __builtin_bit_cast(bf16x16, d);
}
__device__ __forceinline__ bf16x8 frag_dense(const u8* blk, int rsb, int k0, int n0, int lane) {
  int g = lane >> 4;
  u32x2 a = tr4(blk, rsb, k0 + 8 * g, n0, lane), b = tr4(blk, rsb, k0 + 8 * g + 4, n0, lane);
  u32x4 d = {a.x, a.y, b.x, b.y};
  return __builtin_bit_cast(bf16x8, d);
}

template <int MODE> __device__ __forceinline__ int swz(int row) {
  return MODE == 0 ? 2 * (((row >> 1) & 1) | (((row >> 3) & 1) << 1)) : 2 * ((row & 3) | (((row >> 3) & 1) << 2));
}
#define GLDS_I(IMM, sbase, voff, lds_base) \
  asm volatile("s_add_u32 m0, %2, %3\n\ts_nop 0\n\tglobal_load_lds_dwordx4 %0, %1" \
               :: "v"(voff), "s"(sbase), "s"(lds_base), "n"(IMM) : "memory", "scc")
__device__ __forceinline__ u32x2 trr(unsigned addr) {
  s16x4 v = __builtin_amdgcn_ds_read_tr16_b64_v4i16((LAS s16x4*)(size_t)addr);
  return __builtin_bit_cast(u32x2, v);
}
__device__ __forceinline__ bf16x8 trfrag(unsigned a0, unsigned a1) {
  u32x2 a = trr(a0), b = trr(a1);
  u32x4 d = {a.x, a.y, b.x, b.y};
  return __builtin_bit_cast(bf16x8, d);
}
__device__ __forceinline__ float gelu_tanh(float x) {
  float u = 0.7978845608028654f * (x + 0.044715f * x * x * x);
  return x * __builtin_amdgcn_rcpf(1.0f + __builtin_amdgcn_exp2f(-2.8853900817779268f * u));
}

__device__ __forceinline__ float gate8(const u16* xrow, const u16* gw, int part) {
  float sacc = 0.f;
#pragma unroll
  for (int q = 0; q < 10; ++q) {
    u32x4 xv = *(const u32x4*)(xrow + part * 8 + q * 64), gv = *(const u32x4*)(gw + part * 8 + q * 64);
#pragma unroll
    for (int e = 0; e < 4; ++e) {
      sacc += rbf(blo(xv[e]) * blo(gv[e]));
      sacc += rbf(bhi(xv[e]) * bhi(gv[e]));
    }
  }
  sacc += __shfl_xor(sacc, 1); sacc += __shfl_xor(sacc, 2); sacc += __shfl_xor(sacc, 4);
  float z = rbf(sacc);
  return rbf(1.0f / (1.0f + __expf(-z)));
}

template <int SLP = 2>
__device__ __forceinline__ void wait_flag(__amdgpu_buffer_rsrc_t rs, int cw, unsigned v, unsigned* ctl, int lane) {
  for (unsigned spins = 0;; ++spins) {
    asm volatile("" ::: "memory");
    int f = (int)ld32(rs, cw * 4);
    if (__all(f >= (int)v)) break;
    if (spins > SPIN_CAP) { if (lane == 0) atomicAdd(ctl, 1u); break; }
    __builtin_amdgcn_s_sleep(SLP);
  }
  asm volatile("" ::: "memory");
}

#define ADAPT 7
#define AD_DEC 32
#define AD_INC 96
#define D1 3800
#define DV 4300
#define D2 4800
__device__ __forceinline__ void wait_until(unsigned long long t) {
  while (__builtin_amdgcn_s_memtime() < t) __builtin_amdgcn_s_sleep(1);
}
#define START_DLY 16000
#define OUT_SLP 16
#define SCHED() __builtin_amdgcn_sched_barrier(0)
#define PROJ_GEMM(ACC, WF) do { \
    bf16x8 A0_[4], A1_[4]; \
    _Pragma("unroll") for (int ks = 0; ks < 3; ++ks) { \
      A0_[ks] = *(const bf16x8*)(xs + r16 * XS + 32 * ks + 8 * g); A1_[ks] = *(const bf16x8*)(xs + (16 + r16) * XS + 32 * ks + 8 * g); } \
    _Pragma("unroll") for (int ks = 0; ks < 20; ++ks) { \
      if (ks + 3 < 20) { A0_[(ks + 3) & 3] = *(const bf16x8*)(xs + r16 * XS + 32 * (ks + 3) + 8 * g); \
                         A1_[(ks + 3) & 3] = *(const bf16x8*)(xs + (16 + r16) * XS + 32 * (ks + 3) + 8 * g); } \
      SCHED(); \
      _Pragma("unroll") for (int tn = 0; tn < 2; ++tn) { \
        ACC[0][tn] = mf16(A0_[ks & 3], WF[tn][ks], ks == 0 ? Z4 : ACC[0][tn]); \
        ACC[1][tn] = mf16(A1_[ks & 3], WF[tn][ks], ks == 0 ? Z4 : ACC[1][tn]); } \
      SCHED(); \
    } } while (0)

#define LDX(t_) do { _Pragma("unroll") for (int q = 0; q < 10; ++q) { int pc = q * 256 + tid; int row = pc / 80, cc = pc % 80; \
      xp[q] = *(const u32x4*)(p.x + (size_t)((t_) * 32 + row) * 640 + cc * 8); } } while (0)
#define STX() do { _Pragma("unroll") for (int q = 0; q < 10; ++q) { int pc = q * 256 + tid; int row = pc / 80, cc = pc % 80; \
      *(u32x4*)(xs + row * XS + cc * 8) = xp[q]; } } while (0)
constexpr int XS = 648;
constexpr int KS = 136;

struct Params {
  const u16* x; const u16* mem0; const u16* Wq; const u16* Wk; const u16* Wv; const u16* Wo;
  const u16* Wg; const u16* Wu; const u16* Wd; const u16* gw; u8* ws; u16* out;
};
constexpr int NCH = 10;
constexpr int NTASK = NSEG * NCH;

__global__ void __launch_bounds__(256, 1) k_main(Params p) {
  extern __shared__ __attribute__((aligned(16))) u8 smem[];
  u8* ws = p.ws;
  const int tid = threadIdx.x, w = __builtin_amdgcn_readfirstlane(tid >> 6), lane = tid & 63;
  const int r16 = lane & 15, g = lane >> 4, par = r16 >> 3, r = r16 & 7;
  const int idx = par ? 0xEEEE : 0x4444;
#define XMAP 2
  int b;
  {
    const int bx = blockIdx.x, vi = (bx >> 3) * 4 + (bx & 3);
    if (XMAP == 0) b = bx;
    else if (XMAP == 1) {
      if ((bx & 4) == 0) b = vi < 104 ? vi : 232 + (vi - 104);
      else b = 104 + vi;
    } else if (XMAP == 2) {
      if ((bx & 4) == 0) b = vi < 84 ? vi : vi < 108 ? 232 + (vi - 84) : 104 + (vi - 108);
      else b = vi < 20 ? 84 + vi : 124 + (vi - 20);
    } else {
      if ((bx & 4) == 0) b = vi < 32 ? vi : vi < 72 ? 64 + (vi - 32) : vi < 96 ? 232 + (vi - 72) : 104 + (vi - 96);
      else b = vi < 32 ? 32 + vi : 136 + (vi - 32);
    }
  }
  const __amdgpu_buffer_rsrc_t rs = __builtin_amdgcn_make_buffer_rsrc((void*)ws, 0, (int)OFF_END, RSRC_W3);
  unsigned* ctl = (unsigned*)(ws + OFF_CTL);
  const f32x4 Z4 = {0.f, 0.f, 0.f, 0.f};
  unsigned long long tanchor = 0;
  int dly = b < 32 ? D1 : b < 64 ? DV : D2;
#define AD_UPDATE() do { if (s > 0) { if (spins == 0) dly -= AD_DEC; else dly += AD_INC; if (dly < 0) dly = 0; } } while (0)
  const u32x4 PZ = {0xFFFFFFFFu, 0xFFFFFFFFu, 0xFFFFFFFFu, 0xFFFFFFFFu};
  u8* wl = smem + w * 40960;
  const unsigned wla = __builtin_amdgcn_readfirstlane((unsigned)(size_t)(LAS u8*)smem) + w * 40960;
  const unsigned vo64 = (unsigned)((lane >> 2) * 8192 + (lane & 3) * 16);

  if (b < 32) {
    const int h = b;
    u16* sq = (u16*)smem;
    unsigned* spl = (unsigned*)(smem + 4608);
    u32x4* smem_a = (u32x4*)(smem + 8192);
    bf16x16 W[4][10];
    {
      const u8* src = (const u8*)((w < 2 ? p.Wq : p.Wk) + h * 128 + 64 * (w & 1));
      const unsigned vo128 = (unsigned)((lane >> 3) * 8192 + (lane & 7) * 16);
      dma_run(src, vo128, 65536, wla, 40);
#pragma unroll
      for (int i = 0; i < 10; ++i) {
        WAIT_VM(8 * (i < 5 ? 4 : 9 - i));
#pragma unroll
        for (int tn = 0; tn < 4; ++tn) W[tn][i] = frag_sparse(wl + (i % 5) * 8192, 128, 16 * tn, lane);
        if (i < 5) {
          WAIT_LGKM0();
          dma_run(src, vo128 + (unsigned)(40 + 8 * i) * 65536u, 65536, wla + i * 8192, 8);
        }
      }
      WAIT_LGKM0();
    }
#pragma unroll
    for (int tn = 0; tn < 4; ++tn)
#pragma unroll
      for (int i = 0; i < (tn < 2 ? 8 : 7); ++i) PIN_A(W[tn][i]);
    LDS_BARRIER();
    const int moff = (int)OFF_MEM + (2 * g + par) * 128 + r * 16;
    for (int s = 0; s < NSEG; ++s) {
      u32x4 kxf[2][4];
      if (w == 0 && s < NSEG - 1) {
        wait_flag(rs, CW_KXF + 16 * h, (unsigned)s, ctl, lane);
        const int kxo = (int)OFF_KX + (s * 32 + h) * 8192 + r16 * 256 + 16 * g;
#pragma unroll
        for (int t2 = 0; t2 < 2; ++t2)
#pragma unroll
          for (int kk = 0; kk < 4; ++kk) kxf[t2][kk] = ld128(rs, kxo + t2 * 4096 + 64 * kk);
      }
      f32x4 acc[4];
      unsigned spins = 0;
      if ((ADAPT & 1) && s > 0) wait_until(tanchor + dly);
      STAMP(0);
      for (;;) {
        asm volatile("" ::: "memory");
        u32x4 a0 = ld128(rs, moff + s * STEPB + w * 1024);
        u32x4 a1 = ld128(rs, moff + s * STEPB + (w + 4) * 1024);
        u32x4 a2 = ld128(rs, w < 2 ? moff + s * STEPB + (w + 8) * 1024 : OOB);
        asm volatile("" ::: "memory");
        smem_a[w * 64 + lane] = a0; smem_a[(w + 4) * 64 + lane] = a1; smem_a[(w + 8) * 64 + lane] = a2;
        unsigned mxv = umax3(umax3(a0.x, a0.z, a1.x), a1.z, umax3(a2.x, a2.z, 0u));
        if (__all(mxv != 0xFFFFFFFFu) || ++spins > SPIN_CAP) break;
      }
      LDS_BARRIER();
#pragma unroll
      for (int tn = 0; tn < 4; ++tn) { acc[tn] = Z4; PIN_V(acc[tn]); }
      {
        u32x4 af[10];
#pragma unroll
        for (int i = 0; i < 10; ++i) af[i] = smem_a[i * 64 + lane];
        SCHED();
#pragma unroll
        for (int i = 0; i < 10; ++i)
#pragma unroll
          for (int tn = 0; tn < 4; ++tn) acc[tn] = smf(__builtin_bit_cast(bf16x8, af[i]), W[tn][i], acc[tn], idx);
      }
      STAMP(1);
      u16* dst = sq + (w >> 1) * (8 * KS) + (4 * (g & 1)) * KS + 64 * (w & 1) + 16 * (lane >> 5) + r16;
#pragma unroll
      for (int pp = 0; pp < 2; ++pp) {
        float v0 = comb(acc[2 * pp][0], acc[2 * pp + 1][0]), v1 = comb(acc[2 * pp][1], acc[2 * pp + 1][1]);
        float v2 = comb(acc[2 * pp][2], acc[2 * pp + 1][2]), v3 = comb(acc[2 * pp][3], acc[2 * pp + 1][3]);
        unsigned p01 = pk2(v0, v1), p23 = pk2(v2, v3);
        dst[32 * pp] = (u16)p01; dst[32 * pp + KS] = (u16)(p01 >> 16);
        dst[32 * pp + 2 * KS] = (u16)p23; dst[32 * pp + 3 * KS] = (u16)(p23 >> 16);
      }
      LDS_BARRIER();
      STAMP(2);
      if (w == 0 && s < NSEG - 1) {
        f32x4 sc[3] = {Z4, Z4, Z4};
        bf16x8 qf[4], kf[4];
#pragma unroll
        for (int kk = 0; kk < 4; ++kk) qf[kk] = *(const bf16x8*)(sq + r * KS + 32 * kk + 8 * g);
#pragma unroll
        for (int kk = 0; kk < 4; ++kk) kf[kk] = *(const bf16x8*)(sq + 8 * KS + r * KS + 32 * kk + 8 * g);
        SCHED();
#pragma unroll
        for (int kk = 0; kk < 4; ++kk) {
          sc[1] = mf16(__builtin_bit_cast(bf16x8, kxf[0][kk]), qf[kk], sc[1]);
          sc[2] = mf16(__builtin_bit_cast(bf16x8, kxf[1][kk]), qf[kk], sc[2]);
          sc[0] = mf16(kf[kk], qf[kk], sc[0]);
        }
        const float NEG = -3.0e38f;
        float v[6];
        v[0] = par ? sc[1][2] : (g < 2 ? sc[0][0] : NEG);
        v[1] = par ? sc[1][3] : (g < 2 ? sc[0][1] : NEG);
        v[2] = par ? sc[2][0] : (g < 2 ? sc[0][2] : NEG);
        v[3] = par ? sc[2][1] : (g < 2 ? sc[0][3] : NEG);
        v[4] = par ? sc[2][2] : sc[1][0];
        v[5] = par ? sc[2][3] : sc[1][1];
        float mx = rmax3(rmax3(v[0], v[1], v[2]), rmax3(v[3], v[4], v[5]), v[0]);
        mx = xmax32(xmax16(xmax8(mx)));
        const float scale = 0.08838834764831845f;
        const float L2E = 1.4426950408889634f;
        float mr = rbf(rbf(mx) * scale) * L2E;
        float e[6];
#pragma unroll
        for (int q = 0; q < 3; ++q) {
          unsigned p1 = pk2(v[2 * q], v[2 * q + 1]);
          unsigned p2 = pk2(blo(p1) * scale, bhi(p1) * scale);
          e[2 * q] = __builtin_amdgcn_exp2f(fmaf(blo(p2), L2E, -mr));
          e[2 * q + 1] = __builtin_amdgcn_exp2f(fmaf(bhi(p2), L2E, -mr));
        }
        float sum = ((e[0] + e[1]) + (e[2] + e[3])) + (e[4] + e[5]);
        sum = xadd32(xadd16(xadd8(sum)));
        float inv = __builtin_amdgcn_rcpf(sum);
        unsigned o0 = pk2(e[0] * inv, e[1] * inv), o1 = pk2(e[2] * inv, e[3] * inv), o2 = pk2(e[4] * inv, e[5] * inv);
        unsigned* pl = spl + r * 4;
        int dA = 32 + (g >> 1) * 32 + 2 * (g & 1), dB = dA + 64;
        int d0 = par ? dA + 1 : 2 * g, d1 = par ? dB : 2 * g + 1, d2 = par ? dB + 1 : dA;
        if (par || g < 2) { pl[d0] = o0; pl[d1] = o1; }
        pl[d2] = o2;
        WAIT_LGKM0();
        if (lane < 40) {
          u32x4 o = *(const u32x4*)(spl + lane * 4);
          o.x = nosent(o.x); o.y = nosent(o.y); o.z = nosent(o.z); o.w = nosent(o.w);
          st128(o, rs, (int)OFF_P + s * STEPB + h * 640 + lane * 16);
          st128(PZ, rs, (int)OFF_P + (s + 2) * STEPB + h * 640 + lane * 16);
        }
      }
      if (w == 1) {
#pragma unroll
        for (int q = 0; q < 2; ++q) {
          int pc = q * 64 + lane, row = pc >> 4, ch = pc & 15;
          u32x4 o = *(const u32x4*)(sq + 8 * KS + row * KS + ch * 8);
          o.x = nosent(o.x); o.y = nosent(o.y); o.z = nosent(o.z); o.w = nosent(o.w);
          st128(o, rs, (int)OFF_KM + s * STEPB + h * 2048 + pc * 16);
          st128(PZ, rs, (int)OFF_KM + (s + 2) * STEPB + h * 2048 + pc * 16);
        }
      }
      STAMP(3);
      LDS_BARRIER();
      AD_UPDATE();
      tanchor = __builtin_amdgcn_s_memtime();
      STAMP_FLUSH();
    }
  } else if (b < 64) {
    const int h = b - 32;
    u16* sq = (u16*)smem;
    u32x4* smem_a = (u32x4*)(smem + 8192);
    bf16x16 Wv[2][10], Wo[10][2];
    {
      const u8* src = (const u8*)(p.Wv + h * 128 + 32 * w);
      const u8* so = (const u8*)(p.Wo + (size_t)h * 128 * 640 + 160 * w);
      dma_run(src, vo64, 131072, wla, 40);
      WAIT_VM(20);
#pragma unroll
      for (int i = 0; i < 5; ++i)
#pragma unroll
        for (int tn = 0; tn < 2; ++tn) Wv[tn][i] = frag_sparse(wl + i * 4096, 64, 16 * tn, lane);
      WAIT_LGKM0();
      dma_wo(so, 0, wla, lane);
      WAIT_VM(20);
#pragma unroll
      for (int i = 5; i < 10; ++i)
#pragma unroll
        for (int tn = 0; tn < 2; ++tn) Wv[tn][i] = frag_sparse(wl + i * 4096, 64, 16 * tn, lane);
      WAIT_LGKM0();
      dma_wo(so, 64 * 1280, wla + 20480, lane);
      WAIT_VM(20);
#pragma unroll
      for (int t2 = 0; t2 < 10; ++t2) Wo[t2][0] = frag_sparse(WO_BLK(wl, t2), WO_RS(t2), WO_N0(t2), lane);
      WAIT_VM(0);
#pragma unroll
      for (int t2 = 0; t2 < 10; ++t2) Wo[t2][1] = frag_sparse(WO_BLK(wl + 20480, t2), WO_RS(t2), WO_N0(t2), lane);
      WAIT_LGKM0();
    }
#pragma unroll
    for (int tn = 0; tn < 2; ++tn)
#pragma unroll
      for (int i = 0; i < 6; ++i) PIN_A(Wv[tn][i]);
#pragma unroll
    for (int t2 = 0; t2 < 10; ++t2)
#pragma unroll
      for (int i = 0; i < 2; ++i) PIN_A(Wo[t2][i]);
    LDS_BARRIER();
    const int moff = (int)OFF_MEM + (2 * g + par) * 128 + r * 16;
    const int ucol = 160 * w + 16 * (lane >> 5) + r16;
    const int uoff = (int)OFF_UM + h * 128 + (ucol >> 3) * 4096 + (ucol & 7) * 16 + 8 * (g & 1);
    for (int s = 0; s < NSEG; ++s) {
      f32x4 acc[2];
      unsigned spins = 0;
      if ((ADAPT & 2) && s > 0) wait_until(tanchor + dly);
      STAMP(0);
      for (;;) {
        asm volatile("" ::: "memory");
        u32x4 a0 = ld128(rs, moff + s * STEPB + w * 1024);
        u32x4 a1 = ld128(rs, moff + s * STEPB + (w + 4) * 1024);
        u32x4 a2 = ld128(rs, w < 2 ? moff + s * STEPB + (w + 8) * 1024 : OOB);
        asm volatile("" ::: "memory");
        smem_a[w * 64 + lane] = a0; smem_a[(w + 4) * 64 + lane] = a1; smem_a[(w + 8) * 64 + lane] = a2;
        unsigned mxv = umax3(umax3(a0.x, a0.z, a1.x), a1.z, umax3(a2.x, a2.z, 0u));
        if (__all(mxv != 0xFFFFFFFFu) || ++spins > SPIN_CAP) break;
      }
      LDS_BARRIER();
      acc[0] = Z4; acc[1] = Z4; PIN_V(acc[0]); PIN_V(acc[1]);
      {
        u32x4 af[10];
#pragma unroll
        for (int i = 0; i < 10; ++i) af[i] = smem_a[i * 64 + lane];
        SCHED();
#pragma unroll
        for (int i = 0; i < 10; ++i) {
          acc[0] = smf(__builtin_bit_cast(bf16x8, af[i]), Wv[0][i], acc[0], idx);
          acc[1] = smf(__builtin_bit_cast(bf16x8, af[i]), Wv[1][i], acc[1], idx);
        }
      }
      STAMP(1);
      u16* vs = sq + (s & 1) * (8 * KS);
      {
        u16* dst = vs + (4 * (g & 1)) * KS + 32 * w + 16 * (lane >> 5) + r16;
        float v0 = comb(acc[0][0], acc[1][0]), v1 = comb(acc[0][1], acc[1][1]);
        float v2 = comb(acc[0][2], acc[1][2]), v3 = comb(acc[0][3], acc[1][3]);
        unsigned p01 = pk2(v0, v1), p23 = pk2(v2, v3);
        dst[0] = (u16)p01; dst[KS] = (u16)(p01 >> 16); dst[2 * KS] = (u16)p23; dst[3 * KS] = (u16)(p23 >> 16);
      }
      f32x4 c[10];
#pragma unroll
      for (int t2 = 0; t2 < 10; ++t2) { c[t2] = Z4; PIN_V(c[t2]); }
      LDS_BARRIER();
      STAMP(2);
      bf16x8 a0 = *(const bf16x8*)(vs + r * KS + 8 * (2 * g + par));
      bf16x8 a1 = *(const bf16x8*)(vs + r * KS + 64 + 8 * (2 * g + par));
#pragma unroll
      for (int t2 = 0; t2 < 10; ++t2) c[t2] = smf(a0, Wo[t2][0], c[t2], idx);
#pragma unroll
      for (int t2 = 0; t2 < 10; ++t2) c[t2] = smf(a1, Wo[t2][1], c[t2], idx);
#pragma unroll
      for (int pp = 0; pp < 5; ++pp) {
        float v0 = comb(c[2 * pp][0], c[2 * pp + 1][0]), v1 = comb(c[2 * pp][1], c[2 * pp + 1][1]);
        float v2 = comb(c[2 * pp][2], c[2 * pp + 1][2]), v3 = comb(c[2 * pp][3], c[2 * pp + 1][3]);
        u32x2 o = {nosent(pk2(v0, v1)), nosent(pk2(v2, v3))};
        st64(o, rs, uoff + s * STEPB + pp * 4 * 4096);
      }
#pragma unroll
      for (int pp = 0; pp < 5; ++pp) st64(u32x2{0xFFFFFFFFu, 0xFFFFFFFFu}, rs, uoff + (s + 2) * STEPB + pp * 4 * 4096);
      STAMP(3);
      AD_UPDATE();
      tanchor = __builtin_amdgcn_s_memtime();
      STAMP_FLUSH();
    }
  } else if (b < 104) {
    const int c = b - 64;
    float* sred = (float*)smem;
    if (tid < 32) {
      int sh = tid >> 4, row = (tid >> 1) & 7, cp = tid & 1;
      u32x2 v = *(const u32x2*)(p.mem0 + row * 640 + c * 16 + sh * 8 + cp * 4);
      v.x = nosent(v.x); v.y = nosent(v.y);
      st64(v, rs, (int)OFF_MEM + c * 256 + tid * 8);
    }
    const int sl = 2 * c + par;
    for (int s = 0; s < NSEG - 1; ++s) {
      wait_flag(rs, CW_UXF + 16 * (8 * w + (lane & 7)), (unsigned)s, ctl, lane);
      const int uxo = (int)OFF_UX + ((s * 80 + sl) * 32 + 8 * w) * 512 + r * 64 + 16 * g;
      u32x4 bx[8];
#pragma unroll
      for (int i = 0; i < 8; ++i) bx[i] = ld128(rs, uxo + i * 512);
      const int pb = (r16 < 8) ? (int)OFF_P + s * STEPB + (8 * w) * 640 + r * 16 : OOB;
      const int ub = (int)OFF_UM + s * STEPB + sl * 4096 + (8 * w) * 128 + r * 16;
      f32x4 acc0, acc1;
      unsigned spins = 0;
      if ((ADAPT & 4) && s > 0) wait_until(tanchor + dly);
      STAMP(0);
      for (;;) {
        asm volatile("" ::: "memory");
        u32x4 pa[10], um[2];
#pragma unroll
        for (int i = 0; i < 2; ++i) um[i] = ld128(rs, ub + (4 * i + g) * 128);
#pragma unroll
        for (int i = 0; i < 2; ++i) pa[8 + i] = ld128(rs, pb + (4 * i + g) * 640);
#pragma unroll
        for (int i = 0; i < 8; ++i) pa[i] = ld128(rs, pb + i * 640 + 128 + 128 * g);
        asm volatile("" ::: "memory");
        unsigned mxv = umax3(um[0].x, um[0].z, 0u);
        mxv = umax3(mxv, um[1].x, um[1].z);
#pragma unroll
        for (int i = 0; i < 10; ++i) mxv = umax3(mxv, pa[i].x, pa[i].z);
        if (__all(mxv != 0xFFFFFFFFu) || spins > SPIN_CAP) {
          acc0 = mf16(__builtin_bit_cast(bf16x8, pa[8]), __builtin_bit_cast(bf16x8, um[0]), Z4);
          acc1 = mf16(__builtin_bit_cast(bf16x8, pa[9]), __builtin_bit_cast(bf16x8, um[1]), Z4);
#pragma unroll
          for (int i = 0; i < 8; i += 2) {
            acc0 = mf16(__builtin_bit_cast(bf16x8, pa[i]), __builtin_bit_cast(bf16x8, bx[i]), acc0);
            acc1 = mf16(__builtin_bit_cast(bf16x8, pa[i + 1]), __builtin_bit_cast(bf16x8, bx[i + 1]), acc1);
          }
          break;
        }
        ++spins;
      }
      STAMP(1);
      if (g < 2) {
#pragma unroll
        for (int j = 0; j < 4; ++j) sred[w * 128 + par * 64 + (4 * g + j) * 8 + r] = acc0[j] + acc1[j];
      }
      LDS_BARRIER();
      STAMP(2);
      if (w == 0) {
        const f32x4* sr = (const f32x4*)sred;
        if (lane < 32) {
          f32x4 a = sr[lane], b2 = sr[32 + lane], c2 = sr[64 + lane], d2 = sr[96 + lane];
          f32x4 t = (a + b2) + (c2 + d2);
          u32x2 o = {nosent(pk2(t[0], t[1])), nosent(pk2(t[2], t[3]))};
          st64(o, rs, (int)OFF_MEM + (s + 1) * STEPB + c * 256 + lane * 8);
          st64(u32x2{0xFFFFFFFFu, 0xFFFFFFFFu}, rs, (int)OFF_MEM + (s + 3) * STEPB + c * 256 + lane * 8);
        }
      }
      STAMP(3);
      LDS_BARRIER();
      AD_UPDATE();
      tanchor = __builtin_amdgcn_s_memtime();
      STAMP_FLUSH();
    }
  } else if (b < 136) {
    const int h = b - 104;
    bf16x8 Wk[2][20];
    u32x4 xp[10];
    LDX(0);
    asm volatile("" ::: "memory");
    {
      const u8* sk = (const u8*)(p.Wk + h * 128 + 32 * w);
      dma_run(sk, vo64, 131072, wla, 40);
      WAIT_VM0();
#pragma unroll
      for (int ks = 0; ks < 20; ++ks)
#pragma unroll
        for (int tn = 0; tn < 2; ++tn) Wk[tn][ks] = frag_dense(wl, 64, 32 * ks, 16 * tn, lane);
      WAIT_LGKM0();
    }
#pragma unroll
    for (int tn = 0; tn < 2; ++tn)
#pragma unroll
      for (int ks = 0; ks < 20; ++ks) PIN_A(Wk[tn][ks]);
    LDS_BARRIER();
    u16* xs = (u16*)smem;
    u16* kb = (u16*)(smem + 41472);
    for (int t = 0; t < NSEG; ++t) {
      STAMP(0);
      STX();
      LDS_BARRIER();
      STAMP(1);
      LDX(t + 1 < NSEG ? t + 1 : 0);
      f32x4 acc[2][2];
      PROJ_GEMM(acc, Wk);
#pragma unroll
      for (int m = 0; m < 2; ++m)
#pragma unroll
        for (int tn = 0; tn < 2; ++tn) {
          u16* dst = kb + (8 * g + 4 * m) * KS + 32 * w + 16 * tn + r16;
          unsigned p01 = pk2(acc[m][tn][0], acc[m][tn][1]), p23 = pk2(acc[m][tn][2], acc[m][tn][3]);
          dst[0] = (u16)p01; dst[KS] = (u16)(p01 >> 16); dst[2 * KS] = (u16)p23; dst[3 * KS] = (u16)(p23 >> 16);
        }
      WAIT_VM(10);
      LDS_BARRIER();
      if (tid == 0 && t > 1) st32((unsigned)(t - 1), rs, (CW_KXF + 16 * h) * 4);
      STAMP(2);
#pragma unroll
      for (int q = 0; q < 2; ++q) {
        int pc = q * 256 + tid, row = pc >> 4, ch = pc & 15;
        u32x4 o = *(const u32x4*)(kb + row * KS + ch * 8);
        st128(o, rs, (int)OFF_KX + (t * 32 + h) * 8192 + pc * 16);
      }
      if (t == 0 || t == NSEG - 1) {
        WAIT_VM0();
        LDS_BARRIER();
        if (tid == 0) st32((unsigned)t, rs, (CW_KXF + 16 * h) * 4);
      }
      STAMP(3);
    }
  } else if (b < 168) {
    const int h = b - 136;
    wait_until(__builtin_amdgcn_s_memtime() + START_DLY);
    bf16x8 Wq[2][20];
    {
      const u8* sqp = (const u8*)(p.Wq + h * 128 + 32 * w);
      dma_run(sqp, vo64, 131072, wla, 40);
      WAIT_VM0();
#pragma unroll
      for (int ks = 0; ks < 20; ++ks)
#pragma unroll
        for (int tn = 0; tn < 2; ++tn) Wq[tn][ks] = frag_dense(wl, 64, 32 * ks, 16 * tn, lane);
      WAIT_LGKM0();
    }
#pragma unroll
    for (int tn = 0; tn < 2; ++tn)
#pragma unroll
      for (int ks = 0; ks < 20; ++ks) PIN_A(Wq[tn][ks]);
    LDS_BARRIER();
    u16* xs = (u16*)smem;
    u16* kb = (u16*)(smem + 41472);
    u16* kmb = (u16*)(smem + 41472 + 8704);
    u8* pxb = smem + 41472 + 8704 + 2176;
    u32x4 xp[10];
    LDX(0);
    const float scale = 0.08838834764831845f;
    const float L2E = 1.4426950408889634f;
    int kflag = -1;
    u16* kxb = (u16*)(smem + 57344);
    {
      wait_flag(rs, CW_KXF + 16 * h, 0u, ctl, lane);
      u32x4 a = ld128(rs, (int)OFF_KX + h * 8192 + tid * 16), c2 = ld128(rs, (int)OFF_KX + h * 8192 + 4096 + tid * 16);
      *(u32x4*)(kxb + (tid >> 4) * KS + (tid & 15) * 8) = a;
      *(u32x4*)(kxb + (16 + (tid >> 4)) * KS + (tid & 15) * 8) = c2;
    }
    for (int u = 0; u < NSEG; ++u) {
      STAMP(0);
      STX();
      LDS_BARRIER();
      STAMP(1);
      const int un = u + 1 < NSEG ? u + 1 : u;
      if (kflag < un) {
        wait_flag(rs, CW_KXF + 16 * h, (unsigned)un, ctl, lane);
        kflag = __builtin_amdgcn_readfirstlane((int)ld32(rs, (CW_KXF + 16 * h) * 4));
      }
      u32x4 kn0 = ld128(rs, (int)OFF_KX + (un * 32 + h) * 8192 + tid * 16);
      u32x4 kn1 = ld128(rs, (int)OFF_KX + (un * 32 + h) * 8192 + 4096 + tid * 16);
      u32x4 k0 = ld128(rs, w == 3 ? (int)OFF_KM + u * STEPB + h * 2048 + lane * 16 : OOB);
      u32x4 k1 = ld128(rs, w == 3 ? (int)OFF_KM + u * STEPB + h * 2048 + 1024 + lane * 16 : OOB);
      LDX(u + 1 < NSEG ? u + 1 : 0);
      f32x4 acc[2][2];
      PROJ_GEMM(acc, Wq);
#pragma unroll
      for (int m = 0; m < 2; ++m)
#pragma unroll
        for (int tn = 0; tn < 2; ++tn) {
          u16* dst = kb + (16 * m + 4 * g) * KS + 32 * w + 16 * tn + r16;
          unsigned p01 = pk2(acc[m][tn][0], acc[m][tn][1]), p23 = pk2(acc[m][tn][2], acc[m][tn][3]);
          dst[0] = (u16)p01; dst[KS] = (u16)(p01 >> 16); dst[2 * KS] = (u16)p23; dst[3 * KS] = (u16)(p23 >> 16);
        }
      WAIT_VM(14);
      LDS_BARRIER();
      if (tid == 0 && u > 0 && u < NSEG - 1) st32((unsigned)(u - 1), rs, (CW_PXF + 16 * h) * 4);
      if (w == 3) {
        unsigned spins = 0;
        for (;;) {
          unsigned mxv = umax3(umax3(k0.x, k0.z, k1.x), k1.z, 0u);
          if (__all(mxv != 0xFFFFFFFFu) || spins > SPIN_CAP) {
            *(u32x4*)(kmb + (lane >> 4) * KS + (lane & 15) * 8) = k0;
            *(u32x4*)(kmb + (4 + (lane >> 4)) * KS + (lane & 15) * 8) = k1;
            break;
          }
          if (spins) __builtin_amdgcn_s_sleep(6);
          ++spins;
          asm volatile("" ::: "memory");
          k0 = ld128(rs, (int)OFF_KM + u * STEPB + h * 2048 + lane * 16);
          k1 = ld128(rs, (int)OFF_KM + u * STEPB + h * 2048 + 1024 + lane * 16);
          asm volatile("" ::: "memory");
        }
      }
      LDS_BARRIER();
      STAMP(2);
      {
        f32x4 sc[3];
        bf16x8 qf[4], kf[4];
#pragma unroll
        for (int kk = 0; kk < 4; ++kk) qf[kk] = *(const bf16x8*)(kb + (8 * w + r) * KS + 32 * kk + 8 * g);
#pragma unroll
        for (int kk = 0; kk < 4; ++kk) kf[kk] = *(const bf16x8*)(kmb + r * KS + 32 * kk + 8 * g);
        bf16x8 kxf[2][4];
        const u16* kxc = kxb + (u & 1) * (32 * KS);
#pragma unroll
        for (int t2 = 0; t2 < 2; ++t2)
#pragma unroll
          for (int kk = 0; kk < 4; ++kk) kxf[t2][kk] = *(const bf16x8*)(kxc + (16 * t2 + r16) * KS + 32 * kk + 8 * g);
        SCHED();
#pragma unroll
        for (int kk = 0; kk < 4; ++kk) {
          sc[1] = mf16(kxf[0][kk], qf[kk], kk == 0 ? Z4 : sc[1]);
          sc[2] = mf16(kxf[1][kk], qf[kk], kk == 0 ? Z4 : sc[2]);
          sc[0] = mf16(kf[kk], qf[kk], kk == 0 ? Z4 : sc[0]);
        }
        const int tok = 8 * w + r;
        const int tkA = 16 * (g & 1) + 4 * (g >> 1), tkB = tkA + 8;
        float v[6];
        int tk[6];
        v[0] = par ? sc[1][2] : sc[0][0]; tk[0] = par ? tkA + 2 : -1;
        v[1] = par ? sc[1][3] : sc[0][1]; tk[1] = par ? tkA + 3 : -1;
        v[2] = par ? sc[2][0] : sc[0][2]; tk[2] = par ? tkB : -1;
        v[3] = par ? sc[2][1] : sc[0][3]; tk[3] = par ? tkB + 1 : -1;
        v[4] = par ? sc[2][2] : sc[1][0]; tk[4] = par ? tkB + 2 : tkA;
        v[5] = par ? sc[2][3] : sc[1][1]; tk[5] = par ? tkB + 3 : tkA + 1;
        const bool nomem = !par && g >= 2;
        float sv[6];
#pragma unroll
        for (int q = 0; q < 3; ++q) {
          unsigned p1 = pk2(v[2 * q], v[2 * q + 1]);
          unsigned p2 = pk2(blo(p1) * scale, bhi(p1) * scale);
          sv[2 * q] = blo(p2); sv[2 * q + 1] = bhi(p2);
        }
        const float MASKV = rbf(-1.0e9f);
#pragma unroll
        for (int q = 0; q < 6; ++q) {
          if (tk[q] > tok) sv[q] = MASKV;
          if (q < 4 && nomem) sv[q] = -3.0e38f;
        }
        float mx = fmaxf(fmaxf(fmaxf(sv[0], sv[1]), fmaxf(sv[2], sv[3])), fmaxf(sv[4], sv[5]));
        mx = xmax32(xmax16(xmax8(mx)));
        float e[6];
#pragma unroll
        for (int q = 0; q < 6; ++q) e[q] = __builtin_amdgcn_exp2f((sv[q] - mx) * L2E);
        float sum = ((e[0] + e[1]) + (e[2] + e[3])) + (e[4] + e[5]);
        sum = xadd32(xadd16(xadd8(sum)));
        float inv = __builtin_amdgcn_rcpf(sum);
        unsigned o0 = pk2(e[0] * inv, e[1] * inv), o1 = pk2(e[2] * inv, e[3] * inv), o2 = pk2(e[4] * inv, e[5] * inv);
        unsigned* pl = (unsigned*)pxb + tok * 4;
        int dA = 128 + (g >> 1) * 128 + 2 * (g & 1), dB = dA + 256;
        int d0 = par ? dA + 1 : 2 * g, d1 = par ? dB : 2 * g + 1, d2 = par ? dB + 1 : dA;
        if (!nomem) { pl[d0] = o0; pl[d1] = o1; }
        pl[d2] = o2;
      }
      {
        u16* kxn = kxb + ((u + 1) & 1) * (32 * KS);
        *(u32x4*)(kxn + (tid >> 4) * KS + (tid & 15) * 8) = kn0;
        *(u32x4*)(kxn + (16 + (tid >> 4)) * KS + (tid & 15) * 8) = kn1;
      }
      LDS_BARRIER();
      if (tid < 160) {
        u32x4 o = *(const u32x4*)(pxb + tid * 16);
        st128(o, rs, (int)OFF_PX + (u * 32 + h) * 2560 + tid * 16);
      }
      if (u >= NSEG - 2) {
        WAIT_VM0();
        LDS_BARRIER();
        if (tid == 0) st32((unsigned)u, rs, (CW_PXF + 16 * h) * 4);
      }
    }
  } else if (b < 200) {
    const int h = b - 168;
    bf16x8 Wv[2][20], Wo[10][4];
    u32x4 xp[10];
    LDX(0);
    asm volatile("" ::: "memory");
    {
      const u8* sv = (const u8*)(p.Wv + h * 128 + 32 * w);
      const u8* so = (const u8*)(p.Wo + (size_t)h * 128 * 640 + 160 * w);
      dma_run(sv, vo64, 131072, wla, 40);
      WAIT_VM(20);
#pragma unroll
      for (int ks = 0; ks < 10; ++ks)
#pragma unroll
        for (int tn = 0; tn < 2; ++tn) Wv[tn][ks] = frag_dense(wl, 64, 32 * ks, 16 * tn, lane);
      WAIT_LGKM0();
      dma_wo(so, 0, wla, lane);
      WAIT_VM(20);
#pragma unroll
      for (int ks = 10; ks < 20; ++ks)
#pragma unroll
        for (int tn = 0; tn < 2; ++tn) Wv[tn][ks] = frag_dense(wl, 64, 32 * ks, 16 * tn, lane);
      WAIT_LGKM0();
      dma_wo(so, 64 * 1280, wla + 20480, lane);
      WAIT_VM(20);
#pragma unroll
      for (int ks = 0; ks < 2; ++ks)
#pragma unroll
        for (int t2 = 0; t2 < 10; ++t2) Wo[t2][ks] = frag_dense(WO_BLK(wl, t2), WO_RS(t2), 32 * ks, WO_N0(t2), lane);
      WAIT_VM(0);
#pragma unroll
      for (int ks = 2; ks < 4; ++ks)
#pragma unroll
        for (int t2 = 0; t2 < 10; ++t2) Wo[t2][ks] = frag_dense(WO_BLK(wl + 20480, t2), WO_RS(t2), 32 * (ks - 2), WO_N0(t2), lane);
      WAIT_LGKM0();
    }
#pragma unroll
    for (int t2 = 0; t2 < 10; ++t2)
#pragma unroll
      for (int ks = 0; ks < 4; ++ks) PIN_A(Wo[t2][ks]);
#pragma unroll
    for (int tn = 0; tn < 2; ++tn)
#pragma unroll
      for (int ks = 0; ks < 8; ++ks) PIN_A(Wv[tn][ks]);
    LDS_BARRIER();
    u16* xs = (u16*)smem;
    u16* vb = (u16*)(smem + 41472);
    for (int t = 0; t < NSEG; ++t) {
      STX();
      LDS_BARRIER();
      LDX(t + 1 < NSEG ? t + 1 : 0);
      f32x4 acc[2][2];
      PROJ_GEMM(acc, Wv);
#pragma unroll
      for (int m = 0; m < 2; ++m)
#pragma unroll
        for (int tn = 0; tn < 2; ++tn) {
          u16* dst = vb + (16 * m + 4 * g) * KS + 32 * w + 16 * tn + r16;
          unsigned p01 = pk2(acc[m][tn][0], acc[m][tn][1]), p23 = pk2(acc[m][tn][2], acc[m][tn][3]);
          dst[0] = (u16)p01; dst[KS] = (u16)(p01 >> 16); dst[2 * KS] = (u16)p23; dst[3 * KS] = (u16)(p23 >> 16);
        }
      WAIT_VM(10);
      LDS_BARRIER();
      if (tid == 0 && t > 1) st32((unsigned)(t - 1), rs, (CW_UXF + 16 * h) * 4);
      bf16x8 va[2][4];
#pragma unroll
      for (int m = 0; m < 2; ++m)
#pragma unroll
        for (int ks = 0; ks < 4; ++ks) va[m][ks] = *(const bf16x8*)(vb + (16 * m + r16) * KS + 32 * ks + 8 * g);
#pragma unroll
      for (int t2 = 0; t2 < 10; ++t2) {
        f32x4 c0 = Z4, c1 = Z4;
#pragma unroll
        for (int ks = 0; ks < 4; ++ks) { c0 = mf16(va[0][ks], Wo[t2][ks], c0); c1 = mf16(va[1][ks], Wo[t2][ks], c1); }
        int col = 160 * w + 16 * t2 + r16;
        u32x4 o = {pk2(c0[0], c0[1]), pk2(c0[2], c0[3]), pk2(c1[0], c1[1]), pk2(c1[2], c1[3])};
        st128(o, rs, (int)OFF_UX + ((t * 80 + (col >> 3)) * 32 + h) * 512 + (col & 7) * 64 + 16 * g);
      }
      if (t == 0 || t == NSEG - 1) {
        WAIT_VM0();
        LDS_BARRIER();
        if (tid == 0) st32((unsigned)t, rs, (CW_UXF + 16 * h) * 4);
      }
    }
  } else if (b < 232) {
    const int f = b - 200;
    wait_until(__builtin_amdgcn_s_memtime() + START_DLY);
    u8* slab0 = smem;
    const unsigned ldsb = __builtin_amdgcn_readfirstlane((unsigned)(size_t)(LAS u8*)smem);
    u8* wdb = smem + 65536;
    u16* hb = (u16*)(smem + 65536 + 81920);
    bf16x8 A[20];
#pragma unroll
    for (int ks = 0; ks < 20; ++ks)
      A[ks] = *(const bf16x8*)(p.x + (size_t)(64 * f + 16 * w + r16) * 640 + 32 * ks + 8 * g);
#pragma unroll
    for (int ks = 0; ks < 16; ++ks) PIN_A(A[ks]);
    f32x4 acc2[4][10];
#pragma unroll
    for (int m = 0; m < 4; ++m)
#pragma unroll
      for (int n = 0; n < 10; ++n) acc2[m][n] = Z4;
    const int i16 = lane & 15;
    unsigned so[2], tb[4], wo2[2], tw;
#pragma unroll
    for (int q = 0; q < 2; ++q) {
      int row = 8 * (2 * w + q) + (lane >> 3);
      so[q] = (unsigned)(row * (DF * 2) + (((lane & 7) ^ swz<0>(row)) << 4));
      wo2[q] = (unsigned)((lane >> 4) * 1280 + (((lane & 15) ^ (2 * ((lane >> 4) | (q << 2)))) << 4));
    }
#pragma unroll
    for (int q = 0; q < 4; ++q) {
      int s0 = swz<0>(8 * g + (i16 >> 2));
      tb[q] = ldsb + (unsigned)((8 * g + (i16 >> 2)) * 128 + (((2 * q + ((i16 & 3) >> 1)) ^ s0) << 4) + (i16 & 1) * 8);
    }
    const int s1 = 2 * ((i16 >> 2) | ((g & 1) << 2));
    tw = ldsb + 65536 + (unsigned)((8 * g + (i16 >> 2)) * 256 + ((i16 & 3) >> 1) * 16 + (i16 & 1) * 8);
#define FFN_SLAB(c_, si_, slot_) do { \
      const unsigned uo_ = (unsigned)((64 * (si_)) * (DF * 2) + 128 * (c_)); \
      const unsigned v0_ = so[0] + uo_, v1_ = so[1] + uo_; \
      const unsigned bb_ = lw2 + (slot_) * 16384; \
      GLDS_I(0, p.Wg, v0_, bb_); GLDS_I(1024, p.Wg, v1_, bb_); \
      GLDS_I(8192, p.Wu, v0_, bb_); GLDS_I(9216, p.Wu, v1_, bb_); } while (0)
#define FFN_WD(si_) ((si_) < 0 ? 0 : (si_) < 6 ? 3 : (si_) == 6 ? 2 : 0)
    WAIT_VM0();
    int wq = w;
    const unsigned lw2 = ldsb + 2 * w * 1024;
    const unsigned lwd = ldsb + 65536 + w * 4096;
    const unsigned wdw = (unsigned)(w * 4 * 5120);
    const int c0 = f % 27;
    unsigned m0keep;
    asm volatile("s_mov_b32 %0, m0" : "=s"(m0keep) :: "memory");
    FFN_SLAB(c0, 0, 0); FFN_SLAB(c0, 1, 1); FFN_SLAB(c0, 2, 2);
#pragma unroll 1
    for (int ci = 0; ci < 27; ++ci) {
      const int c = c0 + ci < 27 ? c0 + ci : c0 + ci - 27;
      const int cn = c + 1 < 27 ? c + 1 : 0;
      const int slotb = (ci * 10) & 3;
      const unsigned wd0 = wo2[0] + wdw + (unsigned)(c * 64 * 1280), wd1 = wo2[1] + wdw + (unsigned)(c * 64 * 1280);
      f32x4 ag[4], au[4];
#pragma unroll
      for (int si = 0; si < 10; ++si) {
        asm volatile("s_waitcnt vmcnt(%0)" ::"n"(FFN_WD(si - 3) + 4 + FFN_WD(si - 2) + 4 + FFN_WD(si - 1)) : "memory");
        LDS_BARRIER();
        if (si + 3 < 10) FFN_SLAB(c, si + 3, (slotb + si + 3) & 3);
        else FFN_SLAB(cn, si + 3 - 10, (slotb + si + 3) & 3);
#define FFN_WDP(j_) do { if ((j_) < 20 && (j_) < 3 * si + FFN_WD(si)) { \
          const unsigned vo_ = ((((j_) >> 1) & 1) ? wd1 : wd0) + (unsigned)(((j_) & 3) * 5120 + ((j_) >> 2) * 256); \
          GLDS_I((((j_) >> 2) * 16384 + ((j_) & 3) * 1024), p.Wd, vo_, lwd); } } while (0)
        FFN_WDP(3 * si); FFN_WDP(3 * si + 1); FFN_WDP(3 * si + 2);
        const unsigned sof = ((slotb + si) & 3) * 16384;
#pragma unroll
        for (int k2 = 0; k2 < 2; ++k2) {
          const int ks = si * 2 + k2;
          bf16x8 bg[4], bu[4];
#pragma unroll
          for (int ft = 0; ft < 4; ++ft) {
            const unsigned t0 = tb[ft] + sof + k2 * 4096;
            bg[ft] = trfrag(t0, t0 + 512);
            bu[ft] = trfrag(t0 + 8192, t0 + 8192 + 512);
          }
#pragma unroll
          for (int ft = 0; ft < 4; ++ft) {
            ag[ft] = mf16(A[ks], bg[ft], ks == 0 ? Z4 : ag[ft]);
            au[ft] = mf16(A[ks], bu[ft], ks == 0 ? Z4 : au[ft]);
          }
        }
      }
#pragma unroll
      for (int ft = 0; ft < 4; ++ft)
#pragma unroll
        for (int j = 0; j < 4; ++j) {
          float gv = rbf(ag[ft][j]), uv = rbf(au[ft][j]);
          hb[(16 * w + 4 * g + j) * 72 + 16 * ft + r16] = f2bf(rbf(gelu_tanh(gv)) * uv);
        }
      asm volatile("s_waitcnt vmcnt(12)" ::: "memory");
      LDS_BARRIER();
#pragma unroll
      for (int k2 = 0; k2 < 2; ++k2) {
        bf16x8 ha[4];
#pragma unroll
        for (int m = 0; m < 4; ++m) ha[m] = *(const bf16x8*)(hb + (16 * m + r16) * 72 + 32 * k2 + 8 * g);
#pragma unroll
        for (int n = 0; n < 10; ++n) {
          const int nc = 160 * wq + 16 * n;
          const unsigned ta = tw + (nc >> 7) * 16384 + ((((nc & 127) >> 3) ^ s1) << 4) + k2 * 8192;
          bf16x8 bd = trfrag(ta, ta + 1024);
#pragma unroll
          for (int m = 0; m < 4; ++m) acc2[m][n] = mf16(ha[m], bd, acc2[m][n]);
        }
      }
      LDS_BARRIER();
    }
    asm volatile("s_mov_b32 m0, %0" :: "s"(m0keep) : "memory");
    WAIT_VM0();
    LDS_BARRIER();
    {
      u16* ob = (u16*)smem;
#pragma unroll
      for (int m = 0; m < 4; ++m)
#pragma unroll
        for (int n = 0; n < 10; ++n)
#pragma unroll
          for (int j = 0; j < 4; ++j) ob[(16 * m + 4 * g + j) * XS + 160 * w + 16 * n + r16] = f2bf(acc2[m][n][j]);
      LDS_BARRIER();
      for (int pc = tid; pc < 64 * 80; pc += 256) {
        int row = pc / 80, cc = pc % 80;
        u32x4 o = *(const u32x4*)(ob + row * XS + cc * 8);
        st128(o, rs, (int)OFF_BO + (64 * f + row) * 1280 + cc * 16);
      }
      WAIT_VM0();
      LDS_BARRIER();
      unsigned* smx = (unsigned*)(smem + 90112);
      if (tid == 0) {
        st32(0u, rs, (CW_FF + 16 * f) * 4);
        *smx = atomicAnd(ctl + CW_MIX + 16 * f, 0x7FFFFFFFu);
      }
      LDS_BARRIER();
      const unsigned done = ~(*smx) & 0xFFFFFu;
      if (done) {
        const int row = tid >> 3, c8 = tid & 7;
        float omg[2];
#pragma unroll
        for (int hs = 0; hs < 2; ++hs) omg[hs] = rbf(1.0f - gate8(p.x + (size_t)(64 * f + 32 * hs + row) * 640, p.gw, c8));
        for (int k = 0; k < 20; ++k) {
          if (!((done >> k) & 1)) continue;
          const int hs = k / NCH, cj = k % NCH;
          const int tr = 32 * hs + row, cc = cj * 64 + c8 * 8;
          u32x4 ga = ld128(rs, (int)OFF_GA + ((64 * f + tr) * 640 + cc) * 2);
          u32x4 bv = *(const u32x4*)(ob + tr * XS + cc);
          const float om = hs ? omg[1] : omg[0];
          u32x4 o;
#pragma unroll
          for (int e = 0; e < 4; ++e)
            o[e] = pk2(blo(ga[e]) + rbf(om * blo(bv[e])), bhi(ga[e]) + rbf(om * bhi(bv[e])));
          *(u32x4*)(p.out + (size_t)(64 * f + tr) * 640 + cc) = o;
        }
      }
    }
  }

  if (b >= 104 && !(b >= 136 && b < 168)) {
    int tid = threadIdx.x;
    asm volatile("" : "+v"(tid));
    const int lane = tid & 63, r16 = lane & 15, g = lane >> 4;
    float* sg = (float*)smem;
    int* stask = (int*)(smem + 256);
    for (;;) {
      LDS_BARRIER();
      if (tid == 0) *stask = (int)atomicAdd(ctl + CW_QUEUE, 1u) + 1;
      LDS_BARRIER();
      const int task = *stask;
      if (task >= NTASK) break;
      const int t = task / NCH, cj = task % NCH;
      STAMP(0);
      {
        float gv = gate8(p.x + (size_t)(t * 32 + (tid >> 3)) * 640, p.gw, tid & 7);
        if ((tid & 7) == 0) sg[tid >> 3] = gv;
      }
      if (w == 0) wait_flag<OUT_SLP>(rs, CW_UXF + 16 * (lane & 31), (unsigned)t, ctl, lane);
      LDS_BARRIER();
      STAMP(1);
      const int col = cj * 64 + w * 16 + r16;
      const int uxo = (int)OFF_UX + (t * 80 + (col >> 3)) * 32 * 512 + (col & 7) * 64 + 16 * g;
      const int umo = (int)OFF_UM + t * STEPB + (col >> 3) * 4096 + (col & 7) * 16;
      u32x4 bx[32];
#pragma unroll
      for (int h = 0; h < 32; ++h) bx[h] = ld128(rs, uxo + h * 512);
      if (w == 0) wait_flag<OUT_SLP>(rs, CW_PXF + 16 * (lane & 31), (unsigned)t, ctl, lane);
      LDS_BARRIER();
      u8* pxl = smem + 1024;
      {
        const u8* pxg = ws + OFF_PX + (size_t)t * 32 * 2560;
#pragma unroll
        for (int q = 0; q < 20; ++q)
          __builtin_amdgcn_global_load_lds((const unsigned*)(pxg + (20 * w + q) * 1024 + lane * 16), (unsigned*)(pxl + (20 * w + q) * 1024), 16, 0, AUX_SC1);
      }
      u32x4 um[8];
      {
        unsigned spins = 0;
        for (;;) {
          asm volatile("" ::: "memory");
          unsigned mxv = 0;
#pragma unroll
          for (int hq = 0; hq < 8; ++hq) um[hq] = ld128(rs, umo + (4 * hq + g) * 128);
#pragma unroll
          for (int hq = 0; hq < 8; ++hq) mxv = umax3(mxv, um[hq].x, um[hq].z);
          if (__all(mxv != 0xFFFFFFFFu) || spins > SPIN_CAP) break;
          __builtin_amdgcn_s_sleep(4);
          ++spins;
        }
      }
      WAIT_VM0();
      LDS_BARRIER();
      f32x4 acc0 = Z4, acc1 = Z4;
#pragma unroll
      for (int h = 0; h < 32; ++h) {
        bf16x8 a0 = *(const bf16x8*)(pxl + h * 2560 + 512 + g * 512 + r16 * 16);
        bf16x8 a1 = *(const bf16x8*)(pxl + h * 2560 + 512 + g * 512 + 256 + r16 * 16);
        acc0 = mf16(a0, __builtin_bit_cast(bf16x8, bx[h]), acc0);
        acc1 = mf16(a1, __builtin_bit_cast(bf16x8, bx[h]), acc1);
      }
#pragma unroll
      for (int hq = 0; hq < 8; ++hq) {
        bf16x8 a0 = *(const bf16x8*)(pxl + (4 * hq + g) * 2560 + r16 * 16);
        bf16x8 a1 = *(const bf16x8*)(pxl + (4 * hq + g) * 2560 + 256 + r16 * 16);
        acc0 = mf16(a0, __builtin_bit_cast(bf16x8, um[hq]), acc0);
        acc1 = mf16(a1, __builtin_bit_cast(bf16x8, um[hq]), acc1);
      }
      STAMP(2);
      u16* rt = (u16*)(smem + 512 + 83968);
#pragma unroll
      for (int i = 0; i < 2; ++i)
#pragma unroll
        for (int q = 0; q < 4; ++q) {
          int tok = 16 * i + 4 * g + q;
          rt[tok * 72 + w * 16 + r16] = f2bf(sg[tok] * rbf(i == 0 ? acc0[q] : acc1[q]));
        }
      if (tid == 0) stask[1] = (int)ld32(rs, (CW_FF + 16 * (t >> 1)) * 4);
      LDS_BARRIER();
      {
        const int row = tid >> 3, c8 = tid & 7;
        const int go = ((t * 32 + row) * 640 + cj * 64 + c8 * 8) * 2;
        u32x4 ga = *(const u32x4*)(rt + row * 72 + c8 * 8);
        bool mix = stask[1] >= 0;
        if (!mix) {
          st128(ga, rs, (int)OFF_GA + go);
          WAIT_VM0();
          LDS_BARRIER();
          if (tid == 0) stask[2] = (int)atomicAnd(ctl + CW_MIX + 16 * (t >> 1), ~(1u << ((t & 1) * NCH + cj)));
          LDS_BARRIER();
          mix = stask[2] >= 0;
        }
        if (mix) {
          u32x4 bv = ld128(rs, (int)OFF_BO + go);
          const float om = rbf(1.0f - sg[row]);
          u32x4 o;
#pragma unroll
          for (int e = 0; e < 4; ++e)
            o[e] = pk2(blo(ga[e]) + rbf(om * blo(bv[e])), bhi(ga[e]) + rbf(om * bhi(bv[e])));
          *(u32x4*)((u8*)p.out + go) = o;
        }
      }
    }
  }
}

extern "C" void kernel_launch(void* const* d_in, const int* in_sizes, int n_in, void* d_out, int out_size, void* d_ws,
                              size_t ws_size, hipStream_t stream) {
  u8* ws = (u8*)d_ws;
  Params p;
  p.x = (const u16*)d_in[0]; p.mem0 = (const u16*)d_in[1]; p.Wq = (const u16*)d_in[2]; p.Wk = (const u16*)d_in[3];
  p.Wv = (const u16*)d_in[4]; p.Wo = (const u16*)d_in[5];
  p.Wg = (const u16*)d_in[8]; p.Wu = (const u16*)d_in[9]; p.Wd = (const u16*)d_in[10]; p.gw = (const u16*)d_in[11];
  p.ws = ws; p.out = (u16*)d_out;

  (void)hipFuncSetAttribute((const void*)k_main, hipFuncAttributeMaxDynamicSharedMemorySize, 163840);
  (void)hipMemsetAsync(ws, 0xFF, INIT_BYTES, stream);
  k_main<<<256, 256, 163840, stream>>>(p);
}
```
